```python
import torch

def _softmax(x, axis=-1):
    return torch.softmax(x, dim=axis)

def _gelu(x):
    return torch.nn.functional.gelu(x, approximate='tanh')

def _silu(x):
    return torch.nn.functional.silu(x)

def _sigmoid(x):
    return torch.sigmoid(x)

def _layernorm(x, axis=-1, eps=1e-05):
    m = x.mean(dim=axis, keepdim=True)
    v = x.var(dim=axis, keepdim=True, unbiased=False)
    return (x - m) / torch.sqrt(v + eps)

def _scatter_add(tgt, idx, src, axis=0):
    return tgt.clone().index_add_(axis, idx, src)

def _segment_sum(src, seg, n):
    out = torch.zeros((n,) + src.shape[1:], dtype=src.dtype)
    return out.index_add_(0, seg, src)

def _pad(x, pw):
    flat = tuple((v for pair in reversed(pw) for v in pair))
    return torch.nn.functional.pad(x, flat)
kernel_name = 'adaLN_cond__compressed_kv_cla__mixer_token_mlp__prompt'

def setup_inputs():

    _w = lambda a: (lambda t: t.to(torch.bfloat16) if t.is_floating_point() else t)(torch.as_tensor(a))

    def _A_setup():
        B, H, Sq, Sk, D, M = (1, 32, 2048, 2048, 128, 576)
        Dc = 4 * D
        return {'x': _w((lambda *s: torch.randn(*s, dtype=torch.bfloat16) * 0.05)(B, Sq, M)), 'c_kv': _w((lambda *s: torch.randn(*s, dtype=torch.bfloat16) * 0.8)(B, Sk, Dc)), 'w_q': _w((lambda *s: torch.randn(*s, dtype=torch.bfloat16) * 1.6)(M, H, D) * M ** (-0.5)), 'w_uk': _w((lambda *s: torch.randn(*s, dtype=torch.bfloat16) * 1.6)(Dc, H, D) * Dc ** (-0.5)), 'w_uv': _w((lambda *s: torch.randn(*s, dtype=torch.bfloat16) * 0.8)(Dc, H, D) * Dc ** (-0.5)), 'w_o': _w((lambda *s: torch.randn(*s, dtype=torch.bfloat16) * 0.8)(H, D, M) * (H * D) ** (-0.5))}
    _A_in = _A_setup()

    def _B_setup():
        B, S, D, F = (1, 2048, 576, 4608)
        return {'x': _w((lambda *s: torch.randn(*s, dtype=torch.bfloat16) * 0.05)(B, S, D)), 'w1': _w((lambda *s: torch.randn(*s, dtype=torch.bfloat16) * 0.8)(S, F) * S ** (-0.5)), 'b1': _w((lambda *s: torch.randn(*s, dtype=torch.bfloat16) * 12.8)(F) * 0.02), 'w2': _w((lambda *s: torch.randn(*s, dtype=torch.bfloat16) * 0.4)(F, S) * F ** (-0.5)), 'b2': _w((lambda *s: torch.randn(*s, dtype=torch.bfloat16) * 3.2)(S) * 0.02)}
    _B_in = _B_setup()
    return {'A_x': _A_in['x'], 'A_c_kv': _A_in['c_kv'], 'A_w_q': _A_in['w_q'], 'A_w_uk': _A_in['w_uk'], 'A_w_uv': _A_in['w_uv'], 'A_w_o': _A_in['w_o'], 'B_x': _B_in['x'], 'B_w1': _B_in['w1'], 'B_b1': _B_in['b1'], 'B_w2': _B_in['w2'], 'B_b2': _B_in['b2'], 'c': _w((lambda *s: torch.randn(*s, dtype=torch.bfloat16) * 0.1)(1, 576) * 8.0), 'w_mod': _w((lambda *s: torch.randn(*s, dtype=torch.bfloat16) * 0.1)(576, 6 * 576) * 576 ** (-0.5) * 8.0)}

def reference(A_x, A_c_kv, A_w_q, A_w_uk, A_w_uv, A_w_o, B_x, B_w1, B_b1, B_w2, B_b2, c, w_mod):

    def _A_ref(x, c_kv, w_q, w_uk, w_uv, w_o):
        B, Sq, M = (x.shape[0], x.shape[1], x.shape[2])
        Sk = c_kv.shape[1]
        H, D = (w_q.shape[1], w_q.shape[2])
        q = torch.einsum('bqm,mhd->bhqd', x, w_q)
        k = torch.einsum('bkc,chd->bhkd', c_kv, w_uk)
        v = torch.einsum('bkc,chd->bhkd', c_kv, w_uv)
        scores = torch.einsum('bhqd,bhkd->bhqk', q, k) * D ** (-0.5)
        off = Sk - Sq
        qi = torch.arange(Sq)[:, None]
        kj = torch.arange(Sk)[None, :]
        cmask = kj <= qi + off
        scores = torch.where(cmask[None, None], scores, -1000000000.0)
        p = _softmax(scores, axis=-1)
        ctx = torch.einsum('bhqk,bhkd->bhqd', p, v)
        return torch.einsum('bhqd,hdm->bqm', ctx, w_o)

    def _B_ref(x, w1, b1, w2, b2):
        B, S, D = x.shape
        h = _layernorm(x, axis=-1)
        u = torch.einsum('bsd,sf->bdf', h, w1) + b1
        u = _gelu(u)
        y = torch.einsum('bdf,fs->bds', u, w2) + b2
        return (lambda x, *d: x.permute(*d))(y, 0, 2, 1)
    x = A_x
    mod = (_silu(c) @ w_mod).reshape(c.shape[0], 6, 1, -1)
    sa, ca, ga, sb, cb, gb = (mod[:, 0].clone(), mod[:, 1].clone(), mod[:, 2].clone(), mod[:, 3].clone(), mod[:, 4].clone(), mod[:, 5].clone())
    h = _layernorm(x, axis=-1) * (1 + ca) + sa
    x = x + ga * _A_ref(h, A_c_kv, A_w_q, A_w_uk, A_w_uv, A_w_o)
    h = _layernorm(x, axis=-1) * (1 + cb) + sb
    x = x + gb * _B_ref(h, B_w1, B_b1, B_w2, B_b2)
    return x
```

```cpp
#include <hip/hip_runtime.h>
#include <hip/hip_bf16.h>
#include <stdint.h>
typedef unsigned short u16;
typedef short bf16x8 __attribute__((ext_vector_type(8)));
typedef short s16x4 __attribute__((ext_vector_type(4)));
typedef float f32x16 __attribute__((ext_vector_type(16)));
typedef float f32x4 __attribute__((ext_vector_type(4)));
typedef unsigned u32x4 __attribute__((ext_vector_type(4)));
typedef unsigned u32x2 __attribute__((ext_vector_type(2)));
#define LAS __attribute__((address_space(3)))
#define DI __device__ __forceinline__
constexpr int S = 2048, M = 576, H = 32, D = 128, HD = 4096, DC = 512, F = 4608;

DI float bf2f(u16 x) { return __uint_as_float(((unsigned)x) << 16); }
DI unsigned cvtpk(float lo, float hi) { unsigned r; asm("v_cvt_pk_bf16_f32 %0, %1, %2" : "=v"(r) : "v"(lo), "v"(hi)); return r; }
DI u16 f2bf(float x) { return (u16)(cvtpk(x, x) & 0xffff); }
DI float rbf(float x) { return bf2f(f2bf(x)); }
DI float wsum(float v) { for (int o = 32; o >= 1; o >>= 1) v += __shfl_xor(v, o); return v; }

__global__ __launch_bounds__(256) void k_mod(const u16* c, const u16* w, float* mod) {
  __shared__ float red[32][64];
  const int t = threadIdx.x, cg = t & 7, rg = t >> 3;
  const int n0 = blockIdx.x * 64 + cg * 8;
  float acc[8] = {0, 0, 0, 0, 0, 0, 0, 0};
  for (int k = rg; k < M; k += 32) {
    float cv = bf2f(c[k]); float s = rbf(cv / (1.f + __expf(-cv)));
    u32x4 v = *(const u32x4*)(w + (size_t)k * 3456 + n0);
    for (int j = 0; j < 4; ++j) { acc[2 * j] += s * __uint_as_float(v[j] << 16); acc[2 * j + 1] += s * __uint_as_float(v[j] & 0xffff0000u); }
  }
  for (int j = 0; j < 8; ++j) red[rg][cg * 8 + j] = acc[j];
  __syncthreads();
  if (t < 64) { float s = 0; for (int r = 0; r < 32; ++r) s += red[r][t]; mod[blockIdx.x * 64 + t] = rbf(s); }
}
__global__ __launch_bounds__(256) void k_ln1(const u16* x, const float* mod, u16* h) {
  const int row = blockIdx.x * 4 + (threadIdx.x >> 6), l = threadIdx.x & 63;
  float v[9]; float s = 0;
  for (int i = 0; i < 9; ++i) { v[i] = bf2f(x[(size_t)row * M + l + 64 * i]); s += v[i]; }
  const float mean = wsum(s) * (1.f / M); float q = 0;
  for (int i = 0; i < 9; ++i) { float d = v[i] - mean; q += d * d; }
  const float r = 1.0f / sqrtf(wsum(q) * (1.f / M) + 1e-5f);
  for (int i = 0; i < 9; ++i) { int c = l + 64 * i; h[(size_t)row * M + c] = f2bf((v[i] - mean) * r * (1.f + mod[M + c]) + mod[c]); }
}
__global__ __launch_bounds__(256) void k_ln2(const u16* x1, const float* mod, u16* h3T) {
  const int row = blockIdx.x * 4 + (threadIdx.x >> 6), l = threadIdx.x & 63;
  float v[9]; float s = 0;
  for (int i = 0; i < 9; ++i) { v[i] = bf2f(x1[(size_t)row * M + l + 64 * i]); s += v[i]; }
  float mean = wsum(s) * (1.f / M); float q = 0;
  for (int i = 0; i < 9; ++i) { float d = v[i] - mean; q += d * d; }
  float r = 1.0f / sqrtf(wsum(q) * (1.f / M) + 1e-5f);
  s = 0;
  for (int i = 0; i < 9; ++i) { int c = l + 64 * i; v[i] = (v[i] - mean) * r * (1.f + mod[4 * M + c]) + mod[3 * M + c]; s += v[i]; }
  mean = wsum(s) * (1.f / M); q = 0;
  for (int i = 0; i < 9; ++i) { float d = v[i] - mean; q += d * d; }
  r = 1.0f / sqrtf(wsum(q) * (1.f / M) + 1e-5f);
  for (int i = 0; i < 9; ++i) { int c = l + 64 * i; h3T[(size_t)c * S + row] = f2bf((v[i] - mean) * r); }
}

struct EpiArgs { const u16* p0; const u16* p1; const float* mod; u16* out; int ldo; float scale; };
template <int BM, int BN, int EPI>
__global__ __launch_bounds__(256) void k_gemm(const u16* A, int lda, const u16* B, int ldb, int K, int nbn, EpiArgs e) {
  extern __shared__ __attribute__((aligned(16))) char lds[];
  constexpr int AB = BM * 128, BB = BN * 128, SB = AB + BB;
  constexpr int WM = BM / 2, WN = BN / 2, TM = WM / 32, TN = WN / 32;
  const int tid = threadIdx.x, lane = tid & 63, wid = __builtin_amdgcn_readfirstlane(tid >> 6), wm = wid >> 1, wn = wid & 1;
  const int bm = blockIdx.x / nbn, bn = blockIdx.x % nbn;
  const u16* Ab = A + (size_t)bm * BM * lda; const u16* Bb = B + (size_t)bn * BN;
  f32x16 acc[TM][TN];
  for (int i = 0; i < TM; ++i) for (int j = 0; j < TN; ++j) acc[i][j] = f32x16{};
  auto stage = [&](int buf, int kt) {
    char* base = lds + buf * SB;
    for (int p = wid; p < BM / 8; p += 4) {
      int r = p * 8 + (lane >> 3), ch = (lane & 7) ^ ((r >> 1) & 7);
      __builtin_amdgcn_global_load_lds((const unsigned*)(Ab + (size_t)r * lda + kt * 64 + ch * 8), (LAS unsigned*)(base + p * 1024), 16, 0, 0);
    }
    for (int p = wid; p < BN / 8; p += 4) {
      int kg = p / (BN / 64), n64 = p % (BN / 64);
      int k = kg * 8 + ((lane >> 2) & 7), n = n64 * 64 + (lane >> 5) * 32 + (lane & 3) * 8;
      __builtin_amdgcn_global_load_lds((const unsigned*)(Bb + (size_t)(kt * 64 + k) * ldb + n), (LAS unsigned*)(base + AB + p * 1024), 16, 0, 0);
    }
  };
  const int nt = K / 64;
  stage(0, 0);
  asm volatile("s_waitcnt vmcnt(0)" ::: "memory"); __builtin_amdgcn_s_barrier();
  const int r32 = lane & 31, hi = lane >> 5;
  for (int t = 0; t < nt; ++t) {
    if (t + 1 < nt) stage((t + 1) & 1, t + 1);
    LAS const char* base = (LAS const char*)(lds) + (t & 1) * SB;
    for (int s = 0; s < 4; ++s) {
      bf16x8 af[TM], bfr[TN];
      for (int i = 0; i < TM; ++i) { int r = wm * WM + i * 32 + r32; af[i] = *(LAS const bf16x8*)(base + r * 128 + (((2 * s + hi) ^ ((r >> 1) & 7)) << 4)); }
      for (int j = 0; j < TN; ++j) {
        int n0 = wn * WN + j * 32;
        int kg = 2 * s + hi;
        LAS const char* sub = base + AB + (kg * (BN / 32) + n0 / 32) * 512 + ((lane >> 2) & 3) * 64 + ((lane >> 4) & 1) * 32 + (lane & 3) * 8;
        s16x4 lo = __builtin_amdgcn_ds_read_tr16_b64_v4i16((LAS s16x4*)sub);
        s16x4 hi4 = __builtin_amdgcn_ds_read_tr16_b64_v4i16((LAS s16x4*)(sub + 256));
        bfr[j] = __builtin_shufflevector(lo, hi4, 0, 1, 2, 3, 4, 5, 6, 7);
      }
      for (int i = 0; i < TM; ++i) for (int j = 0; j < TN; ++j) acc[i][j] = EPI == 4 ?
          __builtin_amdgcn_mfma_f32_32x32x16_bf16(af[i], bfr[j], acc[i][j], 0, 0, 0) : __builtin_amdgcn_mfma_f32_32x32x16_bf16(bfr[j], af[i], acc[i][j], 0, 0, 0);
    }
    asm volatile("s_waitcnt vmcnt(0) lgkmcnt(0)" ::: "memory"); __builtin_amdgcn_s_barrier();
  }
  for (int i = 0; i < TM; ++i) for (int j = 0; j < TN; ++j) for (int g = 0; g < 4; ++g) {
    float v[4]; for (int q = 0; q < 4; ++q) v[q] = acc[i][j][4 * g + q];
    if (EPI != 4) {
      const int m = bm * BM + wm * WM + i * 32 + r32, n = bn * BN + wn * WN + j * 32 + 8 * g + 4 * hi;
      if (EPI == 0) { for (int q = 0; q < 4; ++q) v[q] *= e.scale; }
      if (EPI == 2) { for (int q = 0; q < 4; ++q) v[q] = bf2f(e.p0[(size_t)m * M + n + q]) + e.mod[2 * M + n + q] * v[q]; }
      if (EPI == 3) { for (int q = 0; q < 4; ++q) { float u = v[q] + bf2f(e.p0[n + q]); float z = 0.7978845608f * (u + 0.044715f * u * u * u); v[q] = u / (1.f + __expf(-2.f * z)); } }
      u32x2 w = {cvtpk(v[0], v[1]), cvtpk(v[2], v[3])};
      *(u32x2*)(e.out + (size_t)m * e.ldo + n) = w;
    } else {
      const int m = bm * BM + wm * WM + i * 32 + 8 * g + 4 * hi, n = bn * BN + wn * WN + j * 32 + r32;
      const float b2 = bf2f(e.p1[n]);
      for (int q = 0; q < 4; ++q) v[q] = bf2f(e.p0[(size_t)n * M + m + q]) + e.mod[5 * M + m + q] * (v[q] + b2);
      u32x2 w = {cvtpk(v[0], v[1]), cvtpk(v[2], v[3])};
      *(u32x2*)(e.out + (size_t)n * M + m) = w;
    }
  }
}

__global__ __launch_bounds__(256) void k_attn(const u16* Q, const u16* Kg, const u16* Vg, u16* O) {
  extern __shared__ __attribute__((aligned(16))) char lds[];
  const int tid = threadIdx.x, lane = tid & 63, wid = __builtin_amdgcn_readfirstlane(tid >> 6), r32 = lane & 31, hi = lane >> 5;
  const int h = (blockIdx.x & 7) + 8 * (blockIdx.x >> 6), x = (blockIdx.x >> 3) & 7;
  for (int pass = 0; pass < 2; ++pass) {
    const int qb = pass == 0 ? 15 - x : x, q0 = qb * 128 + wid * 32, nt = 2 * qb + 2;
    bf16x8 qf[8];
    for (int d0 = 0; d0 < 8; ++d0) qf[d0] = *(const bf16x8*)(Q + (size_t)(q0 + r32) * HD + h * D + d0 * 16 + hi * 8);
    f32x16 o[4]; for (int i = 0; i < 4; ++i) o[i] = f32x16{};
    float m = -1e30f, l = 0.f;
    auto stage = [&](int buf, int t) {
      char* base = lds + buf * 32768;
      for (int p = wid; p < 16; p += 4) {
        int r = p * 4 + (lane >> 4), ch = (lane & 15) ^ (r & 7);
        __builtin_amdgcn_global_load_lds((const unsigned*)(Kg + (size_t)(t * 64 + r) * HD + h * D + ch * 8), (LAS unsigned*)(base + p * 1024), 16, 0, 0);
      }
      for (int p = wid; p < 16; p += 4) {
        int kg = p >> 1, n64 = p & 1; int kk = kg * 8 + ((lane >> 2) & 7);
        int k = (kk & ~0xC) | ((kk & 4) << 1) | ((kk & 8) >> 1);
        __builtin_amdgcn_global_load_lds((const unsigned*)(Vg + (size_t)(t * 64 + k) * HD + h * D + n64 * 64 + hi * 32 + (lane & 3) * 8), (LAS unsigned*)(base + 16384 + p * 1024), 16, 0, 0);
      }
    };
    __builtin_amdgcn_s_barrier();
    stage(0, 0);
    asm volatile("s_waitcnt vmcnt(0)" ::: "memory"); __builtin_amdgcn_s_barrier();
    for (int t = 0; t < nt; ++t) {
      if (t + 1 < nt) stage((t + 1) & 1, t + 1);
      LAS const char* base = (LAS const char*)(lds) + (t & 1) * 32768;
      f32x16 p[2] = {f32x16{}, f32x16{}};
      for (int d0 = 0; d0 < 8; ++d0) for (int kb = 0; kb < 2; ++kb) {
        int r = kb * 32 + r32;
        bf16x8 kf = *(LAS const bf16x8*)(base + r * 256 + (((2 * d0 + hi) ^ (r & 7)) << 4));
        p[kb] = __builtin_amdgcn_mfma_f32_32x32x16_bf16(kf, qf[d0], p[kb], 0, 0, 0);
      }
      if (t * 64 + 63 > q0) for (int kb = 0; kb < 2; ++kb) for (int r = 0; r < 16; ++r) { int key = t * 64 + kb * 32 + (r & 3) + 8 * (r >> 2) + 4 * hi; if (key > q0 + r32) p[kb][r] = -INFINITY; }
      float mx = p[0][0]; for (int kb = 0; kb < 2; ++kb) for (int r = 0; r < 16; ++r) mx = fmaxf(mx, p[kb][r]);
      mx = fmaxf(mx, __shfl_xor(mx, 32));
      const float mn = fmaxf(m, mx), al = __builtin_amdgcn_exp2f(m - mn); m = mn;
      float ps = 0; for (int kb = 0; kb < 2; ++kb) for (int r = 0; r < 16; ++r) { p[kb][r] = __builtin_amdgcn_exp2f(p[kb][r] - mn); ps += p[kb][r]; }
      ps += __shfl_xor(ps, 32); l = l * al + ps;
      for (int i = 0; i < 4; ++i) o[i] *= al;
      for (int ks = 0; ks < 4; ++ks) {
        u32x4 w; for (int j = 0; j < 4; ++j) w[j] = cvtpk(p[ks >> 1][8 * (ks & 1) + 2 * j], p[ks >> 1][8 * (ks & 1) + 2 * j + 1]);
        bf16x8 pf = __builtin_bit_cast(bf16x8, w);
        for (int db = 0; db < 4; ++db) {
          LAS const char* sub = base + 16384 + ((2 * ks + hi) * 4 + db) * 512 + ((lane >> 2) & 3) * 64 + ((lane >> 4) & 1) * 32 + (lane & 3) * 8;
          s16x4 lo = __builtin_amdgcn_ds_read_tr16_b64_v4i16((LAS s16x4*)sub);
          s16x4 hi4 = __builtin_amdgcn_ds_read_tr16_b64_v4i16((LAS s16x4*)(sub + 256));
          bf16x8 vf = __builtin_shufflevector(lo, hi4, 0, 1, 2, 3, 4, 5, 6, 7);
          o[db] = __builtin_amdgcn_mfma_f32_32x32x16_bf16(vf, pf, o[db], 0, 0, 0);
        }
      }
      asm volatile("s_waitcnt vmcnt(0) lgkmcnt(0)" ::: "memory"); __builtin_amdgcn_s_barrier();
    }
    const float il = 1.f / l;
    for (int db = 0; db < 4; ++db) for (int g = 0; g < 4; ++g) {
      u32x2 w = {cvtpk(o[db][4 * g] * il, o[db][4 * g + 1] * il), cvtpk(o[db][4 * g + 2] * il, o[db][4 * g + 3] * il)};
      *(u32x2*)(O + (size_t)(q0 + r32) * HD + h * D + db * 32 + 8 * g + 4 * hi) = w;
    }
  }
}

extern "C" void kernel_launch(void* const* d_in, const int* in_sizes, int n_in, void* d_out, int out_size, void* d_ws, size_t ws_size, hipStream_t stream) {
  const u16* x = (const u16*)d_in[0]; const u16* ckv = (const u16*)d_in[1]; const u16* wq = (const u16*)d_in[2]; const u16* wuk = (const u16*)d_in[3];
  const u16* wuv = (const u16*)d_in[4]; const u16* wo = (const u16*)d_in[5]; const u16* w1 = (const u16*)d_in[7]; const u16* b1 = (const u16*)d_in[8];
  const u16* w2 = (const u16*)d_in[9]; const u16* b2 = (const u16*)d_in[10]; const u16* c = (const u16*)d_in[11]; const u16* wmod = (const u16*)d_in[12];
  char* ws = (char*)d_ws;
  float* mod = (float*)ws; u16* h = (u16*)(ws + (1 << 20)); u16* q = (u16*)(ws + (4 << 20)); u16* k = (u16*)(ws + (24 << 20)); u16* v = (u16*)(ws + (44 << 20));
  u16* ctx = (u16*)(ws + (64 << 20)); u16* x1 = (u16*)(ws + (84 << 20)); u16* h3T = (u16*)(ws + (88 << 20)); u16* u = (u16*)(ws + (92 << 20));
  hipFuncSetAttribute((const void*)k_gemm<128, 128, 0>, hipFuncAttributeMaxDynamicSharedMemorySize, 65536);
  hipFuncSetAttribute((const void*)k_attn, hipFuncAttributeMaxDynamicSharedMemorySize, 65536);
  k_mod<<<54, 256, 0, stream>>>(c, wmod, mod);
  k_ln1<<<512, 256, 0, stream>>>(x, mod, h);
  EpiArgs e{nullptr, nullptr, mod, q, HD, 0.12751743082f};
  k_gemm<128, 128, 0><<<16 * 32, 256, 65536, stream>>>(h, M, wq, HD, M, 32, e);
  e.out = k; e.scale = 1.f; k_gemm<128, 128, 0><<<16 * 32, 256, 65536, stream>>>(ckv, DC, wuk, HD, DC, 32, e);
  e.out = v; k_gemm<128, 128, 0><<<16 * 32, 256, 65536, stream>>>(ckv, DC, wuv, HD, DC, 32, e);
  k_attn<<<256, 256, 65536, stream>>>(q, k, v, ctx);
  e.out = x1; e.ldo = M; e.p0 = x; k_gemm<128, 64, 2><<<16 * 9, 256, 49152, stream>>>(ctx, HD, wo, M, HD, 9, e);
  k_ln2<<<512, 256, 0, stream>>>(x1, mod, h3T);
  e.out = u; e.ldo = F; e.p0 = b1; k_gemm<64, 128, 3><<<9 * 36, 256, 49152, stream>>>(h3T, S, w1, F, S, 36, e);
  e.out = (u16*)d_out; e.p0 = x1; e.p1 = b2; k_gemm<64, 128, 4><<<9 * 16, 256, 49152, stream>>>(u, F, w2, S, F, 16, e);
}
```

```cpp
#include <hip/hip_runtime.h>
#include <hip/hip_bf16.h>
#include <stdint.h>
typedef unsigned short u16;
typedef short bf16x8 __attribute__((ext_vector_type(8)));
typedef short s16x4 __attribute__((ext_vector_type(4)));
typedef float f32x16 __attribute__((ext_vector_type(16)));
typedef float f32x4 __attribute__((ext_vector_type(4)));
typedef unsigned u32x4 __attribute__((ext_vector_type(4)));
typedef unsigned u32x2 __attribute__((ext_vector_type(2)));
#define LAS __attribute__((address_space(3)))
#define DI __device__ __forceinline__
constexpr int S = 2048, M = 576, H = 32, D = 128, HD = 4096, DC = 512, F = 4608;

DI float bf2f(u16 x) { return __uint_as_float(((unsigned)x) << 16); }
DI unsigned cvtpk(float lo, float hi) { unsigned r; asm("v_cvt_pk_bf16_f32 %0, %1, %2" : "=v"(r) : "v"(lo), "v"(hi)); return r; }
DI u16 f2bf(float x) { return (u16)(cvtpk(x, x) & 0xffff); }
DI float rbf(float x) { return bf2f(f2bf(x)); }
DI float wsum(float v) { for (int o = 32; o >= 1; o >>= 1) v += __shfl_xor(v, o); return v; }

__global__ __launch_bounds__(256) void k_mod(const u16* c, const u16* w, float* mod) {
  __shared__ float red[32][64];
  const int t = threadIdx.x, cg = t & 7, rg = t >> 3;
  const int n0 = blockIdx.x * 64 + cg * 8;
  float acc[8] = {0, 0, 0, 0, 0, 0, 0, 0};
  for (int k = rg; k < M; k += 32) {
    float cv = bf2f(c[k]); float s = rbf(cv / (1.f + __expf(-cv)));
    u32x4 v = *(const u32x4*)(w + (size_t)k * 3456 + n0);
    for (int j = 0; j < 4; ++j) { acc[2 * j] += s * __uint_as_float(v[j] << 16); acc[2 * j + 1] += s * __uint_as_float(v[j] & 0xffff0000u); }
  }
  for (int j = 0; j < 8; ++j) red[rg][cg * 8 + j] = acc[j];
  __syncthreads();
  if (t < 64) { float s = 0; for (int r = 0; r < 32; ++r) s += red[r][t]; mod[blockIdx.x * 64 + t] = rbf(s); }
}
__global__ __launch_bounds__(256) void k_ln1(const u16* x, const float* mod, u16* h) {
  const int row = blockIdx.x * 4 + (threadIdx.x >> 6), l = threadIdx.x & 63;
  float v[9]; float s = 0;
  for (int i = 0; i < 9; ++i) { v[i] = bf2f(x[(size_t)row * M + l + 64 * i]); s += v[i]; }
  const float mean = wsum(s) * (1.f / M); float q = 0;
  for (int i = 0; i < 9; ++i) { float d = v[i] - mean; q += d * d; }
  const float r = 1.0f / sqrtf(wsum(q) * (1.f / M) + 1e-5f);
  for (int i = 0; i < 9; ++i) { int c = l + 64 * i; h[(size_t)row * M + c] = f2bf((v[i] - mean) * r * (1.f + mod[M + c]) + mod[c]); }
}
__global__ __launch_bounds__(256) void k_ln2(const u16* x1, const float* mod, u16* h3T) {
  const int row = blockIdx.x * 4 + (threadIdx.x >> 6), l = threadIdx.x & 63;
  float v[9]; float s = 0;
  for (int i = 0; i < 9; ++i) { v[i] = bf2f(x1[(size_t)row * M + l + 64 * i]); s += v[i]; }
  float mean = wsum(s) * (1.f / M); float q = 0;
  for (int i = 0; i < 9; ++i) { float d = v[i] - mean; q += d * d; }
  float r = 1.0f / sqrtf(wsum(q) * (1.f / M) + 1e-5f);
  s = 0;
  for (int i = 0; i < 9; ++i) { int c = l + 64 * i; v[i] = (v[i] - mean) * r * (1.f + mod[4 * M + c]) + mod[3 * M + c]; s += v[i]; }
  mean = wsum(s) * (1.f / M); q = 0;
  for (int i = 0; i < 9; ++i) { float d = v[i] - mean; q += d * d; }
  r = 1.0f / sqrtf(wsum(q) * (1.f / M) + 1e-5f);
  for (int i = 0; i < 9; ++i) { int c = l + 64 * i; h3T[(size_t)c * S + row] = f2bf((v[i] - mean) * r); }
}

struct EpiArgs { const u16* p0; const u16* p1; const float* mod; u16* out; int ldo; float scale; };
template <int BM, int BN, int EPI>
__global__ __launch_bounds__(256) void k_gemm(const u16* A, int lda, const u16* B, int ldb, int K, int nbn, EpiArgs e) {
  extern __shared__ __attribute__((aligned(16))) char lds[];
  constexpr int AB = BM * 128, BB = BN * 128, SB = AB + BB;
  constexpr int WM = BM / 2, WN = BN / 2, TM = WM / 32, TN = WN / 32;
  const int tid = threadIdx.x, lane = tid & 63, wid = __builtin_amdgcn_readfirstlane(tid >> 6), wm = wid >> 1, wn = wid & 1;
  const int bm = blockIdx.x / nbn, bn = blockIdx.x % nbn;
  const u16* Ab = A + (size_t)bm * BM * lda; const u16* Bb = B + (size_t)bn * BN;
  f32x16 acc[TM][TN];
  for (int i = 0; i < TM; ++i) for (int j = 0; j < TN; ++j) acc[i][j] = f32x16{};
  auto stage = [&](int buf, int kt) {
    char* base = lds + buf * SB;
    for (int p = wid; p < BM / 8; p += 4) {
      int r = p * 8 + (lane >> 3), ch = (lane & 7) ^ ((r >> 1) & 7);
      __builtin_amdgcn_global_load_lds((const unsigned*)(Ab + (size_t)r * lda + kt * 64 + ch * 8), (LAS unsigned*)(base + p * 1024), 16, 0, 0);
    }
    for (int p = wid; p < BN / 8; p += 4) {
      int kg = p / (BN / 64), n64 = p % (BN / 64);
      int k = kg * 8 + ((lane >> 2) & 7), n = n64 * 64 + (lane >> 5) * 32 + (lane & 3) * 8;
      __builtin_amdgcn_global_load_lds((const unsigned*)(Bb + (size_t)(kt * 64 + k) * ldb + n), (LAS unsigned*)(base + AB + p * 1024), 16, 0, 0);
    }
  };
  const int nt = K / 64;
  stage(0, 0);
  asm volatile("s_waitcnt vmcnt(0)" ::: "memory"); __builtin_amdgcn_s_barrier();
  const int r32 = lane & 31, hi = lane >> 5;
  for (int t = 0; t < nt; ++t) {
    if (t + 1 < nt) stage((t + 1) & 1, t + 1);
    LAS const char* base = (LAS const char*)(lds) + (t & 1) * SB;
    for (int s = 0; s < 4; ++s) {
      bf16x8 af[TM], bfr[TN];
      for (int i = 0; i < TM; ++i) { int r = wm * WM + i * 32 + r32; af[i] = *(LAS const bf16x8*)(base + r * 128 + (((2 * s + hi) ^ ((r >> 1) & 7)) << 4)); }
      for (int j = 0; j < TN; ++j) {
        int n0 = wn * WN + j * 32;
        int kg = 2 * s + hi;
        LAS const char* sub = base + AB + (kg * (BN / 32) + n0 / 32) * 512 + ((lane >> 2) & 3) * 64 + ((lane >> 4) & 1) * 32 + (lane & 3) * 8;
        s16x4 lo = __builtin_amdgcn_ds_read_tr16_b64_v4i16((LAS s16x4*)sub);
        s16x4 hi4 = __builtin_amdgcn_ds_read_tr16_b64_v4i16((LAS s16x4*)(sub + 256));
        bfr[j] = __builtin_shufflevector(lo, hi4, 0, 1, 2, 3, 4, 5, 6, 7);
      }
      for (int i = 0; i < TM; ++i) for (int j = 0; j < TN; ++j) acc[i][j] = EPI == 4 ?
          __builtin_amdgcn_mfma_f32_32x32x16_bf16(af[i], bfr[j], acc[i][j], 0, 0, 0) : __builtin_amdgcn_mfma_f32_32x32x16_bf16(bfr[j], af[i], acc[i][j], 0, 0, 0);
    }
    asm volatile("s_waitcnt vmcnt(0) lgkmcnt(0)" ::: "memory"); __builtin_amdgcn_s_barrier();
  }
  for (int i = 0; i < TM; ++i) for (int j = 0; j < TN; ++j) for (int g = 0; g < 4; ++g) {
    float v[4]; for (int q = 0; q < 4; ++q) v[q] = acc[i][j][4 * g + q];
    if (EPI != 4) {
      const int m = bm * BM + wm * WM + i * 32 + r32, n = bn * BN + wn * WN + j * 32 + 8 * g + 4 * hi;
      if (EPI == 0) { for (int q = 0; q < 4; ++q) v[q] *= e.scale; }
      if (EPI == 2) { for (int q = 0; q < 4; ++q) v[q] = bf2f(e.p0[(size_t)m * M + n + q]) + e.mod[2 * M + n + q] * v[q]; }
      if (EPI == 3) { for (int q = 0; q < 4; ++q) { float u = v[q] + bf2f(e.p0[n + q]); float z = 0.7978845608f * (u + 0.044715f * u * u * u); v[q] = u / (1.f + __expf(-2.f * z)); } }
      u32x2 w = {cvtpk(v[0], v[1]), cvtpk(v[2], v[3])};
      *(u32x2*)(e.out + (size_t)m * e.ldo + n) = w;
    } else {
      const int m = bm * BM + wm * WM + i * 32 + 8 * g + 4 * hi, n = bn * BN + wn * WN + j * 32 + r32;
      const float b2 = bf2f(e.p1[n]);
      for (int q = 0; q < 4; ++q) v[q] = bf2f(e.p0[(size_t)n * M + m + q]) + e.mod[5 * M + m + q] * (v[q] + b2);
      u32x2 w = {cvtpk(v[0], v[1]), cvtpk(v[2], v[3])};
      *(u32x2*)(e.out + (size_t)n * M + m) = w;
    }
  }
}

__global__ __launch_bounds__(256) void k_attn(const u16* Q, const u16* Kg, const u16* Vg, u16* O) {
  extern __shared__ __attribute__((aligned(16))) char lds[];
  const int tid = threadIdx.x, lane = tid & 63, wid = __builtin_amdgcn_readfirstlane(tid >> 6), r32 = lane & 31, hi = lane >> 5;
  const int h = (blockIdx.x & 7) + 8 * (blockIdx.x >> 6), x = (blockIdx.x >> 3) & 7;
  for (int pass = 0; pass < 2; ++pass) {
    const int qb = pass == 0 ? 15 - x : x, q0 = qb * 128 + wid * 32, nt = 2 * qb + 2;
    bf16x8 qf[8];
    for (int d0 = 0; d0 < 8; ++d0) qf[d0] = *(const bf16x8*)(Q + (size_t)(q0 + r32) * HD + h * D + d0 * 16 + hi * 8);
    f32x16 o[4]; for (int i = 0; i < 4; ++i) o[i] = f32x16{};
    float m = -1e30f, l = 0.f;
    auto stage = [&](int buf, int t) {
      char* base = lds + buf * 32768;
      for (int p = wid; p < 16; p += 4) {
        int r = p * 4 + (lane >> 4), ch = (lane & 15) ^ (r & 7);
        __builtin_amdgcn_global_load_lds((const unsigned*)(Kg + (size_t)(t * 64 + r) * HD + h * D + ch * 8), (LAS unsigned*)(base + p * 1024), 16, 0, 0);
      }
      for (int p = wid; p < 16; p += 4) {
        int kg = p >> 1, n64 = p & 1; int kk = kg * 8 + ((lane >> 2) & 7);
        int k = (kk & ~0xC) | ((kk & 4) << 1) | ((kk & 8) >> 1);
        __builtin_amdgcn_global_load_lds((const unsigned*)(Vg + (size_t)(t * 64 + k) * HD + h * D + n64 * 64 + hi * 32 + (lane & 3) * 8), (LAS unsigned*)(base + 16384 + p * 1024), 16, 0, 0);
      }
    };
    __builtin_amdgcn_s_barrier();
    stage(0, 0);
    asm volatile("s_waitcnt vmcnt(0)" ::: "memory"); __builtin_amdgcn_s_barrier();
    for (int t = 0; t < nt; ++t) {
      if (t + 1 < nt) stage((t + 1) & 1, t + 1);
      LAS const char* base = (LAS const char*)(lds) + (t & 1) * 32768;
      f32x16 p[2] = {f32x16{}, f32x16{}};
      for (int d0 = 0; d0 < 8; ++d0) for (int kb = 0; kb < 2; ++kb) {
        int r = kb * 32 + r32;
        bf16x8 kf = *(LAS const bf16x8*)(base + r * 256 + (((2 * d0 + hi) ^ (r & 7)) << 4));
        p[kb] = __builtin_amdgcn_mfma_f32_32x32x16_bf16(kf, qf[d0], p[kb], 0, 0, 0);
      }
      if (t * 64 + 63 > q0) for (int kb = 0; kb < 2; ++kb) for (int r = 0; r < 16; ++r) { int key = t * 64 + kb * 32 + (r & 3) + 8 * (r >> 2) + 4 * hi; if (key > q0 + r32) p[kb][r] = -INFINITY; }
      float mx = p[0][0]; for (int kb = 0; kb < 2; ++kb) for (int r = 0; r < 16; ++r) mx = fmaxf(mx, p[kb][r]);
      mx = fmaxf(mx, __shfl_xor(mx, 32));
      const float mn = fmaxf(m, mx), al = __builtin_amdgcn_exp2f(m - mn); m = mn;
      float ps = 0; for (int kb = 0; kb < 2; ++kb) for (int r = 0; r < 16; ++r) { p[kb][r] = __builtin_amdgcn_exp2f(p[kb][r] - mn); ps += p[kb][r]; }
      ps += __shfl_xor(ps, 32); l = l * al + ps;
      for (int i = 0; i < 4; ++i) o[i] *= al;
      for (int ks = 0; ks < 4; ++ks) {
        u32x4 w; for (int j = 0; j < 4; ++j) w[j] = cvtpk(p[ks >> 1][8 * (ks & 1) + 2 * j], p[ks >> 1][8 * (ks & 1) + 2 * j + 1]);
        bf16x8 pf = __builtin_bit_cast(bf16x8, w);
        for (int db = 0; db < 4; ++db) {
          LAS const char* sub = base + 16384 + ((2 * ks + hi) * 4 + db) * 512 + ((lane >> 2) & 3) * 64 + ((lane >> 4) & 1) * 32 + (lane & 3) * 8;
          s16x4 lo = __builtin_amdgcn_ds_read_tr16_b64_v4i16((LAS s16x4*)sub);
          s16x4 hi4 = __builtin_amdgcn_ds_read_tr16_b64_v4i16((LAS s16x4*)(sub + 256));
          bf16x8 vf = __builtin_shufflevector(lo, hi4, 0, 1, 2, 3, 4, 5, 6, 7);
          o[db] = __builtin_amdgcn_mfma_f32_32x32x16_bf16(vf, pf, o[db], 0, 0, 0);
        }
      }
      asm volatile("s_waitcnt vmcnt(0) lgkmcnt(0)" ::: "memory"); __builtin_amdgcn_s_barrier();
    }
    const float il = 1.f / l;
    for (int db = 0; db < 4; ++db) for (int g = 0; g < 4; ++g) {
      u32x2 w = {cvtpk(o[db][4 * g] * il, o[db][4 * g + 1] * il), cvtpk(o[db][4 * g + 2] * il, o[db][4 * g + 3] * il)};
      *(u32x2*)(O + (size_t)(q0 + r32) * HD + h * D + db * 32 + 8 * g + 4 * hi) = w;
    }
  }
}

struct PF { const uint4* p[8]; unsigned n[8]; };
__global__ __launch_bounds__(256) void k_pf(PF a, unsigned* sink) {
  unsigned acc = 0;
  for (int j = 0; j < 8; ++j) { const uint4* p = a.p[j]; unsigned n = a.n[j];
    for (unsigned i = blockIdx.x * 256 + threadIdx.x; i < n; i += gridDim.x * 256) { uint4 v = p[i]; acc ^= v.x ^ v.y ^ v.z ^ v.w; } }
  if (acc == 0x12345678u) sink[blockIdx.x] = acc;
}
extern "C" void kernel_launch(void* const* d_in, const int* in_sizes, int n_in, void* d_out, int out_size, void* d_ws, size_t ws_size, hipStream_t stream) {
  const u16* x = (const u16*)d_in[0]; const u16* ckv = (const u16*)d_in[1]; const u16* wq = (const u16*)d_in[2]; const u16* wuk = (const u16*)d_in[3];
  const u16* wuv = (const u16*)d_in[4]; const u16* wo = (const u16*)d_in[5]; const u16* w1 = (const u16*)d_in[7]; const u16* b1 = (const u16*)d_in[8];
  const u16* w2 = (const u16*)d_in[9]; const u16* b2 = (const u16*)d_in[10]; const u16* c = (const u16*)d_in[11]; const u16* wmod = (const u16*)d_in[12];
  char* ws = (char*)d_ws;
  float* mod = (float*)ws; u16* h = (u16*)(ws + (1 << 20)); u16* q = (u16*)(ws + (4 << 20)); u16* k = (u16*)(ws + (24 << 20)); u16* v = (u16*)(ws + (44 << 20));
  u16* ctx = (u16*)(ws + (64 << 20)); u16* x1 = (u16*)(ws + (84 << 20)); u16* h3T = (u16*)(ws + (88 << 20)); u16* u = (u16*)(ws + (92 << 20));
  hipFuncSetAttribute((const void*)k_gemm<128, 128, 0>, hipFuncAttributeMaxDynamicSharedMemorySize, 65536);
  hipFuncSetAttribute((const void*)k_attn, hipFuncAttributeMaxDynamicSharedMemorySize, 65536);
  PF pf; int ids[8] = {0, 1, 2, 3, 4, 5, 7, 9}; for (int j = 0; j < 8; ++j) { pf.p[j] = (const uint4*)d_in[ids[j]]; pf.n[j] = in_sizes[ids[j]] / 8; }
  k_pf<<<2048, 256, 0, stream>>>(pf, (unsigned*)(ws + (200 << 20)));
  k_mod<<<54, 256, 0, stream>>>(c, wmod, mod);
  k_ln1<<<512, 256, 0, stream>>>(x, mod, h);
  EpiArgs e{nullptr, nullptr, mod, q, HD, 0.12751743082f};
  k_gemm<128, 128, 0><<<16 * 32, 256, 65536, stream>>>(h, M, wq, HD, M, 32, e);
  e.out = k; e.scale = 1.f; k_gemm<128, 128, 0><<<16 * 32, 256, 65536, stream>>>(ckv, DC, wuk, HD, DC, 32, e);
  e.out = v; k_gemm<128, 128, 0><<<16 * 32, 256, 65536, stream>>>(ckv, DC, wuv, HD, DC, 32, e);
  k_attn<<<256, 256, 65536, stream>>>(q, k, v, ctx);
  e.out = x1; e.ldo = M; e.p0 = x; k_gemm<128, 64, 2><<<16 * 9, 256, 49152, stream>>>(ctx, HD, wo, M, HD, 9, e);
  k_ln2<<<512, 256, 0, stream>>>(x1, mod, h3T);
  e.out = u; e.ldo = F; e.p0 = b1; k_gemm<64, 128, 3><<<9 * 36, 256, 49152, stream>>>(h3T, S, w1, F, S, 36, e);
  e.out = (u16*)d_out; e.p0 = x1; e.p1 = b2; k_gemm<64, 128, 4><<<9 * 16, 256, 49152, stream>>>(u, F, w2, S, F, 16, e);
}
```

```cpp
#include <hip/hip_runtime.h>
#include <hip/hip_bf16.h>
#include <stdint.h>
typedef unsigned short u16;
typedef short bf16x8 __attribute__((ext_vector_type(8)));
typedef short s16x4 __attribute__((ext_vector_type(4)));
typedef float f32x16 __attribute__((ext_vector_type(16)));
typedef float f32x4 __attribute__((ext_vector_type(4)));
typedef unsigned u32x4 __attribute__((ext_vector_type(4)));
typedef unsigned u32x2 __attribute__((ext_vector_type(2)));
#define LAS __attribute__((address_space(3)))
#define DI __device__ __forceinline__
constexpr int S = 2048, M = 576, H = 32, D = 128, HD = 4096, DC = 512, F = 4608;

DI float bf2f(u16 x) { return __uint_as_float(((unsigned)x) << 16); }
typedef float f32x2 __attribute__((ext_vector_type(2)));
typedef __bf16 bf16x2 __attribute__((ext_vector_type(2)));
DI unsigned cvtpk(float lo, float hi) { return __builtin_bit_cast(unsigned, __builtin_convertvector(f32x2{lo, hi}, bf16x2)); }
DI u16 f2bf(float x) { return (u16)(cvtpk(x, x) & 0xffff); }
DI float rbf(float x) { return bf2f(f2bf(x)); }
#define MEGA
DI float lo16(unsigned v) { return __uint_as_float(v << 16); }
DI float hi16(unsigned v) { return __uint_as_float(v & 0xffff0000u); }
template <int CTRL> DI float dpp_add(float v) { return v + __uint_as_float(__builtin_amdgcn_update_dpp(0u, __float_as_uint(v), CTRL, 0xf, 0xf, true)); }
DI float sum16(float v) { v = dpp_add<0x128>(v); v = dpp_add<0x124>(v); v = dpp_add<0x122>(v); return dpp_add<0x121>(v); }
DI float sum32(float v) { v = sum16(v); auto r = __builtin_amdgcn_permlane16_swap(__float_as_uint(v), __float_as_uint(v), false, false); return __uint_as_float(r[0]) + __uint_as_float(r[1]); }
DI float sum64(float v) { v = sum32(v); auto r = __builtin_amdgcn_permlane32_swap(__float_as_uint(v), __float_as_uint(v), false, false); return __uint_as_float(r[0]) + __uint_as_float(r[1]); }
__global__ __launch_bounds__(512) void k_mod(const u16* c, const u16* w, float* mod, unsigned* flags) {
  __shared__ float red[8][64];
  const int t = threadIdx.x, cg = t & 7, rg = t >> 3, lane = t & 63;
  if (blockIdx.x < 4) flags[blockIdx.x * 512 + t] = 0u; else if (blockIdx.x < 6) flags[2048 + ((blockIdx.x - 4) * 512 + t) * 32] = 0u;
  u32x4 v[9]; u16 cv[9];
#pragma unroll
  for (int i = 0; i < 9; ++i) { v[i] = *(const u32x4*)(w + (size_t)(rg + 64 * i) * 3456 + blockIdx.x * 64 + cg * 8); cv[i] = c[rg + 64 * i]; }
  float acc[8] = {0, 0, 0, 0, 0, 0, 0, 0};
#pragma unroll
  for (int i = 0; i < 9; ++i) { const float x = bf2f(cv[i]), s = rbf(x * __builtin_amdgcn_rcpf(1.f + __builtin_amdgcn_exp2f(-1.4426950409f * x)));
#pragma unroll
    for (int k = 0; k < 4; ++k) { acc[2 * k] += s * lo16(v[i][k]); acc[2 * k + 1] += s * hi16(v[i][k]); } }
#pragma unroll
  for (int k = 0; k < 8; ++k) { float a = dpp_add<0x128>(acc[k]);
    auto r = __builtin_amdgcn_permlane16_swap(__float_as_uint(a), __float_as_uint(a), false, false); a = __uint_as_float(r[0]) + __uint_as_float(r[1]);
    r = __builtin_amdgcn_permlane32_swap(__float_as_uint(a), __float_as_uint(a), false, false); acc[k] = __uint_as_float(r[0]) + __uint_as_float(r[1]); }
  if (lane < 8) { *(f32x4*)&red[t >> 6][lane * 8] = f32x4{acc[0], acc[1], acc[2], acc[3]}; *(f32x4*)&red[t >> 6][lane * 8 + 4] = f32x4{acc[4], acc[5], acc[6], acc[7]}; }
  __syncthreads();
  if (t < 64) { float s = 0; for (int r = 0; r < 8; ++r) s += red[r][t]; s = rbf(s);
    const int part = blockIdx.x / 9, col = (blockIdx.x % 9) * 64 + t;
    if (part == 0) mod[M + col] = s * 0.12751743082f; else if (part == 1) mod[col] = rbf(1.f + s) * 0.12751743082f; else if (part == 4) mod[4 * M + col] = rbf(1.f + s); else mod[part * M + col] = s; }
}
DI void gemm_c(f32x16 (&acc)[5], const void* xb, const void* wb, unsigned lw, unsigned rot, unsigned tid, unsigned& t1, unsigned& t2, const void* fb, unsigned p0, unsigned p1, unsigned tag) {
  asm volatile(
"s_mov_b64 s[22:23], %[wb]\n"
"s_mov_b32 s24, %[lw]\n"
"v_and_b32 v32, 63, %[tid]\n"
"v_lshrrev_b32 v34, 3, v32\n"
"v_lshrrev_b32 v35, 2, v34\n"
"v_and_b32 v33, 3, v34\n"
"v_lshl_add_u32 v35, v35, 4, v33\n"
"v_mul_u32_u24 v35, 1152, v35\n"
"v_bfe_u32 v33, v32, 4, 1\n"
"v_lshlrev_b32 v33, 1, v33\n"
"v_and_b32 v34, 7, v32\n"
"v_xor_b32 v33, v33, v34\n"
"v_lshlrev_b32 v33, 4, v33\n"
"v_xor_b32 v34, 64, v33\n"
"v_add_u32 v33, v33, v35\n"
"v_add_u32 v34, v34, v35\n"
"v_add_u32 v44, 0, v33\n"
"v_add_u32 v45, 3584, v34\n"
"v_add_u32 v46, 7168, v33\n"
"v_add_u32 v47, 10752, v34\n"
"s_mov_b64 s[20:21], %[xb]\n"
"s_mov_b32 s25, %[rot]\n"
"v_lshlrev_b32 v52, 4, v32\n"
"v_add_u32 v53, 131072, v52\n"
"v_add_u32 v54, 262144, v52\n"
"v_add_u32 v55, 393216, v52\n"
"v_add_u32 v56, 524288, v52\n"
"s_mul_i32 s32, s25, 36864\n"
"s_lshl_b32 s50, s25, 10\n"
"s_add_u32 s25, s25, 1\n"
"s_cmp_eq_u32 s25, 4\n"
"s_cselect_b32 s25, 0, s25\n"
"s_add_u32 m0, s24, 0\n"
"s_add_u32 s26, s22, s32\n"
"s_addc_u32 s27, s23, 0\n"
"global_load_lds_dwordx4 v44, s[26:27] offset:0\n"
"global_load_lds_dwordx4 v45, s[26:27] offset:1024\n"
"global_load_lds_dwordx4 v46, s[26:27] offset:2048\n"
"global_load_lds_dwordx4 v47, s[26:27] offset:3072\n"
"s_mul_i32 s33, s25, 36864\n"
"s_lshl_b32 s51, s25, 10\n"
"s_add_u32 s25, s25, 1\n"
"s_cmp_eq_u32 s25, 4\n"
"s_cselect_b32 s25, 0, s25\n"
"s_add_u32 m0, s24, 4096\n"
"s_add_u32 s26, s22, s33\n"
"s_addc_u32 s27, s23, 0\n"
"global_load_lds_dwordx4 v44, s[26:27] offset:0\n"
"global_load_lds_dwordx4 v45, s[26:27] offset:1024\n"
"global_load_lds_dwordx4 v46, s[26:27] offset:2048\n"
"global_load_lds_dwordx4 v47, s[26:27] offset:3072\n"
"v_lshrrev_b32 v33, 4, v32\n"
"v_lshlrev_b32 v33, 10, v33\n"
"v_bfe_u32 v34, v32, 2, 2\n"
"v_lshl_add_u32 v33, v34, 7, v33\n"
"v_and_b32 v34, 3, v32\n"
"v_lshl_add_u32 v33, v34, 3, v33\n"
"v_add_u32 v33, s24, v33\n"
"v_bfe_u32 v34, v32, 3, 2\n"
"v_lshrrev_b32 v35, 6, %[tid]\n"
"v_xor_b32 v34, v34, v35\n"
"v_xor_b32 v35, 0, v34\n"
"v_and_b32 v58, 3, v35\n"
"v_lshl_add_u32 v58, v58, 5, v33\n"
"v_lshrrev_b32 v35, 2, v35\n"
"v_lshl_add_u32 v36, v35, 12, v58\n"
"v_xor_b32 v35, 1, v34\n"
"v_and_b32 v58, 3, v35\n"
"v_lshl_add_u32 v58, v58, 5, v33\n"
"v_lshrrev_b32 v35, 2, v35\n"
"v_lshl_add_u32 v37, v35, 12, v58\n"
"v_xor_b32 v35, 2, v34\n"
"v_and_b32 v58, 3, v35\n"
"v_lshl_add_u32 v58, v58, 5, v33\n"
"v_lshrrev_b32 v35, 2, v35\n"
"v_lshl_add_u32 v38, v35, 12, v58\n"
"v_xor_b32 v35, 3, v34\n"
"v_and_b32 v58, 3, v35\n"
"v_lshl_add_u32 v58, v58, 5, v33\n"
"v_lshrrev_b32 v35, 2, v35\n"
"v_lshl_add_u32 v39, v35, 12, v58\n"
"global_load_dword v60, %[p0], %[fb] sc1\n"
"v_mov_b32 v64, 0\n"
"v_mov_b32 v65, 0\n"
"v_mov_b32 v66, 0\n"
"v_mov_b32 v67, 0\n"
"v_mov_b32 v68, 0\n"
"v_mov_b32 v69, 0\n"
"v_mov_b32 v70, 0\n"
"v_mov_b32 v71, 0\n"
"v_mov_b32 v72, 0\n"
"v_mov_b32 v73, 0\n"
"v_mov_b32 v74, 0\n"
"v_mov_b32 v75, 0\n"
"v_mov_b32 v76, 0\n"
"v_mov_b32 v77, 0\n"
"v_mov_b32 v78, 0\n"
"v_mov_b32 v79, 0\n"
"v_mov_b32 v80, 0\n"
"v_mov_b32 v81, 0\n"
"v_mov_b32 v82, 0\n"
"v_mov_b32 v83, 0\n"
"v_mov_b32 v84, 0\n"
"v_mov_b32 v85, 0\n"
"v_mov_b32 v86, 0\n"
"v_mov_b32 v87, 0\n"
"v_mov_b32 v88, 0\n"
"v_mov_b32 v89, 0\n"
"v_mov_b32 v90, 0\n"
"v_mov_b32 v91, 0\n"
"v_mov_b32 v92, 0\n"
"v_mov_b32 v93, 0\n"
"v_mov_b32 v94, 0\n"
"v_mov_b32 v95, 0\n"
"v_mov_b32 v96, 0\n"
"v_mov_b32 v97, 0\n"
"v_mov_b32 v98, 0\n"
"v_mov_b32 v99, 0\n"
"v_mov_b32 v100, 0\n"
"v_mov_b32 v101, 0\n"
"v_mov_b32 v102, 0\n"
"v_mov_b32 v103, 0\n"
"v_mov_b32 v104, 0\n"
"v_mov_b32 v105, 0\n"
"v_mov_b32 v106, 0\n"
"v_mov_b32 v107, 0\n"
"v_mov_b32 v108, 0\n"
"v_mov_b32 v109, 0\n"
"v_mov_b32 v110, 0\n"
"v_mov_b32 v111, 0\n"
"v_mov_b32 v112, 0\n"
"v_mov_b32 v113, 0\n"
"v_mov_b32 v114, 0\n"
"v_mov_b32 v115, 0\n"
"v_mov_b32 v116, 0\n"
"v_mov_b32 v117, 0\n"
"v_mov_b32 v118, 0\n"
"v_mov_b32 v119, 0\n"
"v_mov_b32 v120, 0\n"
"v_mov_b32 v121, 0\n"
"v_mov_b32 v122, 0\n"
"v_mov_b32 v123, 0\n"
"v_mov_b32 v124, 0\n"
"v_mov_b32 v125, 0\n"
"v_mov_b32 v126, 0\n"
"v_mov_b32 v127, 0\n"
"v_mov_b32 v128, 0\n"
"v_mov_b32 v129, 0\n"
"v_mov_b32 v130, 0\n"
"v_mov_b32 v131, 0\n"
"v_mov_b32 v132, 0\n"
"v_mov_b32 v133, 0\n"
"v_mov_b32 v134, 0\n"
"v_mov_b32 v135, 0\n"
"v_mov_b32 v136, 0\n"
"v_mov_b32 v137, 0\n"
"v_mov_b32 v138, 0\n"
"v_mov_b32 v139, 0\n"
"v_mov_b32 v140, 0\n"
"v_mov_b32 v141, 0\n"
"v_mov_b32 v142, 0\n"
"v_mov_b32 v143, 0\n"
"s_mov_b32 s30, 0x40000\n"
"5:\n"
"s_waitcnt vmcnt(0)\n"
"v_xor_b32 v60, %[tag], v60\n"
"v_cmp_ne_u32 vcc, 0, v60\n"
"s_cbranch_vccz 6f\n"
"s_sub_u32 s30, s30, 1\n"
"s_cmp_eq_u32 s30, 0\n"
"s_cbranch_scc1 6f\n"
"s_sleep 1\n"
"global_load_dword v60, %[p0], %[fb] sc1\n"
"s_branch 5b\n"
"6:\n"
"s_add_u32 s28, s20, s50\n"
"s_addc_u32 s29, s21, 0\n"
"global_load_dwordx4 a[0:3], v52, s[28:29] sc1\n"
"global_load_dwordx4 a[4:7], v53, s[28:29] sc1\n"
"global_load_dwordx4 a[8:11], v54, s[28:29] sc1\n"
"global_load_dwordx4 a[12:15], v55, s[28:29] sc1\n"
"global_load_dwordx4 a[16:19], v56, s[28:29] sc1\n"
"s_add_u32 s28, s20, s51\n"
"s_addc_u32 s29, s21, 0\n"
"global_load_dwordx4 a[20:23], v52, s[28:29] sc1\n"
"global_load_dwordx4 a[24:27], v53, s[28:29] sc1\n"
"global_load_dwordx4 a[28:31], v54, s[28:29] sc1\n"
"global_load_dwordx4 a[32:35], v55, s[28:29] sc1\n"
"global_load_dwordx4 a[36:39], v56, s[28:29] sc1\n"
"s_mul_i32 s34, s25, 36864\n"
"s_lshl_b32 s52, s25, 10\n"
"s_add_u32 s25, s25, 1\n"
"s_cmp_eq_u32 s25, 4\n"
"s_cselect_b32 s25, 0, s25\n"
"s_add_u32 m0, s24, 8192\n"
"s_add_u32 s26, s22, s34\n"
"s_addc_u32 s27, s23, 0\n"
"global_load_lds_dwordx4 v44, s[26:27] offset:0\n"
"s_add_u32 s28, s20, s52\n"
"s_addc_u32 s29, s21, 0\n"
"global_load_lds_dwordx4 v45, s[26:27] offset:1024\n"
"global_load_lds_dwordx4 v46, s[26:27] offset:2048\n"
"global_load_lds_dwordx4 v47, s[26:27] offset:3072\n"
"global_load_dwordx4 a[40:43], v52, s[28:29] sc1\n"
"global_load_dwordx4 a[44:47], v53, s[28:29] sc1\n"
"global_load_dwordx4 a[48:51], v54, s[28:29] sc1\n"
"global_load_dwordx4 a[52:55], v55, s[28:29] sc1\n"
"global_load_dwordx4 a[56:59], v56, s[28:29] sc1\n"
"s_mul_i32 s35, s25, 36864\n"
"s_lshl_b32 s53, s25, 10\n"
"s_add_u32 s25, s25, 1\n"
"s_cmp_eq_u32 s25, 4\n"
"s_cselect_b32 s25, 0, s25\n"
"s_add_u32 m0, s24, 12288\n"
"s_add_u32 s26, s22, s35\n"
"s_addc_u32 s27, s23, 0\n"
"global_load_lds_dwordx4 v44, s[26:27] offset:0\n"
"s_add_u32 s28, s20, s53\n"
"s_addc_u32 s29, s21, 0\n"
"global_load_lds_dwordx4 v45, s[26:27] offset:1024\n"
"global_load_lds_dwordx4 v46, s[26:27] offset:2048\n"
"global_load_lds_dwordx4 v47, s[26:27] offset:3072\n"
"global_load_dwordx4 a[60:63], v52, s[28:29] sc1\n"
"global_load_dwordx4 a[64:67], v53, s[28:29] sc1\n"
"global_load_dwordx4 a[68:71], v54, s[28:29] sc1\n"
"global_load_dwordx4 a[72:75], v55, s[28:29] sc1\n"
"global_load_dwordx4 a[76:79], v56, s[28:29] sc1\n"
"ds_read_b64_tr_b16 a[80:81], v36 offset:0\n"
"ds_read_b64_tr_b16 a[82:83], v36 offset:512\n"
"ds_read_b64_tr_b16 a[84:85], v37 offset:0\n"
"ds_read_b64_tr_b16 a[86:87], v37 offset:512\n"
"ds_read_b64_tr_b16 a[88:89], v38 offset:0\n"
"ds_read_b64_tr_b16 a[90:91], v38 offset:512\n"
"ds_read_b64_tr_b16 a[92:93], v39 offset:0\n"
"ds_read_b64_tr_b16 a[94:95], v39 offset:512\n"
"s_mov_b32 s25, 7\n"
".p2align 4\n"
"1:\n"
"s_waitcnt vmcnt(23) lgkmcnt(0)\n"
"s_add_u32 m0, s24, 0\n"
"s_add_u32 s22, s22, 147456\n"
"s_addc_u32 s23, s23, 0\n"
"s_add_u32 s20, s20, 4096\n"
"s_addc_u32 s21, s21, 0\n"
"s_add_u32 s26, s22, s32\n"
"s_addc_u32 s27, s23, 0\n"
"v_mfma_f32_16x16x32_bf16 v[64:67], a[80:83], a[0:3], v[64:67]\n"
"global_load_lds_dwordx4 v44, s[26:27] offset:0\n"
"s_add_u32 s28, s20, s50\n"
"s_addc_u32 s29, s21, 0\n"
"ds_read_b64_tr_b16 a[96:97], v36 offset:4096\n"
"v_mfma_f32_16x16x32_bf16 v[68:71], a[84:87], a[0:3], v[68:71]\n"
"global_load_lds_dwordx4 v45, s[26:27] offset:1024\n"
"v_mfma_f32_16x16x32_bf16 v[72:75], a[88:91], a[0:3], v[72:75]\n"
"global_load_lds_dwordx4 v46, s[26:27] offset:2048\n"
"ds_read_b64_tr_b16 a[98:99], v36 offset:4608\n"
"v_mfma_f32_16x16x32_bf16 v[76:79], a[92:95], a[0:3], v[76:79]\n"
"global_load_lds_dwordx4 v47, s[26:27] offset:3072\n"
"global_load_dwordx4 a[0:3], v52, s[28:29] sc1\n"
"v_mfma_f32_16x16x32_bf16 v[80:83], a[80:83], a[4:7], v[80:83]\n"
"ds_read_b64_tr_b16 a[100:101], v37 offset:4096\n"
"v_mfma_f32_16x16x32_bf16 v[84:87], a[84:87], a[4:7], v[84:87]\n"
"v_mfma_f32_16x16x32_bf16 v[88:91], a[88:91], a[4:7], v[88:91]\n"
"ds_read_b64_tr_b16 a[102:103], v37 offset:4608\n"
"v_mfma_f32_16x16x32_bf16 v[92:95], a[92:95], a[4:7], v[92:95]\n"
"global_load_dwordx4 a[4:7], v53, s[28:29] sc1\n"
"v_mfma_f32_16x16x32_bf16 v[96:99], a[80:83], a[8:11], v[96:99]\n"
"ds_read_b64_tr_b16 a[104:105], v38 offset:4096\n"
"v_mfma_f32_16x16x32_bf16 v[100:103], a[84:87], a[8:11], v[100:103]\n"
"v_mfma_f32_16x16x32_bf16 v[104:107], a[88:91], a[8:11], v[104:107]\n"
"ds_read_b64_tr_b16 a[106:107], v38 offset:4608\n"
"v_mfma_f32_16x16x32_bf16 v[108:111], a[92:95], a[8:11], v[108:111]\n"
"global_load_dwordx4 a[8:11], v54, s[28:29] sc1\n"
"v_mfma_f32_16x16x32_bf16 v[112:115], a[80:83], a[12:15], v[112:115]\n"
"ds_read_b64_tr_b16 a[108:109], v39 offset:4096\n"
"v_mfma_f32_16x16x32_bf16 v[116:119], a[84:87], a[12:15], v[116:119]\n"
"v_mfma_f32_16x16x32_bf16 v[120:123], a[88:91], a[12:15], v[120:123]\n"
"ds_read_b64_tr_b16 a[110:111], v39 offset:4608\n"
"v_mfma_f32_16x16x32_bf16 v[124:127], a[92:95], a[12:15], v[124:127]\n"
"global_load_dwordx4 a[12:15], v55, s[28:29] sc1\n"
"v_mfma_f32_16x16x32_bf16 v[128:131], a[80:83], a[16:19], v[128:131]\n"
"v_mfma_f32_16x16x32_bf16 v[132:135], a[84:87], a[16:19], v[132:135]\n"
"v_mfma_f32_16x16x32_bf16 v[136:139], a[88:91], a[16:19], v[136:139]\n"
"v_mfma_f32_16x16x32_bf16 v[140:143], a[92:95], a[16:19], v[140:143]\n"
"global_load_dwordx4 a[16:19], v56, s[28:29] sc1\n"
"s_waitcnt vmcnt(23) lgkmcnt(0)\n"
"s_add_u32 m0, s24, 4096\n"
"s_add_u32 s26, s22, s33\n"
"s_addc_u32 s27, s23, 0\n"
"v_mfma_f32_16x16x32_bf16 v[64:67], a[96:99], a[20:23], v[64:67]\n"
"global_load_lds_dwordx4 v44, s[26:27] offset:0\n"
"s_add_u32 s28, s20, s51\n"
"s_addc_u32 s29, s21, 0\n"
"ds_read_b64_tr_b16 a[80:81], v36 offset:8192\n"
"v_mfma_f32_16x16x32_bf16 v[68:71], a[100:103], a[20:23], v[68:71]\n"
"global_load_lds_dwordx4 v45, s[26:27] offset:1024\n"
"v_mfma_f32_16x16x32_bf16 v[72:75], a[104:107], a[20:23], v[72:75]\n"
"global_load_lds_dwordx4 v46, s[26:27] offset:2048\n"
"ds_read_b64_tr_b16 a[82:83], v36 offset:8704\n"
"v_mfma_f32_16x16x32_bf16 v[76:79], a[108:111], a[20:23], v[76:79]\n"
"global_load_lds_dwordx4 v47, s[26:27] offset:3072\n"
"global_load_dwordx4 a[20:23], v52, s[28:29] sc1\n"
"v_mfma_f32_16x16x32_bf16 v[80:83], a[96:99], a[24:27], v[80:83]\n"
"ds_read_b64_tr_b16 a[84:85], v37 offset:8192\n"
"v_mfma_f32_16x16x32_bf16 v[84:87], a[100:103], a[24:27], v[84:87]\n"
"v_mfma_f32_16x16x32_bf16 v[88:91], a[104:107], a[24:27], v[88:91]\n"
"ds_read_b64_tr_b16 a[86:87], v37 offset:8704\n"
"v_mfma_f32_16x16x32_bf16 v[92:95], a[108:111], a[24:27], v[92:95]\n"
"global_load_dwordx4 a[24:27], v53, s[28:29] sc1\n"
"v_mfma_f32_16x16x32_bf16 v[96:99], a[96:99], a[28:31], v[96:99]\n"
"ds_read_b64_tr_b16 a[88:89], v38 offset:8192\n"
"v_mfma_f32_16x16x32_bf16 v[100:103], a[100:103], a[28:31], v[100:103]\n"
"v_mfma_f32_16x16x32_bf16 v[104:107], a[104:107], a[28:31], v[104:107]\n"
"ds_read_b64_tr_b16 a[90:91], v38 offset:8704\n"
"v_mfma_f32_16x16x32_bf16 v[108:111], a[108:111], a[28:31], v[108:111]\n"
"global_load_dwordx4 a[28:31], v54, s[28:29] sc1\n"
"v_mfma_f32_16x16x32_bf16 v[112:115], a[96:99], a[32:35], v[112:115]\n"
"ds_read_b64_tr_b16 a[92:93], v39 offset:8192\n"
"v_mfma_f32_16x16x32_bf16 v[116:119], a[100:103], a[32:35], v[116:119]\n"
"v_mfma_f32_16x16x32_bf16 v[120:123], a[104:107], a[32:35], v[120:123]\n"
"ds_read_b64_tr_b16 a[94:95], v39 offset:8704\n"
"v_mfma_f32_16x16x32_bf16 v[124:127], a[108:111], a[32:35], v[124:127]\n"
"global_load_dwordx4 a[32:35], v55, s[28:29] sc1\n"
"v_mfma_f32_16x16x32_bf16 v[128:131], a[96:99], a[36:39], v[128:131]\n"
"v_mfma_f32_16x16x32_bf16 v[132:135], a[100:103], a[36:39], v[132:135]\n"
"v_mfma_f32_16x16x32_bf16 v[136:139], a[104:107], a[36:39], v[136:139]\n"
"v_mfma_f32_16x16x32_bf16 v[140:143], a[108:111], a[36:39], v[140:143]\n"
"global_load_dwordx4 a[36:39], v56, s[28:29] sc1\n"
"s_waitcnt vmcnt(23) lgkmcnt(0)\n"
"s_add_u32 m0, s24, 8192\n"
"s_add_u32 s26, s22, s34\n"
"s_addc_u32 s27, s23, 0\n"
"v_mfma_f32_16x16x32_bf16 v[64:67], a[80:83], a[40:43], v[64:67]\n"
"global_load_lds_dwordx4 v44, s[26:27] offset:0\n"
"s_add_u32 s28, s20, s52\n"
"s_addc_u32 s29, s21, 0\n"
"ds_read_b64_tr_b16 a[96:97], v36 offset:12288\n"
"v_mfma_f32_16x16x32_bf16 v[68:71], a[84:87], a[40:43], v[68:71]\n"
"global_load_lds_dwordx4 v45, s[26:27] offset:1024\n"
"v_mfma_f32_16x16x32_bf16 v[72:75], a[88:91], a[40:43], v[72:75]\n"
"global_load_lds_dwordx4 v46, s[26:27] offset:2048\n"
"ds_read_b64_tr_b16 a[98:99], v36 offset:12800\n"
"v_mfma_f32_16x16x32_bf16 v[76:79], a[92:95], a[40:43], v[76:79]\n"
"global_load_lds_dwordx4 v47, s[26:27] offset:3072\n"
"global_load_dwordx4 a[40:43], v52, s[28:29] sc1\n"
"v_mfma_f32_16x16x32_bf16 v[80:83], a[80:83], a[44:47], v[80:83]\n"
"ds_read_b64_tr_b16 a[100:101], v37 offset:12288\n"
"v_mfma_f32_16x16x32_bf16 v[84:87], a[84:87], a[44:47], v[84:87]\n"
"v_mfma_f32_16x16x32_bf16 v[88:91], a[88:91], a[44:47], v[88:91]\n"
"ds_read_b64_tr_b16 a[102:103], v37 offset:12800\n"
"v_mfma_f32_16x16x32_bf16 v[92:95], a[92:95], a[44:47], v[92:95]\n"
"global_load_dwordx4 a[44:47], v53, s[28:29] sc1\n"
"v_mfma_f32_16x16x32_bf16 v[96:99], a[80:83], a[48:51], v[96:99]\n"
"ds_read_b64_tr_b16 a[104:105], v38 offset:12288\n"
"v_mfma_f32_16x16x32_bf16 v[100:103], a[84:87], a[48:51], v[100:103]\n"
"v_mfma_f32_16x16x32_bf16 v[104:107], a[88:91], a[48:51], v[104:107]\n"
"ds_read_b64_tr_b16 a[106:107], v38 offset:12800\n"
"v_mfma_f32_16x16x32_bf16 v[108:111], a[92:95], a[48:51], v[108:111]\n"
"global_load_dwordx4 a[48:51], v54, s[28:29] sc1\n"
"v_mfma_f32_16x16x32_bf16 v[112:115], a[80:83], a[52:55], v[112:115]\n"
"ds_read_b64_tr_b16 a[108:109], v39 offset:12288\n"
"v_mfma_f32_16x16x32_bf16 v[116:119], a[84:87], a[52:55], v[116:119]\n"
"v_mfma_f32_16x16x32_bf16 v[120:123], a[88:91], a[52:55], v[120:123]\n"
"ds_read_b64_tr_b16 a[110:111], v39 offset:12800\n"
"v_mfma_f32_16x16x32_bf16 v[124:127], a[92:95], a[52:55], v[124:127]\n"
"global_load_dwordx4 a[52:55], v55, s[28:29] sc1\n"
"v_mfma_f32_16x16x32_bf16 v[128:131], a[80:83], a[56:59], v[128:131]\n"
"v_mfma_f32_16x16x32_bf16 v[132:135], a[84:87], a[56:59], v[132:135]\n"
"v_mfma_f32_16x16x32_bf16 v[136:139], a[88:91], a[56:59], v[136:139]\n"
"v_mfma_f32_16x16x32_bf16 v[140:143], a[92:95], a[56:59], v[140:143]\n"
"global_load_dwordx4 a[56:59], v56, s[28:29] sc1\n"
"s_waitcnt vmcnt(23) lgkmcnt(0)\n"
"s_add_u32 m0, s24, 12288\n"
"s_add_u32 s26, s22, s35\n"
"s_addc_u32 s27, s23, 0\n"
"v_mfma_f32_16x16x32_bf16 v[64:67], a[96:99], a[60:63], v[64:67]\n"
"global_load_lds_dwordx4 v44, s[26:27] offset:0\n"
"s_add_u32 s28, s20, s53\n"
"s_addc_u32 s29, s21, 0\n"
"ds_read_b64_tr_b16 a[80:81], v36 offset:0\n"
"v_mfma_f32_16x16x32_bf16 v[68:71], a[100:103], a[60:63], v[68:71]\n"
"global_load_lds_dwordx4 v45, s[26:27] offset:1024\n"
"v_mfma_f32_16x16x32_bf16 v[72:75], a[104:107], a[60:63], v[72:75]\n"
"global_load_lds_dwordx4 v46, s[26:27] offset:2048\n"
"ds_read_b64_tr_b16 a[82:83], v36 offset:512\n"
"v_mfma_f32_16x16x32_bf16 v[76:79], a[108:111], a[60:63], v[76:79]\n"
"global_load_lds_dwordx4 v47, s[26:27] offset:3072\n"
"global_load_dwordx4 a[60:63], v52, s[28:29] sc1\n"
"v_mfma_f32_16x16x32_bf16 v[80:83], a[96:99], a[64:67], v[80:83]\n"
"ds_read_b64_tr_b16 a[84:85], v37 offset:0\n"
"v_mfma_f32_16x16x32_bf16 v[84:87], a[100:103], a[64:67], v[84:87]\n"
"v_mfma_f32_16x16x32_bf16 v[88:91], a[104:107], a[64:67], v[88:91]\n"
"ds_read_b64_tr_b16 a[86:87], v37 offset:512\n"
"v_mfma_f32_16x16x32_bf16 v[92:95], a[108:111], a[64:67], v[92:95]\n"
"global_load_dwordx4 a[64:67], v53, s[28:29] sc1\n"
"v_mfma_f32_16x16x32_bf16 v[96:99], a[96:99], a[68:71], v[96:99]\n"
"ds_read_b64_tr_b16 a[88:89], v38 offset:0\n"
"v_mfma_f32_16x16x32_bf16 v[100:103], a[100:103], a[68:71], v[100:103]\n"
"v_mfma_f32_16x16x32_bf16 v[104:107], a[104:107], a[68:71], v[104:107]\n"
"ds_read_b64_tr_b16 a[90:91], v38 offset:512\n"
"v_mfma_f32_16x16x32_bf16 v[108:111], a[108:111], a[68:71], v[108:111]\n"
"global_load_dwordx4 a[68:71], v54, s[28:29] sc1\n"
"v_mfma_f32_16x16x32_bf16 v[112:115], a[96:99], a[72:75], v[112:115]\n"
"ds_read_b64_tr_b16 a[92:93], v39 offset:0\n"
"v_mfma_f32_16x16x32_bf16 v[116:119], a[100:103], a[72:75], v[116:119]\n"
"v_mfma_f32_16x16x32_bf16 v[120:123], a[104:107], a[72:75], v[120:123]\n"
"ds_read_b64_tr_b16 a[94:95], v39 offset:512\n"
"v_mfma_f32_16x16x32_bf16 v[124:127], a[108:111], a[72:75], v[124:127]\n"
"global_load_dwordx4 a[72:75], v55, s[28:29] sc1\n"
"v_mfma_f32_16x16x32_bf16 v[128:131], a[96:99], a[76:79], v[128:131]\n"
"v_mfma_f32_16x16x32_bf16 v[132:135], a[100:103], a[76:79], v[132:135]\n"
"v_mfma_f32_16x16x32_bf16 v[136:139], a[104:107], a[76:79], v[136:139]\n"
"v_mfma_f32_16x16x32_bf16 v[140:143], a[108:111], a[76:79], v[140:143]\n"
"global_load_dwordx4 a[76:79], v56, s[28:29] sc1\n"
"s_sub_u32 s25, s25, 1\n"
"s_cmp_lg_u32 s25, 0\n"
"s_cbranch_scc1 1b\n"
"s_waitcnt vmcnt(23) lgkmcnt(0)\n"
"v_mfma_f32_16x16x32_bf16 v[64:67], a[80:83], a[0:3], v[64:67]\n"
"ds_read_b64_tr_b16 a[96:97], v36 offset:4096\n"
"v_mfma_f32_16x16x32_bf16 v[68:71], a[84:87], a[0:3], v[68:71]\n"
"v_mfma_f32_16x16x32_bf16 v[72:75], a[88:91], a[0:3], v[72:75]\n"
"ds_read_b64_tr_b16 a[98:99], v36 offset:4608\n"
"v_mfma_f32_16x16x32_bf16 v[76:79], a[92:95], a[0:3], v[76:79]\n"
"v_mfma_f32_16x16x32_bf16 v[80:83], a[80:83], a[4:7], v[80:83]\n"
"ds_read_b64_tr_b16 a[100:101], v37 offset:4096\n"
"v_mfma_f32_16x16x32_bf16 v[84:87], a[84:87], a[4:7], v[84:87]\n"
"v_mfma_f32_16x16x32_bf16 v[88:91], a[88:91], a[4:7], v[88:91]\n"
"ds_read_b64_tr_b16 a[102:103], v37 offset:4608\n"
"v_mfma_f32_16x16x32_bf16 v[92:95], a[92:95], a[4:7], v[92:95]\n"
"v_mfma_f32_16x16x32_bf16 v[96:99], a[80:83], a[8:11], v[96:99]\n"
"ds_read_b64_tr_b16 a[104:105], v38 offset:4096\n"
"v_mfma_f32_16x16x32_bf16 v[100:103], a[84:87], a[8:11], v[100:103]\n"
"v_mfma_f32_16x16x32_bf16 v[104:107], a[88:91], a[8:11], v[104:107]\n"
"ds_read_b64_tr_b16 a[106:107], v38 offset:4608\n"
"v_mfma_f32_16x16x32_bf16 v[108:111], a[92:95], a[8:11], v[108:111]\n"
"v_mfma_f32_16x16x32_bf16 v[112:115], a[80:83], a[12:15], v[112:115]\n"
"ds_read_b64_tr_b16 a[108:109], v39 offset:4096\n"
"v_mfma_f32_16x16x32_bf16 v[116:119], a[84:87], a[12:15], v[116:119]\n"
"v_mfma_f32_16x16x32_bf16 v[120:123], a[88:91], a[12:15], v[120:123]\n"
"ds_read_b64_tr_b16 a[110:111], v39 offset:4608\n"
"v_mfma_f32_16x16x32_bf16 v[124:127], a[92:95], a[12:15], v[124:127]\n"
"v_mfma_f32_16x16x32_bf16 v[128:131], a[80:83], a[16:19], v[128:131]\n"
"v_mfma_f32_16x16x32_bf16 v[132:135], a[84:87], a[16:19], v[132:135]\n"
"v_mfma_f32_16x16x32_bf16 v[136:139], a[88:91], a[16:19], v[136:139]\n"
"v_mfma_f32_16x16x32_bf16 v[140:143], a[92:95], a[16:19], v[140:143]\n"
"s_waitcnt vmcnt(14) lgkmcnt(0)\n"
"v_mfma_f32_16x16x32_bf16 v[64:67], a[96:99], a[20:23], v[64:67]\n"
"ds_read_b64_tr_b16 a[80:81], v36 offset:8192\n"
"v_mfma_f32_16x16x32_bf16 v[68:71], a[100:103], a[20:23], v[68:71]\n"
"v_mfma_f32_16x16x32_bf16 v[72:75], a[104:107], a[20:23], v[72:75]\n"
"ds_read_b64_tr_b16 a[82:83], v36 offset:8704\n"
"v_mfma_f32_16x16x32_bf16 v[76:79], a[108:111], a[20:23], v[76:79]\n"
"v_mfma_f32_16x16x32_bf16 v[80:83], a[96:99], a[24:27], v[80:83]\n"
"ds_read_b64_tr_b16 a[84:85], v37 offset:8192\n"
"v_mfma_f32_16x16x32_bf16 v[84:87], a[100:103], a[24:27], v[84:87]\n"
"v_mfma_f32_16x16x32_bf16 v[88:91], a[104:107], a[24:27], v[88:91]\n"
"ds_read_b64_tr_b16 a[86:87], v37 offset:8704\n"
"v_mfma_f32_16x16x32_bf16 v[92:95], a[108:111], a[24:27], v[92:95]\n"
"v_mfma_f32_16x16x32_bf16 v[96:99], a[96:99], a[28:31], v[96:99]\n"
"ds_read_b64_tr_b16 a[88:89], v38 offset:8192\n"
"v_mfma_f32_16x16x32_bf16 v[100:103], a[100:103], a[28:31], v[100:103]\n"
"v_mfma_f32_16x16x32_bf16 v[104:107], a[104:107], a[28:31], v[104:107]\n"
"ds_read_b64_tr_b16 a[90:91], v38 offset:8704\n"
"v_mfma_f32_16x16x32_bf16 v[108:111], a[108:111], a[28:31], v[108:111]\n"
"v_mfma_f32_16x16x32_bf16 v[112:115], a[96:99], a[32:35], v[112:115]\n"
"ds_read_b64_tr_b16 a[92:93], v39 offset:8192\n"
"v_mfma_f32_16x16x32_bf16 v[116:119], a[100:103], a[32:35], v[116:119]\n"
"v_mfma_f32_16x16x32_bf16 v[120:123], a[104:107], a[32:35], v[120:123]\n"
"ds_read_b64_tr_b16 a[94:95], v39 offset:8704\n"
"v_mfma_f32_16x16x32_bf16 v[124:127], a[108:111], a[32:35], v[124:127]\n"
"v_mfma_f32_16x16x32_bf16 v[128:131], a[96:99], a[36:39], v[128:131]\n"
"v_mfma_f32_16x16x32_bf16 v[132:135], a[100:103], a[36:39], v[132:135]\n"
"v_mfma_f32_16x16x32_bf16 v[136:139], a[104:107], a[36:39], v[136:139]\n"
"v_mfma_f32_16x16x32_bf16 v[140:143], a[108:111], a[36:39], v[140:143]\n"
"s_waitcnt vmcnt(5) lgkmcnt(0)\n"
"v_mfma_f32_16x16x32_bf16 v[64:67], a[80:83], a[40:43], v[64:67]\n"
"ds_read_b64_tr_b16 a[96:97], v36 offset:12288\n"
"v_mfma_f32_16x16x32_bf16 v[68:71], a[84:87], a[40:43], v[68:71]\n"
"v_mfma_f32_16x16x32_bf16 v[72:75], a[88:91], a[40:43], v[72:75]\n"
"ds_read_b64_tr_b16 a[98:99], v36 offset:12800\n"
"v_mfma_f32_16x16x32_bf16 v[76:79], a[92:95], a[40:43], v[76:79]\n"
"v_mfma_f32_16x16x32_bf16 v[80:83], a[80:83], a[44:47], v[80:83]\n"
"ds_read_b64_tr_b16 a[100:101], v37 offset:12288\n"
"v_mfma_f32_16x16x32_bf16 v[84:87], a[84:87], a[44:47], v[84:87]\n"
"v_mfma_f32_16x16x32_bf16 v[88:91], a[88:91], a[44:47], v[88:91]\n"
"ds_read_b64_tr_b16 a[102:103], v37 offset:12800\n"
"v_mfma_f32_16x16x32_bf16 v[92:95], a[92:95], a[44:47], v[92:95]\n"
"v_mfma_f32_16x16x32_bf16 v[96:99], a[80:83], a[48:51], v[96:99]\n"
"ds_read_b64_tr_b16 a[104:105], v38 offset:12288\n"
"v_mfma_f32_16x16x32_bf16 v[100:103], a[84:87], a[48:51], v[100:103]\n"
"v_mfma_f32_16x16x32_bf16 v[104:107], a[88:91], a[48:51], v[104:107]\n"
"ds_read_b64_tr_b16 a[106:107], v38 offset:12800\n"
"v_mfma_f32_16x16x32_bf16 v[108:111], a[92:95], a[48:51], v[108:111]\n"
"v_mfma_f32_16x16x32_bf16 v[112:115], a[80:83], a[52:55], v[112:115]\n"
"ds_read_b64_tr_b16 a[108:109], v39 offset:12288\n"
"v_mfma_f32_16x16x32_bf16 v[116:119], a[84:87], a[52:55], v[116:119]\n"
"v_mfma_f32_16x16x32_bf16 v[120:123], a[88:91], a[52:55], v[120:123]\n"
"ds_read_b64_tr_b16 a[110:111], v39 offset:12800\n"
"v_mfma_f32_16x16x32_bf16 v[124:127], a[92:95], a[52:55], v[124:127]\n"
"v_mfma_f32_16x16x32_bf16 v[128:131], a[80:83], a[56:59], v[128:131]\n"
"v_mfma_f32_16x16x32_bf16 v[132:135], a[84:87], a[56:59], v[132:135]\n"
"v_mfma_f32_16x16x32_bf16 v[136:139], a[88:91], a[56:59], v[136:139]\n"
"v_mfma_f32_16x16x32_bf16 v[140:143], a[92:95], a[56:59], v[140:143]\n"
"s_waitcnt vmcnt(0) lgkmcnt(0)\n"
"v_mfma_f32_16x16x32_bf16 v[64:67], a[96:99], a[60:63], v[64:67]\n"
"v_mfma_f32_16x16x32_bf16 v[68:71], a[100:103], a[60:63], v[68:71]\n"
"v_mfma_f32_16x16x32_bf16 v[72:75], a[104:107], a[60:63], v[72:75]\n"
"v_mfma_f32_16x16x32_bf16 v[76:79], a[108:111], a[60:63], v[76:79]\n"
"v_mfma_f32_16x16x32_bf16 v[80:83], a[96:99], a[64:67], v[80:83]\n"
"v_mfma_f32_16x16x32_bf16 v[84:87], a[100:103], a[64:67], v[84:87]\n"
"v_mfma_f32_16x16x32_bf16 v[88:91], a[104:107], a[64:67], v[88:91]\n"
"v_mfma_f32_16x16x32_bf16 v[92:95], a[108:111], a[64:67], v[92:95]\n"
"v_mfma_f32_16x16x32_bf16 v[96:99], a[96:99], a[68:71], v[96:99]\n"
"v_mfma_f32_16x16x32_bf16 v[100:103], a[100:103], a[68:71], v[100:103]\n"
"v_mfma_f32_16x16x32_bf16 v[104:107], a[104:107], a[68:71], v[104:107]\n"
"v_mfma_f32_16x16x32_bf16 v[108:111], a[108:111], a[68:71], v[108:111]\n"
"v_mfma_f32_16x16x32_bf16 v[112:115], a[96:99], a[72:75], v[112:115]\n"
"v_mfma_f32_16x16x32_bf16 v[116:119], a[100:103], a[72:75], v[116:119]\n"
"v_mfma_f32_16x16x32_bf16 v[120:123], a[104:107], a[72:75], v[120:123]\n"
"v_mfma_f32_16x16x32_bf16 v[124:127], a[108:111], a[72:75], v[124:127]\n"
"v_mfma_f32_16x16x32_bf16 v[128:131], a[96:99], a[76:79], v[128:131]\n"
"v_mfma_f32_16x16x32_bf16 v[132:135], a[100:103], a[76:79], v[132:135]\n"
"v_mfma_f32_16x16x32_bf16 v[136:139], a[104:107], a[76:79], v[136:139]\n"
"v_mfma_f32_16x16x32_bf16 v[140:143], a[108:111], a[76:79], v[140:143]\n"
"s_nop 7\n"
  : "=&{v[64:79]}"(acc[0]), "=&{v[80:95]}"(acc[1]), "=&{v[96:111]}"(acc[2]), "=&{v[112:127]}"(acc[3]), "=&{v[128:143]}"(acc[4]) : [xb] "s"(xb), [wb] "s"(wb), [lw] "s"(lw), [rot] "s"(rot), [tid] "v"(tid), [fb] "s"(fb), [p0] "v"(p0), [p1] "v"(p1), [tag] "s"(tag) : "v32", "v33", "v34", "v35", "v36", "v37", "v38", "v39", "v40", "v41", "v42", "v43", "v44", "v45", "v46", "v47", "v48", "v49", "v50", "v51", "v52", "v53", "v54", "v55", "v56", "v57", "v58", "v59", "v60", "v61", "v62", "v63", "a0", "a1", "a2", "a3", "a4", "a5", "a6", "a7", "a8", "a9", "a10", "a11", "a12", "a13", "a14", "a15", "a16", "a17", "a18", "a19", "a20", "a21", "a22", "a23", "a24", "a25", "a26", "a27", "a28", "a29", "a30", "a31", "a32", "a33", "a34", "a35", "a36", "a37", "a38", "a39", "a40", "a41", "a42", "a43", "a44", "a45", "a46", "a47", "a48", "a49", "a50", "a51", "a52", "a53", "a54", "a55", "a56", "a57", "a58", "a59", "a60", "a61", "a62", "a63", "a64", "a65", "a66", "a67", "a68", "a69", "a70", "a71", "a72", "a73", "a74", "a75", "a76", "a77", "a78", "a79", "a80", "a81", "a82", "a83", "a84", "a85", "a86", "a87", "a88", "a89", "a90", "a91", "a92", "a93", "a94", "a95", "a96", "a97", "a98", "a99", "a100", "a101", "a102", "a103", "a104", "a105", "a106", "a107", "a108", "a109", "a110", "a111", "s20", "s21", "s22", "s23", "s24", "s25", "s26", "s27", "s28", "s29", "s30", "s31", "s32", "s33", "s34", "s35", "s36", "s37", "s38", "s39", "s40", "s41", "s42", "s43", "s44", "s45", "s46", "s47", "s48", "s49", "s50", "s51", "s52", "s53", "s54", "s55", "s56", "s57", "s58", "s59", "s60", "s61", "s62", "s63", "s64", "s65", "s66", "s67", "s68", "s69", "s70", "s71", "memory", "scc", "vcc");
}
DI void gemm_d(f32x16 (&acc)[12], const void* xb, const void* wb, unsigned lw, unsigned rot, unsigned tid, unsigned& t1, unsigned& t2, const void* fb, unsigned p0, unsigned p1, unsigned tag) {
  asm volatile(
"s_mov_b64 s[22:23], %[wb]\n"
"s_mov_b32 s24, %[lw]\n"
"v_and_b32 v32, 63, %[tid]\n"
"v_lshrrev_b32 v34, 3, v32\n"
"v_mul_u32_u24 v35, 9216, v34\n"
"v_bfe_u32 v33, v32, 4, 1\n"
"v_lshlrev_b32 v33, 1, v33\n"
"v_and_b32 v34, 7, v32\n"
"v_xor_b32 v33, v33, v34\n"
"v_lshlrev_b32 v33, 4, v33\n"
"v_xor_b32 v34, 64, v33\n"
"v_add_u32 v33, v33, v35\n"
"v_add_u32 v34, v34, v35\n"
"v_add_u32 v44, 4096, v33\n"
"v_add_u32 v45, 76800, v34\n"
"v_add_u32 v46, 149504, v33\n"
"v_add_u32 v47, 222208, v34\n"
"v_add_u32 v48, 128, v33\n"
"v_add_u32 v49, 72832, v34\n"
"v_add_u32 v50, 145536, v33\n"
"v_add_u32 v51, 218240, v34\n"
"s_mov_b64 s[20:21], %[xb]\n"
"s_mov_b32 s25, %[rot]\n"
"v_lshlrev_b32 v52, 4, v32\n"
"v_add_u32 v53, 65536, v52\n"
"v_add_u32 v54, 131072, v52\n"
"v_add_u32 v55, 196608, v52\n"
"v_add_u32 v56, 262144, v52\n"
"v_add_u32 v57, 327680, v52\n"
"s_mul_i32 s32, s25, 294912\n"
"s_lshl_b32 s50, s25, 10\n"
"s_add_u32 s25, s25, 1\n"
"s_cmp_eq_u32 s25, 4\n"
"s_cselect_b32 s25, 0, s25\n"
"s_add_u32 m0, s24, 4096\n"
"s_add_u32 s26, s22, s32\n"
"s_addc_u32 s27, s23, 0\n"
"global_load_lds_dwordx4 v44, s[26:27] offset:-4096\n"
"global_load_lds_dwordx4 v45, s[26:27] offset:-3072\n"
"global_load_lds_dwordx4 v46, s[26:27] offset:-2048\n"
"global_load_lds_dwordx4 v47, s[26:27] offset:-1024\n"
"global_load_lds_dwordx4 v48, s[26:27] offset:0\n"
"global_load_lds_dwordx4 v49, s[26:27] offset:1024\n"
"global_load_lds_dwordx4 v50, s[26:27] offset:2048\n"
"global_load_lds_dwordx4 v51, s[26:27] offset:3072\n"
"s_mul_i32 s33, s25, 294912\n"
"s_lshl_b32 s51, s25, 10\n"
"s_add_u32 s25, s25, 1\n"
"s_cmp_eq_u32 s25, 4\n"
"s_cselect_b32 s25, 0, s25\n"
"s_add_u32 m0, s24, 12288\n"
"s_add_u32 s26, s22, s33\n"
"s_addc_u32 s27, s23, 0\n"
"global_load_lds_dwordx4 v44, s[26:27] offset:-4096\n"
"global_load_lds_dwordx4 v45, s[26:27] offset:-3072\n"
"global_load_lds_dwordx4 v46, s[26:27] offset:-2048\n"
"global_load_lds_dwordx4 v47, s[26:27] offset:-1024\n"
"global_load_lds_dwordx4 v48, s[26:27] offset:0\n"
"global_load_lds_dwordx4 v49, s[26:27] offset:1024\n"
"global_load_lds_dwordx4 v50, s[26:27] offset:2048\n"
"global_load_lds_dwordx4 v51, s[26:27] offset:3072\n"
"v_lshrrev_b32 v33, 4, v32\n"
"v_lshlrev_b32 v33, 10, v33\n"
"v_bfe_u32 v34, v32, 2, 2\n"
"v_lshl_add_u32 v33, v34, 7, v33\n"
"v_and_b32 v34, 3, v32\n"
"v_lshl_add_u32 v33, v34, 3, v33\n"
"v_add_u32 v33, s24, v33\n"
"v_bfe_u32 v34, v32, 3, 2\n"
"v_lshrrev_b32 v35, 6, %[tid]\n"
"v_lshlrev_b32 v35, 1, v35\n"
"v_xor_b32 v34, v34, v35\n"
"v_xor_b32 v35, 0, v34\n"
"v_and_b32 v58, 3, v35\n"
"v_lshl_add_u32 v58, v58, 5, v33\n"
"v_lshrrev_b32 v35, 2, v35\n"
"v_lshl_add_u32 v36, v35, 12, v58\n"
"v_xor_b32 v35, 1, v34\n"
"v_and_b32 v58, 3, v35\n"
"v_lshl_add_u32 v58, v58, 5, v33\n"
"v_lshrrev_b32 v35, 2, v35\n"
"v_lshl_add_u32 v37, v35, 12, v58\n"
"v_xor_b32 v35, 2, v34\n"
"v_and_b32 v58, 3, v35\n"
"v_lshl_add_u32 v58, v58, 5, v33\n"
"v_lshrrev_b32 v35, 2, v35\n"
"v_lshl_add_u32 v38, v35, 12, v58\n"
"v_xor_b32 v35, 3, v34\n"
"v_and_b32 v58, 3, v35\n"
"v_lshl_add_u32 v58, v58, 5, v33\n"
"v_lshrrev_b32 v35, 2, v35\n"
"v_lshl_add_u32 v39, v35, 12, v58\n"
"v_xor_b32 v35, 4, v34\n"
"v_and_b32 v58, 3, v35\n"
"v_lshl_add_u32 v58, v58, 5, v33\n"
"v_lshrrev_b32 v35, 2, v35\n"
"v_lshl_add_u32 v40, v35, 12, v58\n"
"v_xor_b32 v35, 5, v34\n"
"v_and_b32 v58, 3, v35\n"
"v_lshl_add_u32 v58, v58, 5, v33\n"
"v_lshrrev_b32 v35, 2, v35\n"
"v_lshl_add_u32 v41, v35, 12, v58\n"
"v_xor_b32 v35, 6, v34\n"
"v_and_b32 v58, 3, v35\n"
"v_lshl_add_u32 v58, v58, 5, v33\n"
"v_lshrrev_b32 v35, 2, v35\n"
"v_lshl_add_u32 v42, v35, 12, v58\n"
"v_xor_b32 v35, 7, v34\n"
"v_and_b32 v58, 3, v35\n"
"v_lshl_add_u32 v58, v58, 5, v33\n"
"v_lshrrev_b32 v35, 2, v35\n"
"v_lshl_add_u32 v43, v35, 12, v58\n"
"global_load_dword v60, %[p0], %[fb] sc1\n"
"v_mov_b32 v64, 0\n"
"v_mov_b32 v65, 0\n"
"v_mov_b32 v66, 0\n"
"v_mov_b32 v67, 0\n"
"v_mov_b32 v68, 0\n"
"v_mov_b32 v69, 0\n"
"v_mov_b32 v70, 0\n"
"v_mov_b32 v71, 0\n"
"v_mov_b32 v72, 0\n"
"v_mov_b32 v73, 0\n"
"v_mov_b32 v74, 0\n"
"v_mov_b32 v75, 0\n"
"v_mov_b32 v76, 0\n"
"v_mov_b32 v77, 0\n"
"v_mov_b32 v78, 0\n"
"v_mov_b32 v79, 0\n"
"v_mov_b32 v80, 0\n"
"v_mov_b32 v81, 0\n"
"v_mov_b32 v82, 0\n"
"v_mov_b32 v83, 0\n"
"v_mov_b32 v84, 0\n"
"v_mov_b32 v85, 0\n"
"v_mov_b32 v86, 0\n"
"v_mov_b32 v87, 0\n"
"v_mov_b32 v88, 0\n"
"v_mov_b32 v89, 0\n"
"v_mov_b32 v90, 0\n"
"v_mov_b32 v91, 0\n"
"v_mov_b32 v92, 0\n"
"v_mov_b32 v93, 0\n"
"v_mov_b32 v94, 0\n"
"v_mov_b32 v95, 0\n"
"v_mov_b32 v96, 0\n"
"v_mov_b32 v97, 0\n"
"v_mov_b32 v98, 0\n"
"v_mov_b32 v99, 0\n"
"v_mov_b32 v100, 0\n"
"v_mov_b32 v101, 0\n"
"v_mov_b32 v102, 0\n"
"v_mov_b32 v103, 0\n"
"v_mov_b32 v104, 0\n"
"v_mov_b32 v105, 0\n"
"v_mov_b32 v106, 0\n"
"v_mov_b32 v107, 0\n"
"v_mov_b32 v108, 0\n"
"v_mov_b32 v109, 0\n"
"v_mov_b32 v110, 0\n"
"v_mov_b32 v111, 0\n"
"v_mov_b32 v112, 0\n"
"v_mov_b32 v113, 0\n"
"v_mov_b32 v114, 0\n"
"v_mov_b32 v115, 0\n"
"v_mov_b32 v116, 0\n"
"v_mov_b32 v117, 0\n"
"v_mov_b32 v118, 0\n"
"v_mov_b32 v119, 0\n"
"v_mov_b32 v120, 0\n"
"v_mov_b32 v121, 0\n"
"v_mov_b32 v122, 0\n"
"v_mov_b32 v123, 0\n"
"v_mov_b32 v124, 0\n"
"v_mov_b32 v125, 0\n"
"v_mov_b32 v126, 0\n"
"v_mov_b32 v127, 0\n"
"v_mov_b32 v128, 0\n"
"v_mov_b32 v129, 0\n"
"v_mov_b32 v130, 0\n"
"v_mov_b32 v131, 0\n"
"v_mov_b32 v132, 0\n"
"v_mov_b32 v133, 0\n"
"v_mov_b32 v134, 0\n"
"v_mov_b32 v135, 0\n"
"v_mov_b32 v136, 0\n"
"v_mov_b32 v137, 0\n"
"v_mov_b32 v138, 0\n"
"v_mov_b32 v139, 0\n"
"v_mov_b32 v140, 0\n"
"v_mov_b32 v141, 0\n"
"v_mov_b32 v142, 0\n"
"v_mov_b32 v143, 0\n"
"v_mov_b32 v144, 0\n"
"v_mov_b32 v145, 0\n"
"v_mov_b32 v146, 0\n"
"v_mov_b32 v147, 0\n"
"v_mov_b32 v148, 0\n"
"v_mov_b32 v149, 0\n"
"v_mov_b32 v150, 0\n"
"v_mov_b32 v151, 0\n"
"v_mov_b32 v152, 0\n"
"v_mov_b32 v153, 0\n"
"v_mov_b32 v154, 0\n"
"v_mov_b32 v155, 0\n"
"v_mov_b32 v156, 0\n"
"v_mov_b32 v157, 0\n"
"v_mov_b32 v158, 0\n"
"v_mov_b32 v159, 0\n"
"v_mov_b32 v160, 0\n"
"v_mov_b32 v161, 0\n"
"v_mov_b32 v162, 0\n"
"v_mov_b32 v163, 0\n"
"v_mov_b32 v164, 0\n"
"v_mov_b32 v165, 0\n"
"v_mov_b32 v166, 0\n"
"v_mov_b32 v167, 0\n"
"v_mov_b32 v168, 0\n"
"v_mov_b32 v169, 0\n"
"v_mov_b32 v170, 0\n"
"v_mov_b32 v171, 0\n"
"v_mov_b32 v172, 0\n"
"v_mov_b32 v173, 0\n"
"v_mov_b32 v174, 0\n"
"v_mov_b32 v175, 0\n"
"v_mov_b32 v176, 0\n"
"v_mov_b32 v177, 0\n"
"v_mov_b32 v178, 0\n"
"v_mov_b32 v179, 0\n"
"v_mov_b32 v180, 0\n"
"v_mov_b32 v181, 0\n"
"v_mov_b32 v182, 0\n"
"v_mov_b32 v183, 0\n"
"v_mov_b32 v184, 0\n"
"v_mov_b32 v185, 0\n"
"v_mov_b32 v186, 0\n"
"v_mov_b32 v187, 0\n"
"v_mov_b32 v188, 0\n"
"v_mov_b32 v189, 0\n"
"v_mov_b32 v190, 0\n"
"v_mov_b32 v191, 0\n"
"v_mov_b32 v192, 0\n"
"v_mov_b32 v193, 0\n"
"v_mov_b32 v194, 0\n"
"v_mov_b32 v195, 0\n"
"v_mov_b32 v196, 0\n"
"v_mov_b32 v197, 0\n"
"v_mov_b32 v198, 0\n"
"v_mov_b32 v199, 0\n"
"v_mov_b32 v200, 0\n"
"v_mov_b32 v201, 0\n"
"v_mov_b32 v202, 0\n"
"v_mov_b32 v203, 0\n"
"v_mov_b32 v204, 0\n"
"v_mov_b32 v205, 0\n"
"v_mov_b32 v206, 0\n"
"v_mov_b32 v207, 0\n"
"v_mov_b32 v208, 0\n"
"v_mov_b32 v209, 0\n"
"v_mov_b32 v210, 0\n"
"v_mov_b32 v211, 0\n"
"v_mov_b32 v212, 0\n"
"v_mov_b32 v213, 0\n"
"v_mov_b32 v214, 0\n"
"v_mov_b32 v215, 0\n"
"v_mov_b32 v216, 0\n"
"v_mov_b32 v217, 0\n"
"v_mov_b32 v218, 0\n"
"v_mov_b32 v219, 0\n"
"v_mov_b32 v220, 0\n"
"v_mov_b32 v221, 0\n"
"v_mov_b32 v222, 0\n"
"v_mov_b32 v223, 0\n"
"v_mov_b32 v224, 0\n"
"v_mov_b32 v225, 0\n"
"v_mov_b32 v226, 0\n"
"v_mov_b32 v227, 0\n"
"v_mov_b32 v228, 0\n"
"v_mov_b32 v229, 0\n"
"v_mov_b32 v230, 0\n"
"v_mov_b32 v231, 0\n"
"v_mov_b32 v232, 0\n"
"v_mov_b32 v233, 0\n"
"v_mov_b32 v234, 0\n"
"v_mov_b32 v235, 0\n"
"v_mov_b32 v236, 0\n"
"v_mov_b32 v237, 0\n"
"v_mov_b32 v238, 0\n"
"v_mov_b32 v239, 0\n"
"v_mov_b32 v240, 0\n"
"v_mov_b32 v241, 0\n"
"v_mov_b32 v242, 0\n"
"v_mov_b32 v243, 0\n"
"v_mov_b32 v244, 0\n"
"v_mov_b32 v245, 0\n"
"v_mov_b32 v246, 0\n"
"v_mov_b32 v247, 0\n"
"v_mov_b32 v248, 0\n"
"v_mov_b32 v249, 0\n"
"v_mov_b32 v250, 0\n"
"v_mov_b32 v251, 0\n"
"v_mov_b32 v252, 0\n"
"v_mov_b32 v253, 0\n"
"v_mov_b32 v254, 0\n"
"v_mov_b32 v255, 0\n"
"s_mov_b32 s30, 0x40000\n"
"5:\n"
"s_waitcnt vmcnt(0)\n"
"v_xor_b32 v60, %[tag], v60\n"
"v_cmp_ne_u32 vcc, 0, v60\n"
"s_cbranch_vccz 6f\n"
"s_sub_u32 s30, s30, 1\n"
"s_cmp_eq_u32 s30, 0\n"
"s_cbranch_scc1 6f\n"
"s_sleep 1\n"
"global_load_dword v60, %[p0], %[fb] sc1\n"
"s_branch 5b\n"
"6:\n"
"s_add_u32 s28, s20, s50\n"
"s_addc_u32 s29, s21, 0\n"
"global_load_dwordx4 a[0:3], v52, s[28:29] sc1\n"
"global_load_dwordx4 a[4:7], v53, s[28:29] sc1\n"
"global_load_dwordx4 a[8:11], v54, s[28:29] sc1\n"
"global_load_dwordx4 a[12:15], v55, s[28:29] sc1\n"
"global_load_dwordx4 a[16:19], v56, s[28:29] sc1\n"
"global_load_dwordx4 a[20:23], v57, s[28:29] sc1\n"
"s_add_u32 s28, s20, s51\n"
"s_addc_u32 s29, s21, 0\n"
"global_load_dwordx4 a[24:27], v52, s[28:29] sc1\n"
"global_load_dwordx4 a[28:31], v53, s[28:29] sc1\n"
"global_load_dwordx4 a[32:35], v54, s[28:29] sc1\n"
"global_load_dwordx4 a[36:39], v55, s[28:29] sc1\n"
"global_load_dwordx4 a[40:43], v56, s[28:29] sc1\n"
"global_load_dwordx4 a[44:47], v57, s[28:29] sc1\n"
"s_mul_i32 s34, s25, 294912\n"
"s_lshl_b32 s52, s25, 10\n"
"s_add_u32 s25, s25, 1\n"
"s_cmp_eq_u32 s25, 4\n"
"s_cselect_b32 s25, 0, s25\n"
"s_add_u32 m0, s24, 20480\n"
"s_add_u32 s26, s22, s34\n"
"s_addc_u32 s27, s23, 0\n"
"global_load_lds_dwordx4 v44, s[26:27] offset:-4096\n"
"s_add_u32 s28, s20, s52\n"
"s_addc_u32 s29, s21, 0\n"
"global_load_lds_dwordx4 v45, s[26:27] offset:-3072\n"
"global_load_lds_dwordx4 v46, s[26:27] offset:-2048\n"
"global_load_lds_dwordx4 v47, s[26:27] offset:-1024\n"
"global_load_lds_dwordx4 v48, s[26:27] offset:0\n"
"global_load_lds_dwordx4 v49, s[26:27] offset:1024\n"
"global_load_lds_dwordx4 v50, s[26:27] offset:2048\n"
"global_load_lds_dwordx4 v51, s[26:27] offset:3072\n"
"global_load_dwordx4 a[48:51], v52, s[28:29] sc1\n"
"global_load_dwordx4 a[52:55], v53, s[28:29] sc1\n"
"global_load_dwordx4 a[56:59], v54, s[28:29] sc1\n"
"global_load_dwordx4 a[60:63], v55, s[28:29] sc1\n"
"global_load_dwordx4 a[64:67], v56, s[28:29] sc1\n"
"global_load_dwordx4 a[68:71], v57, s[28:29] sc1\n"
"s_mul_i32 s35, s25, 294912\n"
"s_lshl_b32 s53, s25, 10\n"
"s_add_u32 s25, s25, 1\n"
"s_cmp_eq_u32 s25, 4\n"
"s_cselect_b32 s25, 0, s25\n"
"s_add_u32 m0, s24, 28672\n"
"s_add_u32 s26, s22, s35\n"
"s_addc_u32 s27, s23, 0\n"
"global_load_lds_dwordx4 v44, s[26:27] offset:-4096\n"
"s_add_u32 s28, s20, s53\n"
"s_addc_u32 s29, s21, 0\n"
"global_load_lds_dwordx4 v45, s[26:27] offset:-3072\n"
"global_load_lds_dwordx4 v46, s[26:27] offset:-2048\n"
"global_load_lds_dwordx4 v47, s[26:27] offset:-1024\n"
"global_load_lds_dwordx4 v48, s[26:27] offset:0\n"
"global_load_lds_dwordx4 v49, s[26:27] offset:1024\n"
"global_load_lds_dwordx4 v50, s[26:27] offset:2048\n"
"global_load_lds_dwordx4 v51, s[26:27] offset:3072\n"
"global_load_dwordx4 a[72:75], v52, s[28:29] sc1\n"
"global_load_dwordx4 a[76:79], v53, s[28:29] sc1\n"
"global_load_dwordx4 a[80:83], v54, s[28:29] sc1\n"
"global_load_dwordx4 a[84:87], v55, s[28:29] sc1\n"
"global_load_dwordx4 a[88:91], v56, s[28:29] sc1\n"
"global_load_dwordx4 a[92:95], v57, s[28:29] sc1\n"
"ds_read_b64_tr_b16 a[96:97], v36 offset:0\n"
"ds_read_b64_tr_b16 a[98:99], v36 offset:512\n"
"ds_read_b64_tr_b16 a[100:101], v37 offset:0\n"
"ds_read_b64_tr_b16 a[102:103], v37 offset:512\n"
"ds_read_b64_tr_b16 a[104:105], v38 offset:0\n"
"ds_read_b64_tr_b16 a[106:107], v38 offset:512\n"
"ds_read_b64_tr_b16 a[108:109], v39 offset:0\n"
"ds_read_b64_tr_b16 a[110:111], v39 offset:512\n"
"ds_read_b64_tr_b16 a[112:113], v40 offset:0\n"
"ds_read_b64_tr_b16 a[114:115], v40 offset:512\n"
"ds_read_b64_tr_b16 a[116:117], v41 offset:0\n"
"ds_read_b64_tr_b16 a[118:119], v41 offset:512\n"
"ds_read_b64_tr_b16 a[120:121], v42 offset:0\n"
"ds_read_b64_tr_b16 a[122:123], v42 offset:512\n"
"ds_read_b64_tr_b16 a[124:125], v43 offset:0\n"
"ds_read_b64_tr_b16 a[126:127], v43 offset:512\n"
"s_mov_b32 s25, 3\n"
".p2align 4\n"
"1:\n"
"s_waitcnt vmcnt(34) lgkmcnt(0)\n"
"s_add_u32 m0, s24, 4096\n"
"s_add_u32 s22, s22, 1179648\n"
"s_addc_u32 s23, s23, 0\n"
"s_add_u32 s20, s20, 4096\n"
"s_addc_u32 s21, s21, 0\n"
"s_add_u32 s26, s22, s32\n"
"s_addc_u32 s27, s23, 0\n"
"v_mfma_f32_16x16x32_bf16 v[64:67], a[96:99], a[0:3], v[64:67]\n"
"global_load_lds_dwordx4 v44, s[26:27] offset:-4096\n"
"s_add_u32 s28, s20, s50\n"
"s_addc_u32 s29, s21, 0\n"
"ds_read_b64_tr_b16 a[128:129], v36 offset:8192\n"
"v_mfma_f32_16x16x32_bf16 v[68:71], a[100:103], a[0:3], v[68:71]\n"
"global_load_lds_dwordx4 v45, s[26:27] offset:-3072\n"
"v_mfma_f32_16x16x32_bf16 v[72:75], a[104:107], a[0:3], v[72:75]\n"
"global_load_lds_dwordx4 v46, s[26:27] offset:-2048\n"
"ds_read_b64_tr_b16 a[130:131], v36 offset:8704\n"
"v_mfma_f32_16x16x32_bf16 v[76:79], a[108:111], a[0:3], v[76:79]\n"
"global_load_lds_dwordx4 v47, s[26:27] offset:-1024\n"
"v_mfma_f32_16x16x32_bf16 v[80:83], a[112:115], a[0:3], v[80:83]\n"
"global_load_lds_dwordx4 v48, s[26:27] offset:0\n"
"v_mfma_f32_16x16x32_bf16 v[84:87], a[116:119], a[0:3], v[84:87]\n"
"global_load_lds_dwordx4 v49, s[26:27] offset:1024\n"
"ds_read_b64_tr_b16 a[132:133], v37 offset:8192\n"
"v_mfma_f32_16x16x32_bf16 v[88:91], a[120:123], a[0:3], v[88:91]\n"
"global_load_lds_dwordx4 v50, s[26:27] offset:2048\n"
"v_mfma_f32_16x16x32_bf16 v[92:95], a[124:127], a[0:3], v[92:95]\n"
"global_load_lds_dwordx4 v51, s[26:27] offset:3072\n"
"global_load_dwordx4 a[0:3], v52, s[28:29] sc1\n"
"v_mfma_f32_16x16x32_bf16 v[96:99], a[96:99], a[4:7], v[96:99]\n"
"ds_read_b64_tr_b16 a[134:135], v37 offset:8704\n"
"v_mfma_f32_16x16x32_bf16 v[100:103], a[100:103], a[4:7], v[100:103]\n"
"v_mfma_f32_16x16x32_bf16 v[104:107], a[104:107], a[4:7], v[104:107]\n"
"v_mfma_f32_16x16x32_bf16 v[108:111], a[108:111], a[4:7], v[108:111]\n"
"ds_read_b64_tr_b16 a[136:137], v38 offset:8192\n"
"v_mfma_f32_16x16x32_bf16 v[112:115], a[112:115], a[4:7], v[112:115]\n"
"v_mfma_f32_16x16x32_bf16 v[116:119], a[116:119], a[4:7], v[116:119]\n"
"ds_read_b64_tr_b16 a[138:139], v38 offset:8704\n"
"v_mfma_f32_16x16x32_bf16 v[120:123], a[120:123], a[4:7], v[120:123]\n"
"v_mfma_f32_16x16x32_bf16 v[124:127], a[124:127], a[4:7], v[124:127]\n"
"global_load_dwordx4 a[4:7], v53, s[28:29] sc1\n"
"v_mfma_f32_16x16x32_bf16 v[128:131], a[96:99], a[8:11], v[128:131]\n"
"ds_read_b64_tr_b16 a[140:141], v39 offset:8192\n"
"v_mfma_f32_16x16x32_bf16 v[132:135], a[100:103], a[8:11], v[132:135]\n"
"v_mfma_f32_16x16x32_bf16 v[136:139], a[104:107], a[8:11], v[136:139]\n"
"v_mfma_f32_16x16x32_bf16 v[140:143], a[108:111], a[8:11], v[140:143]\n"
"ds_read_b64_tr_b16 a[142:143], v39 offset:8704\n"
"v_mfma_f32_16x16x32_bf16 v[144:147], a[112:115], a[8:11], v[144:147]\n"
"v_mfma_f32_16x16x32_bf16 v[148:151], a[116:119], a[8:11], v[148:151]\n"
"v_mfma_f32_16x16x32_bf16 v[152:155], a[120:123], a[8:11], v[152:155]\n"
"ds_read_b64_tr_b16 a[144:145], v40 offset:8192\n"
"v_mfma_f32_16x16x32_bf16 v[156:159], a[124:127], a[8:11], v[156:159]\n"
"global_load_dwordx4 a[8:11], v54, s[28:29] sc1\n"
"v_mfma_f32_16x16x32_bf16 v[160:163], a[96:99], a[12:15], v[160:163]\n"
"ds_read_b64_tr_b16 a[146:147], v40 offset:8704\n"
"v_mfma_f32_16x16x32_bf16 v[164:167], a[100:103], a[12:15], v[164:167]\n"
"v_mfma_f32_16x16x32_bf16 v[168:171], a[104:107], a[12:15], v[168:171]\n"
"v_mfma_f32_16x16x32_bf16 v[172:175], a[108:111], a[12:15], v[172:175]\n"
"ds_read_b64_tr_b16 a[148:149], v41 offset:8192\n"
"v_mfma_f32_16x16x32_bf16 v[176:179], a[112:115], a[12:15], v[176:179]\n"
"v_mfma_f32_16x16x32_bf16 v[180:183], a[116:119], a[12:15], v[180:183]\n"
"v_mfma_f32_16x16x32_bf16 v[184:187], a[120:123], a[12:15], v[184:187]\n"
"ds_read_b64_tr_b16 a[150:151], v41 offset:8704\n"
"v_mfma_f32_16x16x32_bf16 v[188:191], a[124:127], a[12:15], v[188:191]\n"
"global_load_dwordx4 a[12:15], v55, s[28:29] sc1\n"
"v_mfma_f32_16x16x32_bf16 v[192:195], a[96:99], a[16:19], v[192:195]\n"
"v_mfma_f32_16x16x32_bf16 v[196:199], a[100:103], a[16:19], v[196:199]\n"
"ds_read_b64_tr_b16 a[152:153], v42 offset:8192\n"
"v_mfma_f32_16x16x32_bf16 v[200:203], a[104:107], a[16:19], v[200:203]\n"
"v_mfma_f32_16x16x32_bf16 v[204:207], a[108:111], a[16:19], v[204:207]\n"
"ds_read_b64_tr_b16 a[154:155], v42 offset:8704\n"
"v_mfma_f32_16x16x32_bf16 v[208:211], a[112:115], a[16:19], v[208:211]\n"
"v_mfma_f32_16x16x32_bf16 v[212:215], a[116:119], a[16:19], v[212:215]\n"
"v_mfma_f32_16x16x32_bf16 v[216:219], a[120:123], a[16:19], v[216:219]\n"
"ds_read_b64_tr_b16 a[156:157], v43 offset:8192\n"
"v_mfma_f32_16x16x32_bf16 v[220:223], a[124:127], a[16:19], v[220:223]\n"
"global_load_dwordx4 a[16:19], v56, s[28:29] sc1\n"
"v_mfma_f32_16x16x32_bf16 v[224:227], a[96:99], a[20:23], v[224:227]\n"
"v_mfma_f32_16x16x32_bf16 v[228:231], a[100:103], a[20:23], v[228:231]\n"
"ds_read_b64_tr_b16 a[158:159], v43 offset:8704\n"
"v_mfma_f32_16x16x32_bf16 v[232:235], a[104:107], a[20:23], v[232:235]\n"
"v_mfma_f32_16x16x32_bf16 v[236:239], a[108:111], a[20:23], v[236:239]\n"
"v_mfma_f32_16x16x32_bf16 v[240:243], a[112:115], a[20:23], v[240:243]\n"
"v_mfma_f32_16x16x32_bf16 v[244:247], a[116:119], a[20:23], v[244:247]\n"
"v_mfma_f32_16x16x32_bf16 v[248:251], a[120:123], a[20:23], v[248:251]\n"
"v_mfma_f32_16x16x32_bf16 v[252:255], a[124:127], a[20:23], v[252:255]\n"
"global_load_dwordx4 a[20:23], v57, s[28:29] sc1\n"
"s_waitcnt vmcnt(34) lgkmcnt(0)\n"
"s_add_u32 m0, s24, 12288\n"
"s_add_u32 s26, s22, s33\n"
"s_addc_u32 s27, s23, 0\n"
"v_mfma_f32_16x16x32_bf16 v[64:67], a[128:131], a[24:27], v[64:67]\n"
"global_load_lds_dwordx4 v44, s[26:27] offset:-4096\n"
"s_add_u32 s28, s20, s51\n"
"s_addc_u32 s29, s21, 0\n"
"ds_read_b64_tr_b16 a[96:97], v36 offset:16384\n"
"v_mfma_f32_16x16x32_bf16 v[68:71], a[132:135], a[24:27], v[68:71]\n"
"global_load_lds_dwordx4 v45, s[26:27] offset:-3072\n"
"v_mfma_f32_16x16x32_bf16 v[72:75], a[136:139], a[24:27], v[72:75]\n"
"global_load_lds_dwordx4 v46, s[26:27] offset:-2048\n"
"ds_read_b64_tr_b16 a[98:99], v36 offset:16896\n"
"v_mfma_f32_16x16x32_bf16 v[76:79], a[140:143], a[24:27], v[76:79]\n"
"global_load_lds_dwordx4 v47, s[26:27] offset:-1024\n"
"v_mfma_f32_16x16x32_bf16 v[80:83], a[144:147], a[24:27], v[80:83]\n"
"global_load_lds_dwordx4 v48, s[26:27] offset:0\n"
"v_mfma_f32_16x16x32_bf16 v[84:87], a[148:151], a[24:27], v[84:87]\n"
"global_load_lds_dwordx4 v49, s[26:27] offset:1024\n"
"ds_read_b64_tr_b16 a[100:101], v37 offset:16384\n"
"v_mfma_f32_16x16x32_bf16 v[88:91], a[152:155], a[24:27], v[88:91]\n"
"global_load_lds_dwordx4 v50, s[26:27] offset:2048\n"
"v_mfma_f32_16x16x32_bf16 v[92:95], a[156:159], a[24:27], v[92:95]\n"
"global_load_lds_dwordx4 v51, s[26:27] offset:3072\n"
"global_load_dwordx4 a[24:27], v52, s[28:29] sc1\n"
"v_mfma_f32_16x16x32_bf16 v[96:99], a[128:131], a[28:31], v[96:99]\n"
"ds_read_b64_tr_b16 a[102:103], v37 offset:16896\n"
"v_mfma_f32_16x16x32_bf16 v[100:103], a[132:135], a[28:31], v[100:103]\n"
"v_mfma_f32_16x16x32_bf16 v[104:107], a[136:139], a[28:31], v[104:107]\n"
"v_mfma_f32_16x16x32_bf16 v[108:111], a[140:143], a[28:31], v[108:111]\n"
"ds_read_b64_tr_b16 a[104:105], v38 offset:16384\n"
"v_mfma_f32_16x16x32_bf16 v[112:115], a[144:147], a[28:31], v[112:115]\n"
"v_mfma_f32_16x16x32_bf16 v[116:119], a[148:151], a[28:31], v[116:119]\n"
"ds_read_b64_tr_b16 a[106:107], v38 offset:16896\n"
"v_mfma_f32_16x16x32_bf16 v[120:123], a[152:155], a[28:31], v[120:123]\n"
"v_mfma_f32_16x16x32_bf16 v[124:127], a[156:159], a[28:31], v[124:127]\n"
"global_load_dwordx4 a[28:31], v53, s[28:29] sc1\n"
"v_mfma_f32_16x16x32_bf16 v[128:131], a[128:131], a[32:35], v[128:131]\n"
"ds_read_b64_tr_b16 a[108:109], v39 offset:16384\n"
"v_mfma_f32_16x16x32_bf16 v[132:135], a[132:135], a[32:35], v[132:135]\n"
"v_mfma_f32_16x16x32_bf16 v[136:139], a[136:139], a[32:35], v[136:139]\n"
"v_mfma_f32_16x16x32_bf16 v[140:143], a[140:143], a[32:35], v[140:143]\n"
"ds_read_b64_tr_b16 a[110:111], v39 offset:16896\n"
"v_mfma_f32_16x16x32_bf16 v[144:147], a[144:147], a[32:35], v[144:147]\n"
"v_mfma_f32_16x16x32_bf16 v[148:151], a[148:151], a[32:35], v[148:151]\n"
"v_mfma_f32_16x16x32_bf16 v[152:155], a[152:155], a[32:35], v[152:155]\n"
"ds_read_b64_tr_b16 a[112:113], v40 offset:16384\n"
"v_mfma_f32_16x16x32_bf16 v[156:159], a[156:159], a[32:35], v[156:159]\n"
"global_load_dwordx4 a[32:35], v54, s[28:29] sc1\n"
"v_mfma_f32_16x16x32_bf16 v[160:163], a[128:131], a[36:39], v[160:163]\n"
"ds_read_b64_tr_b16 a[114:115], v40 offset:16896\n"
"v_mfma_f32_16x16x32_bf16 v[164:167], a[132:135], a[36:39], v[164:167]\n"
"v_mfma_f32_16x16x32_bf16 v[168:171], a[136:139], a[36:39], v[168:171]\n"
"v_mfma_f32_16x16x32_bf16 v[172:175], a[140:143], a[36:39], v[172:175]\n"
"ds_read_b64_tr_b16 a[116:117], v41 offset:16384\n"
"v_mfma_f32_16x16x32_bf16 v[176:179], a[144:147], a[36:39], v[176:179]\n"
"v_mfma_f32_16x16x32_bf16 v[180:183], a[148:151], a[36:39], v[180:183]\n"
"v_mfma_f32_16x16x32_bf16 v[184:187], a[152:155], a[36:39], v[184:187]\n"
"ds_read_b64_tr_b16 a[118:119], v41 offset:16896\n"
"v_mfma_f32_16x16x32_bf16 v[188:191], a[156:159], a[36:39], v[188:191]\n"
"global_load_dwordx4 a[36:39], v55, s[28:29] sc1\n"
"v_mfma_f32_16x16x32_bf16 v[192:195], a[128:131], a[40:43], v[192:195]\n"
"v_mfma_f32_16x16x32_bf16 v[196:199], a[132:135], a[40:43], v[196:199]\n"
"ds_read_b64_tr_b16 a[120:121], v42 offset:16384\n"
"v_mfma_f32_16x16x32_bf16 v[200:203], a[136:139], a[40:43], v[200:203]\n"
"v_mfma_f32_16x16x32_bf16 v[204:207], a[140:143], a[40:43], v[204:207]\n"
"ds_read_b64_tr_b16 a[122:123], v42 offset:16896\n"
"v_mfma_f32_16x16x32_bf16 v[208:211], a[144:147], a[40:43], v[208:211]\n"
"v_mfma_f32_16x16x32_bf16 v[212:215], a[148:151], a[40:43], v[212:215]\n"
"v_mfma_f32_16x16x32_bf16 v[216:219], a[152:155], a[40:43], v[216:219]\n"
"ds_read_b64_tr_b16 a[124:125], v43 offset:16384\n"
"v_mfma_f32_16x16x32_bf16 v[220:223], a[156:159], a[40:43], v[220:223]\n"
"global_load_dwordx4 a[40:43], v56, s[28:29] sc1\n"
"v_mfma_f32_16x16x32_bf16 v[224:227], a[128:131], a[44:47], v[224:227]\n"
"v_mfma_f32_16x16x32_bf16 v[228:231], a[132:135], a[44:47], v[228:231]\n"
"ds_read_b64_tr_b16 a[126:127], v43 offset:16896\n"
"v_mfma_f32_16x16x32_bf16 v[232:235], a[136:139], a[44:47], v[232:235]\n"
"v_mfma_f32_16x16x32_bf16 v[236:239], a[140:143], a[44:47], v[236:239]\n"
"v_mfma_f32_16x16x32_bf16 v[240:243], a[144:147], a[44:47], v[240:243]\n"
"v_mfma_f32_16x16x32_bf16 v[244:247], a[148:151], a[44:47], v[244:247]\n"
"v_mfma_f32_16x16x32_bf16 v[248:251], a[152:155], a[44:47], v[248:251]\n"
"v_mfma_f32_16x16x32_bf16 v[252:255], a[156:159], a[44:47], v[252:255]\n"
"global_load_dwordx4 a[44:47], v57, s[28:29] sc1\n"
"s_waitcnt vmcnt(34) lgkmcnt(0)\n"
"s_add_u32 m0, s24, 20480\n"
"s_add_u32 s26, s22, s34\n"
"s_addc_u32 s27, s23, 0\n"
"v_mfma_f32_16x16x32_bf16 v[64:67], a[96:99], a[48:51], v[64:67]\n"
"global_load_lds_dwordx4 v44, s[26:27] offset:-4096\n"
"s_add_u32 s28, s20, s52\n"
"s_addc_u32 s29, s21, 0\n"
"ds_read_b64_tr_b16 a[128:129], v36 offset:24576\n"
"v_mfma_f32_16x16x32_bf16 v[68:71], a[100:103], a[48:51], v[68:71]\n"
"global_load_lds_dwordx4 v45, s[26:27] offset:-3072\n"
"v_mfma_f32_16x16x32_bf16 v[72:75], a[104:107], a[48:51], v[72:75]\n"
"global_load_lds_dwordx4 v46, s[26:27] offset:-2048\n"
"ds_read_b64_tr_b16 a[130:131], v36 offset:25088\n"
"v_mfma_f32_16x16x32_bf16 v[76:79], a[108:111], a[48:51], v[76:79]\n"
"global_load_lds_dwordx4 v47, s[26:27] offset:-1024\n"
"v_mfma_f32_16x16x32_bf16 v[80:83], a[112:115], a[48:51], v[80:83]\n"
"global_load_lds_dwordx4 v48, s[26:27] offset:0\n"
"v_mfma_f32_16x16x32_bf16 v[84:87], a[116:119], a[48:51], v[84:87]\n"
"global_load_lds_dwordx4 v49, s[26:27] offset:1024\n"
"ds_read_b64_tr_b16 a[132:133], v37 offset:24576\n"
"v_mfma_f32_16x16x32_bf16 v[88:91], a[120:123], a[48:51], v[88:91]\n"
"global_load_lds_dwordx4 v50, s[26:27] offset:2048\n"
"v_mfma_f32_16x16x32_bf16 v[92:95], a[124:127], a[48:51], v[92:95]\n"
"global_load_lds_dwordx4 v51, s[26:27] offset:3072\n"
"global_load_dwordx4 a[48:51], v52, s[28:29] sc1\n"
"v_mfma_f32_16x16x32_bf16 v[96:99], a[96:99], a[52:55], v[96:99]\n"
"ds_read_b64_tr_b16 a[134:135], v37 offset:25088\n"
"v_mfma_f32_16x16x32_bf16 v[100:103], a[100:103], a[52:55], v[100:103]\n"
"v_mfma_f32_16x16x32_bf16 v[104:107], a[104:107], a[52:55], v[104:107]\n"
"v_mfma_f32_16x16x32_bf16 v[108:111], a[108:111], a[52:55], v[108:111]\n"
"ds_read_b64_tr_b16 a[136:137], v38 offset:24576\n"
"v_mfma_f32_16x16x32_bf16 v[112:115], a[112:115], a[52:55], v[112:115]\n"
"v_mfma_f32_16x16x32_bf16 v[116:119], a[116:119], a[52:55], v[116:119]\n"
"ds_read_b64_tr_b16 a[138:139], v38 offset:25088\n"
"v_mfma_f32_16x16x32_bf16 v[120:123], a[120:123], a[52:55], v[120:123]\n"
"v_mfma_f32_16x16x32_bf16 v[124:127], a[124:127], a[52:55], v[124:127]\n"
"global_load_dwordx4 a[52:55], v53, s[28:29] sc1\n"
"v_mfma_f32_16x16x32_bf16 v[128:131], a[96:99], a[56:59], v[128:131]\n"
"ds_read_b64_tr_b16 a[140:141], v39 offset:24576\n"
"v_mfma_f32_16x16x32_bf16 v[132:135], a[100:103], a[56:59], v[132:135]\n"
"v_mfma_f32_16x16x32_bf16 v[136:139], a[104:107], a[56:59], v[136:139]\n"
"v_mfma_f32_16x16x32_bf16 v[140:143], a[108:111], a[56:59], v[140:143]\n"
"ds_read_b64_tr_b16 a[142:143], v39 offset:25088\n"
"v_mfma_f32_16x16x32_bf16 v[144:147], a[112:115], a[56:59], v[144:147]\n"
"v_mfma_f32_16x16x32_bf16 v[148:151], a[116:119], a[56:59], v[148:151]\n"
"v_mfma_f32_16x16x32_bf16 v[152:155], a[120:123], a[56:59], v[152:155]\n"
"ds_read_b64_tr_b16 a[144:145], v40 offset:24576\n"
"v_mfma_f32_16x16x32_bf16 v[156:159], a[124:127], a[56:59], v[156:159]\n"
"global_load_dwordx4 a[56:59], v54, s[28:29] sc1\n"
"v_mfma_f32_16x16x32_bf16 v[160:163], a[96:99], a[60:63], v[160:163]\n"
"ds_read_b64_tr_b16 a[146:147], v40 offset:25088\n"
"v_mfma_f32_16x16x32_bf16 v[164:167], a[100:103], a[60:63], v[164:167]\n"
"v_mfma_f32_16x16x32_bf16 v[168:171], a[104:107], a[60:63], v[168:171]\n"
"v_mfma_f32_16x16x32_bf16 v[172:175], a[108:111], a[60:63], v[172:175]\n"
"ds_read_b64_tr_b16 a[148:149], v41 offset:24576\n"
"v_mfma_f32_16x16x32_bf16 v[176:179], a[112:115], a[60:63], v[176:179]\n"
"v_mfma_f32_16x16x32_bf16 v[180:183], a[116:119], a[60:63], v[180:183]\n"
"v_mfma_f32_16x16x32_bf16 v[184:187], a[120:123], a[60:63], v[184:187]\n"
"ds_read_b64_tr_b16 a[150:151], v41 offset:25088\n"
"v_mfma_f32_16x16x32_bf16 v[188:191], a[124:127], a[60:63], v[188:191]\n"
"global_load_dwordx4 a[60:63], v55, s[28:29] sc1\n"
"v_mfma_f32_16x16x32_bf16 v[192:195], a[96:99], a[64:67], v[192:195]\n"
"v_mfma_f32_16x16x32_bf16 v[196:199], a[100:103], a[64:67], v[196:199]\n"
"ds_read_b64_tr_b16 a[152:153], v42 offset:24576\n"
"v_mfma_f32_16x16x32_bf16 v[200:203], a[104:107], a[64:67], v[200:203]\n"
"v_mfma_f32_16x16x32_bf16 v[204:207], a[108:111], a[64:67], v[204:207]\n"
"ds_read_b64_tr_b16 a[154:155], v42 offset:25088\n"
"v_mfma_f32_16x16x32_bf16 v[208:211], a[112:115], a[64:67], v[208:211]\n"
"v_mfma_f32_16x16x32_bf16 v[212:215], a[116:119], a[64:67], v[212:215]\n"
"v_mfma_f32_16x16x32_bf16 v[216:219], a[120:123], a[64:67], v[216:219]\n"
"ds_read_b64_tr_b16 a[156:157], v43 offset:24576\n"
"v_mfma_f32_16x16x32_bf16 v[220:223], a[124:127], a[64:67], v[220:223]\n"
"global_load_dwordx4 a[64:67], v56, s[28:29] sc1\n"
"v_mfma_f32_16x16x32_bf16 v[224:227], a[96:99], a[68:71], v[224:227]\n"
"v_mfma_f32_16x16x32_bf16 v[228:231], a[100:103], a[68:71], v[228:231]\n"
"ds_read_b64_tr_b16 a[158:159], v43 offset:25088\n"
"v_mfma_f32_16x16x32_bf16 v[232:235], a[104:107], a[68:71], v[232:235]\n"
"v_mfma_f32_16x16x32_bf16 v[236:239], a[108:111], a[68:71], v[236:239]\n"
"v_mfma_f32_16x16x32_bf16 v[240:243], a[112:115], a[68:71], v[240:243]\n"
"v_mfma_f32_16x16x32_bf16 v[244:247], a[116:119], a[68:71], v[244:247]\n"
"v_mfma_f32_16x16x32_bf16 v[248:251], a[120:123], a[68:71], v[248:251]\n"
"v_mfma_f32_16x16x32_bf16 v[252:255], a[124:127], a[68:71], v[252:255]\n"
"global_load_dwordx4 a[68:71], v57, s[28:29] sc1\n"
"s_waitcnt vmcnt(34) lgkmcnt(0)\n"
"s_add_u32 m0, s24, 28672\n"
"s_add_u32 s26, s22, s35\n"
"s_addc_u32 s27, s23, 0\n"
"v_mfma_f32_16x16x32_bf16 v[64:67], a[128:131], a[72:75], v[64:67]\n"
"global_load_lds_dwordx4 v44, s[26:27] offset:-4096\n"
"s_add_u32 s28, s20, s53\n"
"s_addc_u32 s29, s21, 0\n"
"ds_read_b64_tr_b16 a[96:97], v36 offset:0\n"
"v_mfma_f32_16x16x32_bf16 v[68:71], a[132:135], a[72:75], v[68:71]\n"
"global_load_lds_dwordx4 v45, s[26:27] offset:-3072\n"
"v_mfma_f32_16x16x32_bf16 v[72:75], a[136:139], a[72:75], v[72:75]\n"
"global_load_lds_dwordx4 v46, s[26:27] offset:-2048\n"
"ds_read_b64_tr_b16 a[98:99], v36 offset:512\n"
"v_mfma_f32_16x16x32_bf16 v[76:79], a[140:143], a[72:75], v[76:79]\n"
"global_load_lds_dwordx4 v47, s[26:27] offset:-1024\n"
"v_mfma_f32_16x16x32_bf16 v[80:83], a[144:147], a[72:75], v[80:83]\n"
"global_load_lds_dwordx4 v48, s[26:27] offset:0\n"
"v_mfma_f32_16x16x32_bf16 v[84:87], a[148:151], a[72:75], v[84:87]\n"
"global_load_lds_dwordx4 v49, s[26:27] offset:1024\n"
"ds_read_b64_tr_b16 a[100:101], v37 offset:0\n"
"v_mfma_f32_16x16x32_bf16 v[88:91], a[152:155], a[72:75], v[88:91]\n"
"global_load_lds_dwordx4 v50, s[26:27] offset:2048\n"
"v_mfma_f32_16x16x32_bf16 v[92:95], a[156:159], a[72:75], v[92:95]\n"
"global_load_lds_dwordx4 v51, s[26:27] offset:3072\n"
"global_load_dwordx4 a[72:75], v52, s[28:29] sc1\n"
"v_mfma_f32_16x16x32_bf16 v[96:99], a[128:131], a[76:79], v[96:99]\n"
"ds_read_b64_tr_b16 a[102:103], v37 offset:512\n"
"v_mfma_f32_16x16x32_bf16 v[100:103], a[132:135], a[76:79], v[100:103]\n"
"v_mfma_f32_16x16x32_bf16 v[104:107], a[136:139], a[76:79], v[104:107]\n"
"v_mfma_f32_16x16x32_bf16 v[108:111], a[140:143], a[76:79], v[108:111]\n"
"ds_read_b64_tr_b16 a[104:105], v38 offset:0\n"
"v_mfma_f32_16x16x32_bf16 v[112:115], a[144:147], a[76:79], v[112:115]\n"
"v_mfma_f32_16x16x32_bf16 v[116:119], a[148:151], a[76:79], v[116:119]\n"
"ds_read_b64_tr_b16 a[106:107], v38 offset:512\n"
"v_mfma_f32_16x16x32_bf16 v[120:123], a[152:155], a[76:79], v[120:123]\n"
"v_mfma_f32_16x16x32_bf16 v[124:127], a[156:159], a[76:79], v[124:127]\n"
"global_load_dwordx4 a[76:79], v53, s[28:29] sc1\n"
"v_mfma_f32_16x16x32_bf16 v[128:131], a[128:131], a[80:83], v[128:131]\n"
"ds_read_b64_tr_b16 a[108:109], v39 offset:0\n"
"v_mfma_f32_16x16x32_bf16 v[132:135], a[132:135], a[80:83], v[132:135]\n"
"v_mfma_f32_16x16x32_bf16 v[136:139], a[136:139], a[80:83], v[136:139]\n"
"v_mfma_f32_16x16x32_bf16 v[140:143], a[140:143], a[80:83], v[140:143]\n"
"ds_read_b64_tr_b16 a[110:111], v39 offset:512\n"
"v_mfma_f32_16x16x32_bf16 v[144:147], a[144:147], a[80:83], v[144:147]\n"
"v_mfma_f32_16x16x32_bf16 v[148:151], a[148:151], a[80:83], v[148:151]\n"
"v_mfma_f32_16x16x32_bf16 v[152:155], a[152:155], a[80:83], v[152:155]\n"
"ds_read_b64_tr_b16 a[112:113], v40 offset:0\n"
"v_mfma_f32_16x16x32_bf16 v[156:159], a[156:159], a[80:83], v[156:159]\n"
"global_load_dwordx4 a[80:83], v54, s[28:29] sc1\n"
"v_mfma_f32_16x16x32_bf16 v[160:163], a[128:131], a[84:87], v[160:163]\n"
"ds_read_b64_tr_b16 a[114:115], v40 offset:512\n"
"v_mfma_f32_16x16x32_bf16 v[164:167], a[132:135], a[84:87], v[164:167]\n"
"v_mfma_f32_16x16x32_bf16 v[168:171], a[136:139], a[84:87], v[168:171]\n"
"v_mfma_f32_16x16x32_bf16 v[172:175], a[140:143], a[84:87], v[172:175]\n"
"ds_read_b64_tr_b16 a[116:117], v41 offset:0\n"
"v_mfma_f32_16x16x32_bf16 v[176:179], a[144:147], a[84:87], v[176:179]\n"
"v_mfma_f32_16x16x32_bf16 v[180:183], a[148:151], a[84:87], v[180:183]\n"
"v_mfma_f32_16x16x32_bf16 v[184:187], a[152:155], a[84:87], v[184:187]\n"
"ds_read_b64_tr_b16 a[118:119], v41 offset:512\n"
"v_mfma_f32_16x16x32_bf16 v[188:191], a[156:159], a[84:87], v[188:191]\n"
"global_load_dwordx4 a[84:87], v55, s[28:29] sc1\n"
"v_mfma_f32_16x16x32_bf16 v[192:195], a[128:131], a[88:91], v[192:195]\n"
"v_mfma_f32_16x16x32_bf16 v[196:199], a[132:135], a[88:91], v[196:199]\n"
"ds_read_b64_tr_b16 a[120:121], v42 offset:0\n"
"v_mfma_f32_16x16x32_bf16 v[200:203], a[136:139], a[88:91], v[200:203]\n"
"v_mfma_f32_16x16x32_bf16 v[204:207], a[140:143], a[88:91], v[204:207]\n"
"ds_read_b64_tr_b16 a[122:123], v42 offset:512\n"
"v_mfma_f32_16x16x32_bf16 v[208:211], a[144:147], a[88:91], v[208:211]\n"
"v_mfma_f32_16x16x32_bf16 v[212:215], a[148:151], a[88:91], v[212:215]\n"
"v_mfma_f32_16x16x32_bf16 v[216:219], a[152:155], a[88:91], v[216:219]\n"
"ds_read_b64_tr_b16 a[124:125], v43 offset:0\n"
"v_mfma_f32_16x16x32_bf16 v[220:223], a[156:159], a[88:91], v[220:223]\n"
"global_load_dwordx4 a[88:91], v56, s[28:29] sc1\n"
"v_mfma_f32_16x16x32_bf16 v[224:227], a[128:131], a[92:95], v[224:227]\n"
"v_mfma_f32_16x16x32_bf16 v[228:231], a[132:135], a[92:95], v[228:231]\n"
"ds_read_b64_tr_b16 a[126:127], v43 offset:512\n"
"v_mfma_f32_16x16x32_bf16 v[232:235], a[136:139], a[92:95], v[232:235]\n"
"v_mfma_f32_16x16x32_bf16 v[236:239], a[140:143], a[92:95], v[236:239]\n"
"v_mfma_f32_16x16x32_bf16 v[240:243], a[144:147], a[92:95], v[240:243]\n"
"v_mfma_f32_16x16x32_bf16 v[244:247], a[148:151], a[92:95], v[244:247]\n"
"v_mfma_f32_16x16x32_bf16 v[248:251], a[152:155], a[92:95], v[248:251]\n"
"v_mfma_f32_16x16x32_bf16 v[252:255], a[156:159], a[92:95], v[252:255]\n"
"global_load_dwordx4 a[92:95], v57, s[28:29] sc1\n"
"s_sub_u32 s25, s25, 1\n"
"s_cmp_lg_u32 s25, 0\n"
"s_cbranch_scc1 1b\n"
"s_waitcnt vmcnt(34) lgkmcnt(0)\n"
"v_mfma_f32_16x16x32_bf16 v[64:67], a[96:99], a[0:3], v[64:67]\n"
"ds_read_b64_tr_b16 a[128:129], v36 offset:8192\n"
"v_mfma_f32_16x16x32_bf16 v[68:71], a[100:103], a[0:3], v[68:71]\n"
"v_mfma_f32_16x16x32_bf16 v[72:75], a[104:107], a[0:3], v[72:75]\n"
"ds_read_b64_tr_b16 a[130:131], v36 offset:8704\n"
"v_mfma_f32_16x16x32_bf16 v[76:79], a[108:111], a[0:3], v[76:79]\n"
"v_mfma_f32_16x16x32_bf16 v[80:83], a[112:115], a[0:3], v[80:83]\n"
"v_mfma_f32_16x16x32_bf16 v[84:87], a[116:119], a[0:3], v[84:87]\n"
"ds_read_b64_tr_b16 a[132:133], v37 offset:8192\n"
"v_mfma_f32_16x16x32_bf16 v[88:91], a[120:123], a[0:3], v[88:91]\n"
"v_mfma_f32_16x16x32_bf16 v[92:95], a[124:127], a[0:3], v[92:95]\n"
"v_mfma_f32_16x16x32_bf16 v[96:99], a[96:99], a[4:7], v[96:99]\n"
"ds_read_b64_tr_b16 a[134:135], v37 offset:8704\n"
"v_mfma_f32_16x16x32_bf16 v[100:103], a[100:103], a[4:7], v[100:103]\n"
"v_mfma_f32_16x16x32_bf16 v[104:107], a[104:107], a[4:7], v[104:107]\n"
"v_mfma_f32_16x16x32_bf16 v[108:111], a[108:111], a[4:7], v[108:111]\n"
"ds_read_b64_tr_b16 a[136:137], v38 offset:8192\n"
"v_mfma_f32_16x16x32_bf16 v[112:115], a[112:115], a[4:7], v[112:115]\n"
"v_mfma_f32_16x16x32_bf16 v[116:119], a[116:119], a[4:7], v[116:119]\n"
"ds_read_b64_tr_b16 a[138:139], v38 offset:8704\n"
"v_mfma_f32_16x16x32_bf16 v[120:123], a[120:123], a[4:7], v[120:123]\n"
"v_mfma_f32_16x16x32_bf16 v[124:127], a[124:127], a[4:7], v[124:127]\n"
"v_mfma_f32_16x16x32_bf16 v[128:131], a[96:99], a[8:11], v[128:131]\n"
"ds_read_b64_tr_b16 a[140:141], v39 offset:8192\n"
"v_mfma_f32_16x16x32_bf16 v[132:135], a[100:103], a[8:11], v[132:135]\n"
"v_mfma_f32_16x16x32_bf16 v[136:139], a[104:107], a[8:11], v[136:139]\n"
"v_mfma_f32_16x16x32_bf16 v[140:143], a[108:111], a[8:11], v[140:143]\n"
"ds_read_b64_tr_b16 a[142:143], v39 offset:8704\n"
"v_mfma_f32_16x16x32_bf16 v[144:147], a[112:115], a[8:11], v[144:147]\n"
"v_mfma_f32_16x16x32_bf16 v[148:151], a[116:119], a[8:11], v[148:151]\n"
"v_mfma_f32_16x16x32_bf16 v[152:155], a[120:123], a[8:11], v[152:155]\n"
"ds_read_b64_tr_b16 a[144:145], v40 offset:8192\n"
"v_mfma_f32_16x16x32_bf16 v[156:159], a[124:127], a[8:11], v[156:159]\n"
"v_mfma_f32_16x16x32_bf16 v[160:163], a[96:99], a[12:15], v[160:163]\n"
"ds_read_b64_tr_b16 a[146:147], v40 offset:8704\n"
"v_mfma_f32_16x16x32_bf16 v[164:167], a[100:103], a[12:15], v[164:167]\n"
"v_mfma_f32_16x16x32_bf16 v[168:171], a[104:107], a[12:15], v[168:171]\n"
"v_mfma_f32_16x16x32_bf16 v[172:175], a[108:111], a[12:15], v[172:175]\n"
"ds_read_b64_tr_b16 a[148:149], v41 offset:8192\n"
"v_mfma_f32_16x16x32_bf16 v[176:179], a[112:115], a[12:15], v[176:179]\n"
"v_mfma_f32_16x16x32_bf16 v[180:183], a[116:119], a[12:15], v[180:183]\n"
"v_mfma_f32_16x16x32_bf16 v[184:187], a[120:123], a[12:15], v[184:187]\n"
"ds_read_b64_tr_b16 a[150:151], v41 offset:8704\n"
"v_mfma_f32_16x16x32_bf16 v[188:191], a[124:127], a[12:15], v[188:191]\n"
"v_mfma_f32_16x16x32_bf16 v[192:195], a[96:99], a[16:19], v[192:195]\n"
"v_mfma_f32_16x16x32_bf16 v[196:199], a[100:103], a[16:19], v[196:199]\n"
"ds_read_b64_tr_b16 a[152:153], v42 offset:8192\n"
"v_mfma_f32_16x16x32_bf16 v[200:203], a[104:107], a[16:19], v[200:203]\n"
"v_mfma_f32_16x16x32_bf16 v[204:207], a[108:111], a[16:19], v[204:207]\n"
"ds_read_b64_tr_b16 a[154:155], v42 offset:8704\n"
"v_mfma_f32_16x16x32_bf16 v[208:211], a[112:115], a[16:19], v[208:211]\n"
"v_mfma_f32_16x16x32_bf16 v[212:215], a[116:119], a[16:19], v[212:215]\n"
"v_mfma_f32_16x16x32_bf16 v[216:219], a[120:123], a[16:19], v[216:219]\n"
"ds_read_b64_tr_b16 a[156:157], v43 offset:8192\n"
"v_mfma_f32_16x16x32_bf16 v[220:223], a[124:127], a[16:19], v[220:223]\n"
"v_mfma_f32_16x16x32_bf16 v[224:227], a[96:99], a[20:23], v[224:227]\n"
"v_mfma_f32_16x16x32_bf16 v[228:231], a[100:103], a[20:23], v[228:231]\n"
"ds_read_b64_tr_b16 a[158:159], v43 offset:8704\n"
"v_mfma_f32_16x16x32_bf16 v[232:235], a[104:107], a[20:23], v[232:235]\n"
"v_mfma_f32_16x16x32_bf16 v[236:239], a[108:111], a[20:23], v[236:239]\n"
"v_mfma_f32_16x16x32_bf16 v[240:243], a[112:115], a[20:23], v[240:243]\n"
"v_mfma_f32_16x16x32_bf16 v[244:247], a[116:119], a[20:23], v[244:247]\n"
"v_mfma_f32_16x16x32_bf16 v[248:251], a[120:123], a[20:23], v[248:251]\n"
"v_mfma_f32_16x16x32_bf16 v[252:255], a[124:127], a[20:23], v[252:255]\n"
"s_waitcnt vmcnt(20) lgkmcnt(0)\n"
"v_mfma_f32_16x16x32_bf16 v[64:67], a[128:131], a[24:27], v[64:67]\n"
"ds_read_b64_tr_b16 a[96:97], v36 offset:16384\n"
"v_mfma_f32_16x16x32_bf16 v[68:71], a[132:135], a[24:27], v[68:71]\n"
"v_mfma_f32_16x16x32_bf16 v[72:75], a[136:139], a[24:27], v[72:75]\n"
"ds_read_b64_tr_b16 a[98:99], v36 offset:16896\n"
"v_mfma_f32_16x16x32_bf16 v[76:79], a[140:143], a[24:27], v[76:79]\n"
"v_mfma_f32_16x16x32_bf16 v[80:83], a[144:147], a[24:27], v[80:83]\n"
"v_mfma_f32_16x16x32_bf16 v[84:87], a[148:151], a[24:27], v[84:87]\n"
"ds_read_b64_tr_b16 a[100:101], v37 offset:16384\n"
"v_mfma_f32_16x16x32_bf16 v[88:91], a[152:155], a[24:27], v[88:91]\n"
"v_mfma_f32_16x16x32_bf16 v[92:95], a[156:159], a[24:27], v[92:95]\n"
"v_mfma_f32_16x16x32_bf16 v[96:99], a[128:131], a[28:31], v[96:99]\n"
"ds_read_b64_tr_b16 a[102:103], v37 offset:16896\n"
"v_mfma_f32_16x16x32_bf16 v[100:103], a[132:135], a[28:31], v[100:103]\n"
"v_mfma_f32_16x16x32_bf16 v[104:107], a[136:139], a[28:31], v[104:107]\n"
"v_mfma_f32_16x16x32_bf16 v[108:111], a[140:143], a[28:31], v[108:111]\n"
"ds_read_b64_tr_b16 a[104:105], v38 offset:16384\n"
"v_mfma_f32_16x16x32_bf16 v[112:115], a[144:147], a[28:31], v[112:115]\n"
"v_mfma_f32_16x16x32_bf16 v[116:119], a[148:151], a[28:31], v[116:119]\n"
"ds_read_b64_tr_b16 a[106:107], v38 offset:16896\n"
"v_mfma_f32_16x16x32_bf16 v[120:123], a[152:155], a[28:31], v[120:123]\n"
"v_mfma_f32_16x16x32_bf16 v[124:127], a[156:159], a[28:31], v[124:127]\n"
"v_mfma_f32_16x16x32_bf16 v[128:131], a[128:131], a[32:35], v[128:131]\n"
"ds_read_b64_tr_b16 a[108:109], v39 offset:16384\n"
"v_mfma_f32_16x16x32_bf16 v[132:135], a[132:135], a[32:35], v[132:135]\n"
"v_mfma_f32_16x16x32_bf16 v[136:139], a[136:139], a[32:35], v[136:139]\n"
"v_mfma_f32_16x16x32_bf16 v[140:143], a[140:143], a[32:35], v[140:143]\n"
"ds_read_b64_tr_b16 a[110:111], v39 offset:16896\n"
"v_mfma_f32_16x16x32_bf16 v[144:147], a[144:147], a[32:35], v[144:147]\n"
"v_mfma_f32_16x16x32_bf16 v[148:151], a[148:151], a[32:35], v[148:151]\n"
"v_mfma_f32_16x16x32_bf16 v[152:155], a[152:155], a[32:35], v[152:155]\n"
"ds_read_b64_tr_b16 a[112:113], v40 offset:16384\n"
"v_mfma_f32_16x16x32_bf16 v[156:159], a[156:159], a[32:35], v[156:159]\n"
"v_mfma_f32_16x16x32_bf16 v[160:163], a[128:131], a[36:39], v[160:163]\n"
"ds_read_b64_tr_b16 a[114:115], v40 offset:16896\n"
"v_mfma_f32_16x16x32_bf16 v[164:167], a[132:135], a[36:39], v[164:167]\n"
"v_mfma_f32_16x16x32_bf16 v[168:171], a[136:139], a[36:39], v[168:171]\n"
"v_mfma_f32_16x16x32_bf16 v[172:175], a[140:143], a[36:39], v[172:175]\n"
"ds_read_b64_tr_b16 a[116:117], v41 offset:16384\n"
"v_mfma_f32_16x16x32_bf16 v[176:179], a[144:147], a[36:39], v[176:179]\n"
"v_mfma_f32_16x16x32_bf16 v[180:183], a[148:151], a[36:39], v[180:183]\n"
"v_mfma_f32_16x16x32_bf16 v[184:187], a[152:155], a[36:39], v[184:187]\n"
"ds_read_b64_tr_b16 a[118:119], v41 offset:16896\n"
"v_mfma_f32_16x16x32_bf16 v[188:191], a[156:159], a[36:39], v[188:191]\n"
"v_mfma_f32_16x16x32_bf16 v[192:195], a[128:131], a[40:43], v[192:195]\n"
"v_mfma_f32_16x16x32_bf16 v[196:199], a[132:135], a[40:43], v[196:199]\n"
"ds_read_b64_tr_b16 a[120:121], v42 offset:16384\n"
"v_mfma_f32_16x16x32_bf16 v[200:203], a[136:139], a[40:43], v[200:203]\n"
"v_mfma_f32_16x16x32_bf16 v[204:207], a[140:143], a[40:43], v[204:207]\n"
"ds_read_b64_tr_b16 a[122:123], v42 offset:16896\n"
"v_mfma_f32_16x16x32_bf16 v[208:211], a[144:147], a[40:43], v[208:211]\n"
"v_mfma_f32_16x16x32_bf16 v[212:215], a[148:151], a[40:43], v[212:215]\n"
"v_mfma_f32_16x16x32_bf16 v[216:219], a[152:155], a[40:43], v[216:219]\n"
"ds_read_b64_tr_b16 a[124:125], v43 offset:16384\n"
"v_mfma_f32_16x16x32_bf16 v[220:223], a[156:159], a[40:43], v[220:223]\n"
"v_mfma_f32_16x16x32_bf16 v[224:227], a[128:131], a[44:47], v[224:227]\n"
"v_mfma_f32_16x16x32_bf16 v[228:231], a[132:135], a[44:47], v[228:231]\n"
"ds_read_b64_tr_b16 a[126:127], v43 offset:16896\n"
"v_mfma_f32_16x16x32_bf16 v[232:235], a[136:139], a[44:47], v[232:235]\n"
"v_mfma_f32_16x16x32_bf16 v[236:239], a[140:143], a[44:47], v[236:239]\n"
"v_mfma_f32_16x16x32_bf16 v[240:243], a[144:147], a[44:47], v[240:243]\n"
"v_mfma_f32_16x16x32_bf16 v[244:247], a[148:151], a[44:47], v[244:247]\n"
"v_mfma_f32_16x16x32_bf16 v[248:251], a[152:155], a[44:47], v[248:251]\n"
"v_mfma_f32_16x16x32_bf16 v[252:255], a[156:159], a[44:47], v[252:255]\n"
"s_waitcnt vmcnt(6) lgkmcnt(0)\n"
"v_mfma_f32_16x16x32_bf16 v[64:67], a[96:99], a[48:51], v[64:67]\n"
"ds_read_b64_tr_b16 a[128:129], v36 offset:24576\n"
"v_mfma_f32_16x16x32_bf16 v[68:71], a[100:103], a[48:51], v[68:71]\n"
"v_mfma_f32_16x16x32_bf16 v[72:75], a[104:107], a[48:51], v[72:75]\n"
"ds_read_b64_tr_b16 a[130:131], v36 offset:25088\n"
"v_mfma_f32_16x16x32_bf16 v[76:79], a[108:111], a[48:51], v[76:79]\n"
"v_mfma_f32_16x16x32_bf16 v[80:83], a[112:115], a[48:51], v[80:83]\n"
"v_mfma_f32_16x16x32_bf16 v[84:87], a[116:119], a[48:51], v[84:87]\n"
"ds_read_b64_tr_b16 a[132:133], v37 offset:24576\n"
"v_mfma_f32_16x16x32_bf16 v[88:91], a[120:123], a[48:51], v[88:91]\n"
"v_mfma_f32_16x16x32_bf16 v[92:95], a[124:127], a[48:51], v[92:95]\n"
"v_mfma_f32_16x16x32_bf16 v[96:99], a[96:99], a[52:55], v[96:99]\n"
"ds_read_b64_tr_b16 a[134:135], v37 offset:25088\n"
"v_mfma_f32_16x16x32_bf16 v[100:103], a[100:103], a[52:55], v[100:103]\n"
"v_mfma_f32_16x16x32_bf16 v[104:107], a[104:107], a[52:55], v[104:107]\n"
"v_mfma_f32_16x16x32_bf16 v[108:111], a[108:111], a[52:55], v[108:111]\n"
"ds_read_b64_tr_b16 a[136:137], v38 offset:24576\n"
"v_mfma_f32_16x16x32_bf16 v[112:115], a[112:115], a[52:55], v[112:115]\n"
"v_mfma_f32_16x16x32_bf16 v[116:119], a[116:119], a[52:55], v[116:119]\n"
"ds_read_b64_tr_b16 a[138:139], v38 offset:25088\n"
"v_mfma_f32_16x16x32_bf16 v[120:123], a[120:123], a[52:55], v[120:123]\n"
"v_mfma_f32_16x16x32_bf16 v[124:127], a[124:127], a[52:55], v[124:127]\n"
"v_mfma_f32_16x16x32_bf16 v[128:131], a[96:99], a[56:59], v[128:131]\n"
"ds_read_b64_tr_b16 a[140:141], v39 offset:24576\n"
"v_mfma_f32_16x16x32_bf16 v[132:135], a[100:103], a[56:59], v[132:135]\n"
"v_mfma_f32_16x16x32_bf16 v[136:139], a[104:107], a[56:59], v[136:139]\n"
"v_mfma_f32_16x16x32_bf16 v[140:143], a[108:111], a[56:59], v[140:143]\n"
"ds_read_b64_tr_b16 a[142:143], v39 offset:25088\n"
"v_mfma_f32_16x16x32_bf16 v[144:147], a[112:115], a[56:59], v[144:147]\n"
"v_mfma_f32_16x16x32_bf16 v[148:151], a[116:119], a[56:59], v[148:151]\n"
"v_mfma_f32_16x16x32_bf16 v[152:155], a[120:123], a[56:59], v[152:155]\n"
"ds_read_b64_tr_b16 a[144:145], v40 offset:24576\n"
"v_mfma_f32_16x16x32_bf16 v[156:159], a[124:127], a[56:59], v[156:159]\n"
"v_mfma_f32_16x16x32_bf16 v[160:163], a[96:99], a[60:63], v[160:163]\n"
"ds_read_b64_tr_b16 a[146:147], v40 offset:25088\n"
"v_mfma_f32_16x16x32_bf16 v[164:167], a[100:103], a[60:63], v[164:167]\n"
"v_mfma_f32_16x16x32_bf16 v[168:171], a[104:107], a[60:63], v[168:171]\n"
"v_mfma_f32_16x16x32_bf16 v[172:175], a[108:111], a[60:63], v[172:175]\n"
"ds_read_b64_tr_b16 a[148:149], v41 offset:24576\n"
"v_mfma_f32_16x16x32_bf16 v[176:179], a[112:115], a[60:63], v[176:179]\n"
"v_mfma_f32_16x16x32_bf16 v[180:183], a[116:119], a[60:63], v[180:183]\n"
"v_mfma_f32_16x16x32_bf16 v[184:187], a[120:123], a[60:63], v[184:187]\n"
"ds_read_b64_tr_b16 a[150:151], v41 offset:25088\n"
"v_mfma_f32_16x16x32_bf16 v[188:191], a[124:127], a[60:63], v[188:191]\n"
"v_mfma_f32_16x16x32_bf16 v[192:195], a[96:99], a[64:67], v[192:195]\n"
"v_mfma_f32_16x16x32_bf16 v[196:199], a[100:103], a[64:67], v[196:199]\n"
"ds_read_b64_tr_b16 a[152:153], v42 offset:24576\n"
"v_mfma_f32_16x16x32_bf16 v[200:203], a[104:107], a[64:67], v[200:203]\n"
"v_mfma_f32_16x16x32_bf16 v[204:207], a[108:111], a[64:67], v[204:207]\n"
"ds_read_b64_tr_b16 a[154:155], v42 offset:25088\n"
"v_mfma_f32_16x16x32_bf16 v[208:211], a[112:115], a[64:67], v[208:211]\n"
"v_mfma_f32_16x16x32_bf16 v[212:215], a[116:119], a[64:67], v[212:215]\n"
"v_mfma_f32_16x16x32_bf16 v[216:219], a[120:123], a[64:67], v[216:219]\n"
"ds_read_b64_tr_b16 a[156:157], v43 offset:24576\n"
"v_mfma_f32_16x16x32_bf16 v[220:223], a[124:127], a[64:67], v[220:223]\n"
"v_mfma_f32_16x16x32_bf16 v[224:227], a[96:99], a[68:71], v[224:227]\n"
"v_mfma_f32_16x16x32_bf16 v[228:231], a[100:103], a[68:71], v[228:231]\n"
"ds_read_b64_tr_b16 a[158:159], v43 offset:25088\n"
"v_mfma_f32_16x16x32_bf16 v[232:235], a[104:107], a[68:71], v[232:235]\n"
"v_mfma_f32_16x16x32_bf16 v[236:239], a[108:111], a[68:71], v[236:239]\n"
"v_mfma_f32_16x16x32_bf16 v[240:243], a[112:115], a[68:71], v[240:243]\n"
"v_mfma_f32_16x16x32_bf16 v[244:247], a[116:119], a[68:71], v[244:247]\n"
"v_mfma_f32_16x16x32_bf16 v[248:251], a[120:123], a[68:71], v[248:251]\n"
"v_mfma_f32_16x16x32_bf16 v[252:255], a[124:127], a[68:71], v[252:255]\n"
"s_waitcnt vmcnt(0) lgkmcnt(0)\n"
"v_mfma_f32_16x16x32_bf16 v[64:67], a[128:131], a[72:75], v[64:67]\n"
"v_mfma_f32_16x16x32_bf16 v[68:71], a[132:135], a[72:75], v[68:71]\n"
"v_mfma_f32_16x16x32_bf16 v[72:75], a[136:139], a[72:75], v[72:75]\n"
"v_mfma_f32_16x16x32_bf16 v[76:79], a[140:143], a[72:75], v[76:79]\n"
"v_mfma_f32_16x16x32_bf16 v[80:83], a[144:147], a[72:75], v[80:83]\n"
"v_mfma_f32_16x16x32_bf16 v[84:87], a[148:151], a[72:75], v[84:87]\n"
"v_mfma_f32_16x16x32_bf16 v[88:91], a[152:155], a[72:75], v[88:91]\n"
"v_mfma_f32_16x16x32_bf16 v[92:95], a[156:159], a[72:75], v[92:95]\n"
"v_mfma_f32_16x16x32_bf16 v[96:99], a[128:131], a[76:79], v[96:99]\n"
"v_mfma_f32_16x16x32_bf16 v[100:103], a[132:135], a[76:79], v[100:103]\n"
"v_mfma_f32_16x16x32_bf16 v[104:107], a[136:139], a[76:79], v[104:107]\n"
"v_mfma_f32_16x16x32_bf16 v[108:111], a[140:143], a[76:79], v[108:111]\n"
"v_mfma_f32_16x16x32_bf16 v[112:115], a[144:147], a[76:79], v[112:115]\n"
"v_mfma_f32_16x16x32_bf16 v[116:119], a[148:151], a[76:79], v[116:119]\n"
"v_mfma_f32_16x16x32_bf16 v[120:123], a[152:155], a[76:79], v[120:123]\n"
"v_mfma_f32_16x16x32_bf16 v[124:127], a[156:159], a[76:79], v[124:127]\n"
"v_mfma_f32_16x16x32_bf16 v[128:131], a[128:131], a[80:83], v[128:131]\n"
"v_mfma_f32_16x16x32_bf16 v[132:135], a[132:135], a[80:83], v[132:135]\n"
"v_mfma_f32_16x16x32_bf16 v[136:139], a[136:139], a[80:83], v[136:139]\n"
"v_mfma_f32_16x16x32_bf16 v[140:143], a[140:143], a[80:83], v[140:143]\n"
"v_mfma_f32_16x16x32_bf16 v[144:147], a[144:147], a[80:83], v[144:147]\n"
"v_mfma_f32_16x16x32_bf16 v[148:151], a[148:151], a[80:83], v[148:151]\n"
"v_mfma_f32_16x16x32_bf16 v[152:155], a[152:155], a[80:83], v[152:155]\n"
"v_mfma_f32_16x16x32_bf16 v[156:159], a[156:159], a[80:83], v[156:159]\n"
"v_mfma_f32_16x16x32_bf16 v[160:163], a[128:131], a[84:87], v[160:163]\n"
"v_mfma_f32_16x16x32_bf16 v[164:167], a[132:135], a[84:87], v[164:167]\n"
"v_mfma_f32_16x16x32_bf16 v[168:171], a[136:139], a[84:87], v[168:171]\n"
"v_mfma_f32_16x16x32_bf16 v[172:175], a[140:143], a[84:87], v[172:175]\n"
"v_mfma_f32_16x16x32_bf16 v[176:179], a[144:147], a[84:87], v[176:179]\n"
"v_mfma_f32_16x16x32_bf16 v[180:183], a[148:151], a[84:87], v[180:183]\n"
"v_mfma_f32_16x16x32_bf16 v[184:187], a[152:155], a[84:87], v[184:187]\n"
"v_mfma_f32_16x16x32_bf16 v[188:191], a[156:159], a[84:87], v[188:191]\n"
"v_mfma_f32_16x16x32_bf16 v[192:195], a[128:131], a[88:91], v[192:195]\n"
"v_mfma_f32_16x16x32_bf16 v[196:199], a[132:135], a[88:91], v[196:199]\n"
"v_mfma_f32_16x16x32_bf16 v[200:203], a[136:139], a[88:91], v[200:203]\n"
"v_mfma_f32_16x16x32_bf16 v[204:207], a[140:143], a[88:91], v[204:207]\n"
"v_mfma_f32_16x16x32_bf16 v[208:211], a[144:147], a[88:91], v[208:211]\n"
"v_mfma_f32_16x16x32_bf16 v[212:215], a[148:151], a[88:91], v[212:215]\n"
"v_mfma_f32_16x16x32_bf16 v[216:219], a[152:155], a[88:91], v[216:219]\n"
"v_mfma_f32_16x16x32_bf16 v[220:223], a[156:159], a[88:91], v[220:223]\n"
"v_mfma_f32_16x16x32_bf16 v[224:227], a[128:131], a[92:95], v[224:227]\n"
"v_mfma_f32_16x16x32_bf16 v[228:231], a[132:135], a[92:95], v[228:231]\n"
"v_mfma_f32_16x16x32_bf16 v[232:235], a[136:139], a[92:95], v[232:235]\n"
"v_mfma_f32_16x16x32_bf16 v[236:239], a[140:143], a[92:95], v[236:239]\n"
"v_mfma_f32_16x16x32_bf16 v[240:243], a[144:147], a[92:95], v[240:243]\n"
"v_mfma_f32_16x16x32_bf16 v[244:247], a[148:151], a[92:95], v[244:247]\n"
"v_mfma_f32_16x16x32_bf16 v[248:251], a[152:155], a[92:95], v[248:251]\n"
"v_mfma_f32_16x16x32_bf16 v[252:255], a[156:159], a[92:95], v[252:255]\n"
"s_nop 7\n"
  : "=&{v[64:79]}"(acc[0]), "=&{v[80:95]}"(acc[1]), "=&{v[96:111]}"(acc[2]), "=&{v[112:127]}"(acc[3]), "=&{v[128:143]}"(acc[4]), "=&{v[144:159]}"(acc[5]), "=&{v[160:175]}"(acc[6]), "=&{v[176:191]}"(acc[7]), "=&{v[192:207]}"(acc[8]), "=&{v[208:223]}"(acc[9]), "=&{v[224:239]}"(acc[10]), "=&{v[240:255]}"(acc[11]) : [xb] "s"(xb), [wb] "s"(wb), [lw] "s"(lw), [rot] "s"(rot), [tid] "v"(tid), [fb] "s"(fb), [p0] "v"(p0), [p1] "v"(p1), [tag] "s"(tag) : "v32", "v33", "v34", "v35", "v36", "v37", "v38", "v39", "v40", "v41", "v42", "v43", "v44", "v45", "v46", "v47", "v48", "v49", "v50", "v51", "v52", "v53", "v54", "v55", "v56", "v57", "v58", "v59", "v60", "v61", "v62", "v63", "a0", "a1", "a2", "a3", "a4", "a5", "a6", "a7", "a8", "a9", "a10", "a11", "a12", "a13", "a14", "a15", "a16", "a17", "a18", "a19", "a20", "a21", "a22", "a23", "a24", "a25", "a26", "a27", "a28", "a29", "a30", "a31", "a32", "a33", "a34", "a35", "a36", "a37", "a38", "a39", "a40", "a41", "a42", "a43", "a44", "a45", "a46", "a47", "a48", "a49", "a50", "a51", "a52", "a53", "a54", "a55", "a56", "a57", "a58", "a59", "a60", "a61", "a62", "a63", "a64", "a65", "a66", "a67", "a68", "a69", "a70", "a71", "a72", "a73", "a74", "a75", "a76", "a77", "a78", "a79", "a80", "a81", "a82", "a83", "a84", "a85", "a86", "a87", "a88", "a89", "a90", "a91", "a92", "a93", "a94", "a95", "a96", "a97", "a98", "a99", "a100", "a101", "a102", "a103", "a104", "a105", "a106", "a107", "a108", "a109", "a110", "a111", "a112", "a113", "a114", "a115", "a116", "a117", "a118", "a119", "a120", "a121", "a122", "a123", "a124", "a125", "a126", "a127", "a128", "a129", "a130", "a131", "a132", "a133", "a134", "a135", "a136", "a137", "a138", "a139", "a140", "a141", "a142", "a143", "a144", "a145", "a146", "a147", "a148", "a149", "a150", "a151", "a152", "a153", "a154", "a155", "a156", "a157", "a158", "a159", "s20", "s21", "s22", "s23", "s24", "s25", "s26", "s27", "s28", "s29", "s30", "s31", "s32", "s33", "s34", "s35", "s36", "s37", "s38", "s39", "s40", "s41", "s42", "s43", "s44", "s45", "s46", "s47", "s48", "s49", "s50", "s51", "s52", "s53", "s54", "s55", "s56", "s57", "s58", "s59", "s60", "s61", "s62", "s63", "s64", "s65", "s66", "s67", "s68", "s69", "s70", "s71", "memory", "scc", "vcc");
}
DI void gemm_e(f32x16 (&acc)[5], const void* xb, const void* wb, unsigned lw, unsigned rot, unsigned tid, unsigned& t1, unsigned& t2, const void* fb, unsigned p0, unsigned p1, unsigned tag) {
  asm volatile(
"s_mov_b64 s[22:23], %[wb]\n"
"s_mov_b32 s24, %[lw]\n"
"v_and_b32 v32, 63, %[tid]\n"
"v_lshrrev_b32 v34, 3, v32\n"
"v_lshrrev_b32 v35, 2, v34\n"
"v_and_b32 v33, 3, v34\n"
"v_lshl_add_u32 v35, v35, 4, v33\n"
"v_mul_u32_u24 v35, 4096, v35\n"
"v_bfe_u32 v33, v32, 4, 1\n"
"v_lshlrev_b32 v33, 1, v33\n"
"v_and_b32 v34, 7, v32\n"
"v_xor_b32 v33, v33, v34\n"
"v_lshlrev_b32 v33, 4, v33\n"
"v_xor_b32 v34, 64, v33\n"
"v_add_u32 v33, v33, v35\n"
"v_add_u32 v34, v34, v35\n"
"v_add_u32 v44, 0, v33\n"
"v_add_u32 v45, 15360, v34\n"
"v_add_u32 v46, 30720, v33\n"
"v_add_u32 v47, 46080, v34\n"
"s_mov_b64 s[20:21], %[xb]\n"
"s_mov_b32 s25, %[rot]\n"
"v_lshlrev_b32 v52, 4, v32\n"
"v_add_u32 v53, 147456, v52\n"
"v_add_u32 v54, 294912, v52\n"
"v_add_u32 v55, 442368, v52\n"
"v_add_u32 v56, 589824, v52\n"
"s_mul_i32 s32, s25, 131072\n"
"s_lshl_b32 s50, s25, 10\n"
"s_add_u32 s25, s25, 1\n"
"s_cmp_eq_u32 s25, 4\n"
"s_cselect_b32 s25, 0, s25\n"
"s_add_u32 m0, s24, 0\n"
"s_add_u32 s26, s22, s32\n"
"s_addc_u32 s27, s23, 0\n"
"global_load_lds_dwordx4 v44, s[26:27] offset:0\n"
"global_load_lds_dwordx4 v45, s[26:27] offset:1024\n"
"global_load_lds_dwordx4 v46, s[26:27] offset:2048\n"
"global_load_lds_dwordx4 v47, s[26:27] offset:3072\n"
"s_mul_i32 s33, s25, 131072\n"
"s_lshl_b32 s51, s25, 10\n"
"s_add_u32 s25, s25, 1\n"
"s_cmp_eq_u32 s25, 4\n"
"s_cselect_b32 s25, 0, s25\n"
"s_add_u32 m0, s24, 4096\n"
"s_add_u32 s26, s22, s33\n"
"s_addc_u32 s27, s23, 0\n"
"global_load_lds_dwordx4 v44, s[26:27] offset:0\n"
"global_load_lds_dwordx4 v45, s[26:27] offset:1024\n"
"global_load_lds_dwordx4 v46, s[26:27] offset:2048\n"
"global_load_lds_dwordx4 v47, s[26:27] offset:3072\n"
"v_lshrrev_b32 v33, 4, v32\n"
"v_lshlrev_b32 v33, 10, v33\n"
"v_bfe_u32 v34, v32, 2, 2\n"
"v_lshl_add_u32 v33, v34, 7, v33\n"
"v_and_b32 v34, 3, v32\n"
"v_lshl_add_u32 v33, v34, 3, v33\n"
"v_add_u32 v33, s24, v33\n"
"v_bfe_u32 v34, v32, 3, 2\n"
"v_lshrrev_b32 v35, 6, %[tid]\n"
"v_xor_b32 v34, v34, v35\n"
"v_xor_b32 v35, 0, v34\n"
"v_and_b32 v58, 3, v35\n"
"v_lshl_add_u32 v58, v58, 5, v33\n"
"v_lshrrev_b32 v35, 2, v35\n"
"v_lshl_add_u32 v36, v35, 12, v58\n"
"v_xor_b32 v35, 1, v34\n"
"v_and_b32 v58, 3, v35\n"
"v_lshl_add_u32 v58, v58, 5, v33\n"
"v_lshrrev_b32 v35, 2, v35\n"
"v_lshl_add_u32 v37, v35, 12, v58\n"
"v_xor_b32 v35, 2, v34\n"
"v_and_b32 v58, 3, v35\n"
"v_lshl_add_u32 v58, v58, 5, v33\n"
"v_lshrrev_b32 v35, 2, v35\n"
"v_lshl_add_u32 v38, v35, 12, v58\n"
"v_xor_b32 v35, 3, v34\n"
"v_and_b32 v58, 3, v35\n"
"v_lshl_add_u32 v58, v58, 5, v33\n"
"v_lshrrev_b32 v35, 2, v35\n"
"v_lshl_add_u32 v39, v35, 12, v58\n"
"global_load_dword v60, %[p0], %[fb] sc1\n"
"v_mov_b32 v64, 0\n"
"v_mov_b32 v65, 0\n"
"v_mov_b32 v66, 0\n"
"v_mov_b32 v67, 0\n"
"v_mov_b32 v68, 0\n"
"v_mov_b32 v69, 0\n"
"v_mov_b32 v70, 0\n"
"v_mov_b32 v71, 0\n"
"v_mov_b32 v72, 0\n"
"v_mov_b32 v73, 0\n"
"v_mov_b32 v74, 0\n"
"v_mov_b32 v75, 0\n"
"v_mov_b32 v76, 0\n"
"v_mov_b32 v77, 0\n"
"v_mov_b32 v78, 0\n"
"v_mov_b32 v79, 0\n"
"v_mov_b32 v80, 0\n"
"v_mov_b32 v81, 0\n"
"v_mov_b32 v82, 0\n"
"v_mov_b32 v83, 0\n"
"v_mov_b32 v84, 0\n"
"v_mov_b32 v85, 0\n"
"v_mov_b32 v86, 0\n"
"v_mov_b32 v87, 0\n"
"v_mov_b32 v88, 0\n"
"v_mov_b32 v89, 0\n"
"v_mov_b32 v90, 0\n"
"v_mov_b32 v91, 0\n"
"v_mov_b32 v92, 0\n"
"v_mov_b32 v93, 0\n"
"v_mov_b32 v94, 0\n"
"v_mov_b32 v95, 0\n"
"v_mov_b32 v96, 0\n"
"v_mov_b32 v97, 0\n"
"v_mov_b32 v98, 0\n"
"v_mov_b32 v99, 0\n"
"v_mov_b32 v100, 0\n"
"v_mov_b32 v101, 0\n"
"v_mov_b32 v102, 0\n"
"v_mov_b32 v103, 0\n"
"v_mov_b32 v104, 0\n"
"v_mov_b32 v105, 0\n"
"v_mov_b32 v106, 0\n"
"v_mov_b32 v107, 0\n"
"v_mov_b32 v108, 0\n"
"v_mov_b32 v109, 0\n"
"v_mov_b32 v110, 0\n"
"v_mov_b32 v111, 0\n"
"v_mov_b32 v112, 0\n"
"v_mov_b32 v113, 0\n"
"v_mov_b32 v114, 0\n"
"v_mov_b32 v115, 0\n"
"v_mov_b32 v116, 0\n"
"v_mov_b32 v117, 0\n"
"v_mov_b32 v118, 0\n"
"v_mov_b32 v119, 0\n"
"v_mov_b32 v120, 0\n"
"v_mov_b32 v121, 0\n"
"v_mov_b32 v122, 0\n"
"v_mov_b32 v123, 0\n"
"v_mov_b32 v124, 0\n"
"v_mov_b32 v125, 0\n"
"v_mov_b32 v126, 0\n"
"v_mov_b32 v127, 0\n"
"v_mov_b32 v128, 0\n"
"v_mov_b32 v129, 0\n"
"v_mov_b32 v130, 0\n"
"v_mov_b32 v131, 0\n"
"v_mov_b32 v132, 0\n"
"v_mov_b32 v133, 0\n"
"v_mov_b32 v134, 0\n"
"v_mov_b32 v135, 0\n"
"v_mov_b32 v136, 0\n"
"v_mov_b32 v137, 0\n"
"v_mov_b32 v138, 0\n"
"v_mov_b32 v139, 0\n"
"v_mov_b32 v140, 0\n"
"v_mov_b32 v141, 0\n"
"v_mov_b32 v142, 0\n"
"v_mov_b32 v143, 0\n"
"s_mov_b32 s30, 0x40000\n"
"5:\n"
"s_waitcnt vmcnt(0)\n"
"v_xor_b32 v60, %[tag], v60\n"
"v_cmp_ne_u32 vcc, 0, v60\n"
"s_cbranch_vccz 6f\n"
"s_sub_u32 s30, s30, 1\n"
"s_cmp_eq_u32 s30, 0\n"
"s_cbranch_scc1 6f\n"
"s_sleep 1\n"
"global_load_dword v60, %[p0], %[fb] sc1\n"
"s_branch 5b\n"
"6:\n"
"s_add_u32 s28, s20, s50\n"
"s_addc_u32 s29, s21, 0\n"
"global_load_dwordx4 a[0:3], v52, s[28:29] sc1\n"
"global_load_dwordx4 a[4:7], v53, s[28:29] sc1\n"
"global_load_dwordx4 a[8:11], v54, s[28:29] sc1\n"
"global_load_dwordx4 a[12:15], v55, s[28:29] sc1\n"
"global_load_dwordx4 a[16:19], v56, s[28:29] sc1\n"
"s_add_u32 s28, s20, s51\n"
"s_addc_u32 s29, s21, 0\n"
"global_load_dwordx4 a[20:23], v52, s[28:29] sc1\n"
"global_load_dwordx4 a[24:27], v53, s[28:29] sc1\n"
"global_load_dwordx4 a[28:31], v54, s[28:29] sc1\n"
"global_load_dwordx4 a[32:35], v55, s[28:29] sc1\n"
"global_load_dwordx4 a[36:39], v56, s[28:29] sc1\n"
"s_mul_i32 s34, s25, 131072\n"
"s_lshl_b32 s52, s25, 10\n"
"s_add_u32 s25, s25, 1\n"
"s_cmp_eq_u32 s25, 4\n"
"s_cselect_b32 s25, 0, s25\n"
"s_add_u32 m0, s24, 8192\n"
"s_add_u32 s26, s22, s34\n"
"s_addc_u32 s27, s23, 0\n"
"global_load_lds_dwordx4 v44, s[26:27] offset:0\n"
"s_add_u32 s28, s20, s52\n"
"s_addc_u32 s29, s21, 0\n"
"global_load_lds_dwordx4 v45, s[26:27] offset:1024\n"
"global_load_lds_dwordx4 v46, s[26:27] offset:2048\n"
"global_load_lds_dwordx4 v47, s[26:27] offset:3072\n"
"global_load_dwordx4 a[40:43], v52, s[28:29] sc1\n"
"global_load_dwordx4 a[44:47], v53, s[28:29] sc1\n"
"global_load_dwordx4 a[48:51], v54, s[28:29] sc1\n"
"global_load_dwordx4 a[52:55], v55, s[28:29] sc1\n"
"global_load_dwordx4 a[56:59], v56, s[28:29] sc1\n"
"s_mul_i32 s35, s25, 131072\n"
"s_lshl_b32 s53, s25, 10\n"
"s_add_u32 s25, s25, 1\n"
"s_cmp_eq_u32 s25, 4\n"
"s_cselect_b32 s25, 0, s25\n"
"s_add_u32 m0, s24, 12288\n"
"s_add_u32 s26, s22, s35\n"
"s_addc_u32 s27, s23, 0\n"
"global_load_lds_dwordx4 v44, s[26:27] offset:0\n"
"s_add_u32 s28, s20, s53\n"
"s_addc_u32 s29, s21, 0\n"
"global_load_lds_dwordx4 v45, s[26:27] offset:1024\n"
"global_load_lds_dwordx4 v46, s[26:27] offset:2048\n"
"global_load_lds_dwordx4 v47, s[26:27] offset:3072\n"
"global_load_dwordx4 a[60:63], v52, s[28:29] sc1\n"
"global_load_dwordx4 a[64:67], v53, s[28:29] sc1\n"
"global_load_dwordx4 a[68:71], v54, s[28:29] sc1\n"
"global_load_dwordx4 a[72:75], v55, s[28:29] sc1\n"
"global_load_dwordx4 a[76:79], v56, s[28:29] sc1\n"
"ds_read_b64_tr_b16 a[80:81], v36 offset:0\n"
"ds_read_b64_tr_b16 a[82:83], v36 offset:512\n"
"ds_read_b64_tr_b16 a[84:85], v37 offset:0\n"
"ds_read_b64_tr_b16 a[86:87], v37 offset:512\n"
"ds_read_b64_tr_b16 a[88:89], v38 offset:0\n"
"ds_read_b64_tr_b16 a[90:91], v38 offset:512\n"
"ds_read_b64_tr_b16 a[92:93], v39 offset:0\n"
"ds_read_b64_tr_b16 a[94:95], v39 offset:512\n"
"s_mov_b32 s25, 8\n"
".p2align 4\n"
"1:\n"
"s_waitcnt vmcnt(23) lgkmcnt(0)\n"
"s_add_u32 m0, s24, 0\n"
"s_add_u32 s22, s22, 524288\n"
"s_addc_u32 s23, s23, 0\n"
"s_add_u32 s20, s20, 4096\n"
"s_addc_u32 s21, s21, 0\n"
"s_add_u32 s26, s22, s32\n"
"s_addc_u32 s27, s23, 0\n"
"v_mfma_f32_16x16x32_bf16 v[64:67], a[0:3], a[80:83], v[64:67]\n"
"global_load_lds_dwordx4 v44, s[26:27] offset:0\n"
"s_add_u32 s28, s20, s50\n"
"s_addc_u32 s29, s21, 0\n"
"ds_read_b64_tr_b16 a[96:97], v36 offset:4096\n"
"v_mfma_f32_16x16x32_bf16 v[68:71], a[0:3], a[84:87], v[68:71]\n"
"global_load_lds_dwordx4 v45, s[26:27] offset:1024\n"
"v_mfma_f32_16x16x32_bf16 v[72:75], a[0:3], a[88:91], v[72:75]\n"
"global_load_lds_dwordx4 v46, s[26:27] offset:2048\n"
"ds_read_b64_tr_b16 a[98:99], v36 offset:4608\n"
"v_mfma_f32_16x16x32_bf16 v[76:79], a[0:3], a[92:95], v[76:79]\n"
"global_load_lds_dwordx4 v47, s[26:27] offset:3072\n"
"global_load_dwordx4 a[0:3], v52, s[28:29] sc1\n"
"v_mfma_f32_16x16x32_bf16 v[80:83], a[4:7], a[80:83], v[80:83]\n"
"ds_read_b64_tr_b16 a[100:101], v37 offset:4096\n"
"v_mfma_f32_16x16x32_bf16 v[84:87], a[4:7], a[84:87], v[84:87]\n"
"v_mfma_f32_16x16x32_bf16 v[88:91], a[4:7], a[88:91], v[88:91]\n"
"ds_read_b64_tr_b16 a[102:103], v37 offset:4608\n"
"v_mfma_f32_16x16x32_bf16 v[92:95], a[4:7], a[92:95], v[92:95]\n"
"global_load_dwordx4 a[4:7], v53, s[28:29] sc1\n"
"v_mfma_f32_16x16x32_bf16 v[96:99], a[8:11], a[80:83], v[96:99]\n"
"ds_read_b64_tr_b16 a[104:105], v38 offset:4096\n"
"v_mfma_f32_16x16x32_bf16 v[100:103], a[8:11], a[84:87], v[100:103]\n"
"v_mfma_f32_16x16x32_bf16 v[104:107], a[8:11], a[88:91], v[104:107]\n"
"ds_read_b64_tr_b16 a[106:107], v38 offset:4608\n"
"v_mfma_f32_16x16x32_bf16 v[108:111], a[8:11], a[92:95], v[108:111]\n"
"global_load_dwordx4 a[8:11], v54, s[28:29] sc1\n"
"v_mfma_f32_16x16x32_bf16 v[112:115], a[12:15], a[80:83], v[112:115]\n"
"ds_read_b64_tr_b16 a[108:109], v39 offset:4096\n"
"v_mfma_f32_16x16x32_bf16 v[116:119], a[12:15], a[84:87], v[116:119]\n"
"v_mfma_f32_16x16x32_bf16 v[120:123], a[12:15], a[88:91], v[120:123]\n"
"ds_read_b64_tr_b16 a[110:111], v39 offset:4608\n"
"v_mfma_f32_16x16x32_bf16 v[124:127], a[12:15], a[92:95], v[124:127]\n"
"global_load_dwordx4 a[12:15], v55, s[28:29] sc1\n"
"v_mfma_f32_16x16x32_bf16 v[128:131], a[16:19], a[80:83], v[128:131]\n"
"v_mfma_f32_16x16x32_bf16 v[132:135], a[16:19], a[84:87], v[132:135]\n"
"v_mfma_f32_16x16x32_bf16 v[136:139], a[16:19], a[88:91], v[136:139]\n"
"v_mfma_f32_16x16x32_bf16 v[140:143], a[16:19], a[92:95], v[140:143]\n"
"global_load_dwordx4 a[16:19], v56, s[28:29] sc1\n"
"s_waitcnt vmcnt(23) lgkmcnt(0)\n"
"s_add_u32 m0, s24, 4096\n"
"s_add_u32 s26, s22, s33\n"
"s_addc_u32 s27, s23, 0\n"
"v_mfma_f32_16x16x32_bf16 v[64:67], a[20:23], a[96:99], v[64:67]\n"
"global_load_lds_dwordx4 v44, s[26:27] offset:0\n"
"s_add_u32 s28, s20, s51\n"
"s_addc_u32 s29, s21, 0\n"
"ds_read_b64_tr_b16 a[80:81], v36 offset:8192\n"
"v_mfma_f32_16x16x32_bf16 v[68:71], a[20:23], a[100:103], v[68:71]\n"
"global_load_lds_dwordx4 v45, s[26:27] offset:1024\n"
"v_mfma_f32_16x16x32_bf16 v[72:75], a[20:23], a[104:107], v[72:75]\n"
"global_load_lds_dwordx4 v46, s[26:27] offset:2048\n"
"ds_read_b64_tr_b16 a[82:83], v36 offset:8704\n"
"v_mfma_f32_16x16x32_bf16 v[76:79], a[20:23], a[108:111], v[76:79]\n"
"global_load_lds_dwordx4 v47, s[26:27] offset:3072\n"
"global_load_dwordx4 a[20:23], v52, s[28:29] sc1\n"
"v_mfma_f32_16x16x32_bf16 v[80:83], a[24:27], a[96:99], v[80:83]\n"
"ds_read_b64_tr_b16 a[84:85], v37 offset:8192\n"
"v_mfma_f32_16x16x32_bf16 v[84:87], a[24:27], a[100:103], v[84:87]\n"
"v_mfma_f32_16x16x32_bf16 v[88:91], a[24:27], a[104:107], v[88:91]\n"
"ds_read_b64_tr_b16 a[86:87], v37 offset:8704\n"
"v_mfma_f32_16x16x32_bf16 v[92:95], a[24:27], a[108:111], v[92:95]\n"
"global_load_dwordx4 a[24:27], v53, s[28:29] sc1\n"
"v_mfma_f32_16x16x32_bf16 v[96:99], a[28:31], a[96:99], v[96:99]\n"
"ds_read_b64_tr_b16 a[88:89], v38 offset:8192\n"
"v_mfma_f32_16x16x32_bf16 v[100:103], a[28:31], a[100:103], v[100:103]\n"
"v_mfma_f32_16x16x32_bf16 v[104:107], a[28:31], a[104:107], v[104:107]\n"
"ds_read_b64_tr_b16 a[90:91], v38 offset:8704\n"
"v_mfma_f32_16x16x32_bf16 v[108:111], a[28:31], a[108:111], v[108:111]\n"
"global_load_dwordx4 a[28:31], v54, s[28:29] sc1\n"
"v_mfma_f32_16x16x32_bf16 v[112:115], a[32:35], a[96:99], v[112:115]\n"
"ds_read_b64_tr_b16 a[92:93], v39 offset:8192\n"
"v_mfma_f32_16x16x32_bf16 v[116:119], a[32:35], a[100:103], v[116:119]\n"
"v_mfma_f32_16x16x32_bf16 v[120:123], a[32:35], a[104:107], v[120:123]\n"
"ds_read_b64_tr_b16 a[94:95], v39 offset:8704\n"
"v_mfma_f32_16x16x32_bf16 v[124:127], a[32:35], a[108:111], v[124:127]\n"
"global_load_dwordx4 a[32:35], v55, s[28:29] sc1\n"
"v_mfma_f32_16x16x32_bf16 v[128:131], a[36:39], a[96:99], v[128:131]\n"
"v_mfma_f32_16x16x32_bf16 v[132:135], a[36:39], a[100:103], v[132:135]\n"
"v_mfma_f32_16x16x32_bf16 v[136:139], a[36:39], a[104:107], v[136:139]\n"
"v_mfma_f32_16x16x32_bf16 v[140:143], a[36:39], a[108:111], v[140:143]\n"
"global_load_dwordx4 a[36:39], v56, s[28:29] sc1\n"
"s_waitcnt vmcnt(23) lgkmcnt(0)\n"
"s_add_u32 m0, s24, 8192\n"
"s_add_u32 s26, s22, s34\n"
"s_addc_u32 s27, s23, 0\n"
"v_mfma_f32_16x16x32_bf16 v[64:67], a[40:43], a[80:83], v[64:67]\n"
"global_load_lds_dwordx4 v44, s[26:27] offset:0\n"
"s_add_u32 s28, s20, s52\n"
"s_addc_u32 s29, s21, 0\n"
"ds_read_b64_tr_b16 a[96:97], v36 offset:12288\n"
"v_mfma_f32_16x16x32_bf16 v[68:71], a[40:43], a[84:87], v[68:71]\n"
"global_load_lds_dwordx4 v45, s[26:27] offset:1024\n"
"v_mfma_f32_16x16x32_bf16 v[72:75], a[40:43], a[88:91], v[72:75]\n"
"global_load_lds_dwordx4 v46, s[26:27] offset:2048\n"
"ds_read_b64_tr_b16 a[98:99], v36 offset:12800\n"
"v_mfma_f32_16x16x32_bf16 v[76:79], a[40:43], a[92:95], v[76:79]\n"
"global_load_lds_dwordx4 v47, s[26:27] offset:3072\n"
"global_load_dwordx4 a[40:43], v52, s[28:29] sc1\n"
"v_mfma_f32_16x16x32_bf16 v[80:83], a[44:47], a[80:83], v[80:83]\n"
"ds_read_b64_tr_b16 a[100:101], v37 offset:12288\n"
"v_mfma_f32_16x16x32_bf16 v[84:87], a[44:47], a[84:87], v[84:87]\n"
"v_mfma_f32_16x16x32_bf16 v[88:91], a[44:47], a[88:91], v[88:91]\n"
"ds_read_b64_tr_b16 a[102:103], v37 offset:12800\n"
"v_mfma_f32_16x16x32_bf16 v[92:95], a[44:47], a[92:95], v[92:95]\n"
"global_load_dwordx4 a[44:47], v53, s[28:29] sc1\n"
"v_mfma_f32_16x16x32_bf16 v[96:99], a[48:51], a[80:83], v[96:99]\n"
"ds_read_b64_tr_b16 a[104:105], v38 offset:12288\n"
"v_mfma_f32_16x16x32_bf16 v[100:103], a[48:51], a[84:87], v[100:103]\n"
"v_mfma_f32_16x16x32_bf16 v[104:107], a[48:51], a[88:91], v[104:107]\n"
"ds_read_b64_tr_b16 a[106:107], v38 offset:12800\n"
"v_mfma_f32_16x16x32_bf16 v[108:111], a[48:51], a[92:95], v[108:111]\n"
"global_load_dwordx4 a[48:51], v54, s[28:29] sc1\n"
"v_mfma_f32_16x16x32_bf16 v[112:115], a[52:55], a[80:83], v[112:115]\n"
"ds_read_b64_tr_b16 a[108:109], v39 offset:12288\n"
"v_mfma_f32_16x16x32_bf16 v[116:119], a[52:55], a[84:87], v[116:119]\n"
"v_mfma_f32_16x16x32_bf16 v[120:123], a[52:55], a[88:91], v[120:123]\n"
"ds_read_b64_tr_b16 a[110:111], v39 offset:12800\n"
"v_mfma_f32_16x16x32_bf16 v[124:127], a[52:55], a[92:95], v[124:127]\n"
"global_load_dwordx4 a[52:55], v55, s[28:29] sc1\n"
"v_mfma_f32_16x16x32_bf16 v[128:131], a[56:59], a[80:83], v[128:131]\n"
"v_mfma_f32_16x16x32_bf16 v[132:135], a[56:59], a[84:87], v[132:135]\n"
"v_mfma_f32_16x16x32_bf16 v[136:139], a[56:59], a[88:91], v[136:139]\n"
"v_mfma_f32_16x16x32_bf16 v[140:143], a[56:59], a[92:95], v[140:143]\n"
"global_load_dwordx4 a[56:59], v56, s[28:29] sc1\n"
"s_waitcnt vmcnt(23) lgkmcnt(0)\n"
"s_add_u32 m0, s24, 12288\n"
"s_add_u32 s26, s22, s35\n"
"s_addc_u32 s27, s23, 0\n"
"v_mfma_f32_16x16x32_bf16 v[64:67], a[60:63], a[96:99], v[64:67]\n"
"global_load_lds_dwordx4 v44, s[26:27] offset:0\n"
"s_add_u32 s28, s20, s53\n"
"s_addc_u32 s29, s21, 0\n"
"ds_read_b64_tr_b16 a[80:81], v36 offset:0\n"
"v_mfma_f32_16x16x32_bf16 v[68:71], a[60:63], a[100:103], v[68:71]\n"
"global_load_lds_dwordx4 v45, s[26:27] offset:1024\n"
"v_mfma_f32_16x16x32_bf16 v[72:75], a[60:63], a[104:107], v[72:75]\n"
"global_load_lds_dwordx4 v46, s[26:27] offset:2048\n"
"ds_read_b64_tr_b16 a[82:83], v36 offset:512\n"
"v_mfma_f32_16x16x32_bf16 v[76:79], a[60:63], a[108:111], v[76:79]\n"
"global_load_lds_dwordx4 v47, s[26:27] offset:3072\n"
"global_load_dwordx4 a[60:63], v52, s[28:29] sc1\n"
"v_mfma_f32_16x16x32_bf16 v[80:83], a[64:67], a[96:99], v[80:83]\n"
"ds_read_b64_tr_b16 a[84:85], v37 offset:0\n"
"v_mfma_f32_16x16x32_bf16 v[84:87], a[64:67], a[100:103], v[84:87]\n"
"v_mfma_f32_16x16x32_bf16 v[88:91], a[64:67], a[104:107], v[88:91]\n"
"ds_read_b64_tr_b16 a[86:87], v37 offset:512\n"
"v_mfma_f32_16x16x32_bf16 v[92:95], a[64:67], a[108:111], v[92:95]\n"
"global_load_dwordx4 a[64:67], v53, s[28:29] sc1\n"
"v_mfma_f32_16x16x32_bf16 v[96:99], a[68:71], a[96:99], v[96:99]\n"
"ds_read_b64_tr_b16 a[88:89], v38 offset:0\n"
"v_mfma_f32_16x16x32_bf16 v[100:103], a[68:71], a[100:103], v[100:103]\n"
"v_mfma_f32_16x16x32_bf16 v[104:107], a[68:71], a[104:107], v[104:107]\n"
"ds_read_b64_tr_b16 a[90:91], v38 offset:512\n"
"v_mfma_f32_16x16x32_bf16 v[108:111], a[68:71], a[108:111], v[108:111]\n"
"global_load_dwordx4 a[68:71], v54, s[28:29] sc1\n"
"v_mfma_f32_16x16x32_bf16 v[112:115], a[72:75], a[96:99], v[112:115]\n"
"ds_read_b64_tr_b16 a[92:93], v39 offset:0\n"
"v_mfma_f32_16x16x32_bf16 v[116:119], a[72:75], a[100:103], v[116:119]\n"
"v_mfma_f32_16x16x32_bf16 v[120:123], a[72:75], a[104:107], v[120:123]\n"
"ds_read_b64_tr_b16 a[94:95], v39 offset:512\n"
"v_mfma_f32_16x16x32_bf16 v[124:127], a[72:75], a[108:111], v[124:127]\n"
"global_load_dwordx4 a[72:75], v55, s[28:29] sc1\n"
"v_mfma_f32_16x16x32_bf16 v[128:131], a[76:79], a[96:99], v[128:131]\n"
"v_mfma_f32_16x16x32_bf16 v[132:135], a[76:79], a[100:103], v[132:135]\n"
"v_mfma_f32_16x16x32_bf16 v[136:139], a[76:79], a[104:107], v[136:139]\n"
"v_mfma_f32_16x16x32_bf16 v[140:143], a[76:79], a[108:111], v[140:143]\n"
"global_load_dwordx4 a[76:79], v56, s[28:29] sc1\n"
"s_sub_u32 s25, s25, 1\n"
"s_cmp_lg_u32 s25, 0\n"
"s_cbranch_scc1 1b\n"
"s_waitcnt vmcnt(23) lgkmcnt(0)\n"
"v_mfma_f32_16x16x32_bf16 v[64:67], a[0:3], a[80:83], v[64:67]\n"
"ds_read_b64_tr_b16 a[96:97], v36 offset:4096\n"
"v_mfma_f32_16x16x32_bf16 v[68:71], a[0:3], a[84:87], v[68:71]\n"
"v_mfma_f32_16x16x32_bf16 v[72:75], a[0:3], a[88:91], v[72:75]\n"
"ds_read_b64_tr_b16 a[98:99], v36 offset:4608\n"
"v_mfma_f32_16x16x32_bf16 v[76:79], a[0:3], a[92:95], v[76:79]\n"
"v_mfma_f32_16x16x32_bf16 v[80:83], a[4:7], a[80:83], v[80:83]\n"
"ds_read_b64_tr_b16 a[100:101], v37 offset:4096\n"
"v_mfma_f32_16x16x32_bf16 v[84:87], a[4:7], a[84:87], v[84:87]\n"
"v_mfma_f32_16x16x32_bf16 v[88:91], a[4:7], a[88:91], v[88:91]\n"
"ds_read_b64_tr_b16 a[102:103], v37 offset:4608\n"
"v_mfma_f32_16x16x32_bf16 v[92:95], a[4:7], a[92:95], v[92:95]\n"
"v_mfma_f32_16x16x32_bf16 v[96:99], a[8:11], a[80:83], v[96:99]\n"
"ds_read_b64_tr_b16 a[104:105], v38 offset:4096\n"
"v_mfma_f32_16x16x32_bf16 v[100:103], a[8:11], a[84:87], v[100:103]\n"
"v_mfma_f32_16x16x32_bf16 v[104:107], a[8:11], a[88:91], v[104:107]\n"
"ds_read_b64_tr_b16 a[106:107], v38 offset:4608\n"
"v_mfma_f32_16x16x32_bf16 v[108:111], a[8:11], a[92:95], v[108:111]\n"
"v_mfma_f32_16x16x32_bf16 v[112:115], a[12:15], a[80:83], v[112:115]\n"
"ds_read_b64_tr_b16 a[108:109], v39 offset:4096\n"
"v_mfma_f32_16x16x32_bf16 v[116:119], a[12:15], a[84:87], v[116:119]\n"
"v_mfma_f32_16x16x32_bf16 v[120:123], a[12:15], a[88:91], v[120:123]\n"
"ds_read_b64_tr_b16 a[110:111], v39 offset:4608\n"
"v_mfma_f32_16x16x32_bf16 v[124:127], a[12:15], a[92:95], v[124:127]\n"
"v_mfma_f32_16x16x32_bf16 v[128:131], a[16:19], a[80:83], v[128:131]\n"
"v_mfma_f32_16x16x32_bf16 v[132:135], a[16:19], a[84:87], v[132:135]\n"
"v_mfma_f32_16x16x32_bf16 v[136:139], a[16:19], a[88:91], v[136:139]\n"
"v_mfma_f32_16x16x32_bf16 v[140:143], a[16:19], a[92:95], v[140:143]\n"
"s_waitcnt vmcnt(14) lgkmcnt(0)\n"
"v_mfma_f32_16x16x32_bf16 v[64:67], a[20:23], a[96:99], v[64:67]\n"
"ds_read_b64_tr_b16 a[80:81], v36 offset:8192\n"
"v_mfma_f32_16x16x32_bf16 v[68:71], a[20:23], a[100:103], v[68:71]\n"
"v_mfma_f32_16x16x32_bf16 v[72:75], a[20:23], a[104:107], v[72:75]\n"
"ds_read_b64_tr_b16 a[82:83], v36 offset:8704\n"
"v_mfma_f32_16x16x32_bf16 v[76:79], a[20:23], a[108:111], v[76:79]\n"
"v_mfma_f32_16x16x32_bf16 v[80:83], a[24:27], a[96:99], v[80:83]\n"
"ds_read_b64_tr_b16 a[84:85], v37 offset:8192\n"
"v_mfma_f32_16x16x32_bf16 v[84:87], a[24:27], a[100:103], v[84:87]\n"
"v_mfma_f32_16x16x32_bf16 v[88:91], a[24:27], a[104:107], v[88:91]\n"
"ds_read_b64_tr_b16 a[86:87], v37 offset:8704\n"
"v_mfma_f32_16x16x32_bf16 v[92:95], a[24:27], a[108:111], v[92:95]\n"
"v_mfma_f32_16x16x32_bf16 v[96:99], a[28:31], a[96:99], v[96:99]\n"
"ds_read_b64_tr_b16 a[88:89], v38 offset:8192\n"
"v_mfma_f32_16x16x32_bf16 v[100:103], a[28:31], a[100:103], v[100:103]\n"
"v_mfma_f32_16x16x32_bf16 v[104:107], a[28:31], a[104:107], v[104:107]\n"
"ds_read_b64_tr_b16 a[90:91], v38 offset:8704\n"
"v_mfma_f32_16x16x32_bf16 v[108:111], a[28:31], a[108:111], v[108:111]\n"
"v_mfma_f32_16x16x32_bf16 v[112:115], a[32:35], a[96:99], v[112:115]\n"
"ds_read_b64_tr_b16 a[92:93], v39 offset:8192\n"
"v_mfma_f32_16x16x32_bf16 v[116:119], a[32:35], a[100:103], v[116:119]\n"
"v_mfma_f32_16x16x32_bf16 v[120:123], a[32:35], a[104:107], v[120:123]\n"
"ds_read_b64_tr_b16 a[94:95], v39 offset:8704\n"
"v_mfma_f32_16x16x32_bf16 v[124:127], a[32:35], a[108:111], v[124:127]\n"
"v_mfma_f32_16x16x32_bf16 v[128:131], a[36:39], a[96:99], v[128:131]\n"
"v_mfma_f32_16x16x32_bf16 v[132:135], a[36:39], a[100:103], v[132:135]\n"
"v_mfma_f32_16x16x32_bf16 v[136:139], a[36:39], a[104:107], v[136:139]\n"
"v_mfma_f32_16x16x32_bf16 v[140:143], a[36:39], a[108:111], v[140:143]\n"
"s_waitcnt vmcnt(5) lgkmcnt(0)\n"
"v_mfma_f32_16x16x32_bf16 v[64:67], a[40:43], a[80:83], v[64:67]\n"
"ds_read_b64_tr_b16 a[96:97], v36 offset:12288\n"
"v_mfma_f32_16x16x32_bf16 v[68:71], a[40:43], a[84:87], v[68:71]\n"
"v_mfma_f32_16x16x32_bf16 v[72:75], a[40:43], a[88:91], v[72:75]\n"
"ds_read_b64_tr_b16 a[98:99], v36 offset:12800\n"
"v_mfma_f32_16x16x32_bf16 v[76:79], a[40:43], a[92:95], v[76:79]\n"
"v_mfma_f32_16x16x32_bf16 v[80:83], a[44:47], a[80:83], v[80:83]\n"
"ds_read_b64_tr_b16 a[100:101], v37 offset:12288\n"
"v_mfma_f32_16x16x32_bf16 v[84:87], a[44:47], a[84:87], v[84:87]\n"
"v_mfma_f32_16x16x32_bf16 v[88:91], a[44:47], a[88:91], v[88:91]\n"
"ds_read_b64_tr_b16 a[102:103], v37 offset:12800\n"
"v_mfma_f32_16x16x32_bf16 v[92:95], a[44:47], a[92:95], v[92:95]\n"
"v_mfma_f32_16x16x32_bf16 v[96:99], a[48:51], a[80:83], v[96:99]\n"
"ds_read_b64_tr_b16 a[104:105], v38 offset:12288\n"
"v_mfma_f32_16x16x32_bf16 v[100:103], a[48:51], a[84:87], v[100:103]\n"
"v_mfma_f32_16x16x32_bf16 v[104:107], a[48:51], a[88:91], v[104:107]\n"
"ds_read_b64_tr_b16 a[106:107], v38 offset:12800\n"
"v_mfma_f32_16x16x32_bf16 v[108:111], a[48:51], a[92:95], v[108:111]\n"
"v_mfma_f32_16x16x32_bf16 v[112:115], a[52:55], a[80:83], v[112:115]\n"
"ds_read_b64_tr_b16 a[108:109], v39 offset:12288\n"
"v_mfma_f32_16x16x32_bf16 v[116:119], a[52:55], a[84:87], v[116:119]\n"
"v_mfma_f32_16x16x32_bf16 v[120:123], a[52:55], a[88:91], v[120:123]\n"
"ds_read_b64_tr_b16 a[110:111], v39 offset:12800\n"
"v_mfma_f32_16x16x32_bf16 v[124:127], a[52:55], a[92:95], v[124:127]\n"
"v_mfma_f32_16x16x32_bf16 v[128:131], a[56:59], a[80:83], v[128:131]\n"
"v_mfma_f32_16x16x32_bf16 v[132:135], a[56:59], a[84:87], v[132:135]\n"
"v_mfma_f32_16x16x32_bf16 v[136:139], a[56:59], a[88:91], v[136:139]\n"
"v_mfma_f32_16x16x32_bf16 v[140:143], a[56:59], a[92:95], v[140:143]\n"
"s_waitcnt vmcnt(0) lgkmcnt(0)\n"
"v_mfma_f32_16x16x32_bf16 v[64:67], a[60:63], a[96:99], v[64:67]\n"
"v_mfma_f32_16x16x32_bf16 v[68:71], a[60:63], a[100:103], v[68:71]\n"
"v_mfma_f32_16x16x32_bf16 v[72:75], a[60:63], a[104:107], v[72:75]\n"
"v_mfma_f32_16x16x32_bf16 v[76:79], a[60:63], a[108:111], v[76:79]\n"
"v_mfma_f32_16x16x32_bf16 v[80:83], a[64:67], a[96:99], v[80:83]\n"
"v_mfma_f32_16x16x32_bf16 v[84:87], a[64:67], a[100:103], v[84:87]\n"
"v_mfma_f32_16x16x32_bf16 v[88:91], a[64:67], a[104:107], v[88:91]\n"
"v_mfma_f32_16x16x32_bf16 v[92:95], a[64:67], a[108:111], v[92:95]\n"
"v_mfma_f32_16x16x32_bf16 v[96:99], a[68:71], a[96:99], v[96:99]\n"
"v_mfma_f32_16x16x32_bf16 v[100:103], a[68:71], a[100:103], v[100:103]\n"
"v_mfma_f32_16x16x32_bf16 v[104:107], a[68:71], a[104:107], v[104:107]\n"
"v_mfma_f32_16x16x32_bf16 v[108:111], a[68:71], a[108:111], v[108:111]\n"
"v_mfma_f32_16x16x32_bf16 v[112:115], a[72:75], a[96:99], v[112:115]\n"
"v_mfma_f32_16x16x32_bf16 v[116:119], a[72:75], a[100:103], v[116:119]\n"
"v_mfma_f32_16x16x32_bf16 v[120:123], a[72:75], a[104:107], v[120:123]\n"
"v_mfma_f32_16x16x32_bf16 v[124:127], a[72:75], a[108:111], v[124:127]\n"
"v_mfma_f32_16x16x32_bf16 v[128:131], a[76:79], a[96:99], v[128:131]\n"
"v_mfma_f32_16x16x32_bf16 v[132:135], a[76:79], a[100:103], v[132:135]\n"
"v_mfma_f32_16x16x32_bf16 v[136:139], a[76:79], a[104:107], v[136:139]\n"
"v_mfma_f32_16x16x32_bf16 v[140:143], a[76:79], a[108:111], v[140:143]\n"
"s_nop 7\n"
  : "=&{v[64:79]}"(acc[0]), "=&{v[80:95]}"(acc[1]), "=&{v[96:111]}"(acc[2]), "=&{v[112:127]}"(acc[3]), "=&{v[128:143]}"(acc[4]) : [xb] "s"(xb), [wb] "s"(wb), [lw] "s"(lw), [rot] "s"(rot), [tid] "v"(tid), [fb] "s"(fb), [p0] "v"(p0), [p1] "v"(p1), [tag] "s"(tag) : "v32", "v33", "v34", "v35", "v36", "v37", "v38", "v39", "v40", "v41", "v42", "v43", "v44", "v45", "v46", "v47", "v48", "v49", "v50", "v51", "v52", "v53", "v54", "v55", "v56", "v57", "v58", "v59", "v60", "v61", "v62", "v63", "a0", "a1", "a2", "a3", "a4", "a5", "a6", "a7", "a8", "a9", "a10", "a11", "a12", "a13", "a14", "a15", "a16", "a17", "a18", "a19", "a20", "a21", "a22", "a23", "a24", "a25", "a26", "a27", "a28", "a29", "a30", "a31", "a32", "a33", "a34", "a35", "a36", "a37", "a38", "a39", "a40", "a41", "a42", "a43", "a44", "a45", "a46", "a47", "a48", "a49", "a50", "a51", "a52", "a53", "a54", "a55", "a56", "a57", "a58", "a59", "a60", "a61", "a62", "a63", "a64", "a65", "a66", "a67", "a68", "a69", "a70", "a71", "a72", "a73", "a74", "a75", "a76", "a77", "a78", "a79", "a80", "a81", "a82", "a83", "a84", "a85", "a86", "a87", "a88", "a89", "a90", "a91", "a92", "a93", "a94", "a95", "a96", "a97", "a98", "a99", "a100", "a101", "a102", "a103", "a104", "a105", "a106", "a107", "a108", "a109", "a110", "a111", "s20", "s21", "s22", "s23", "s24", "s25", "s26", "s27", "s28", "s29", "s30", "s31", "s32", "s33", "s34", "s35", "s36", "s37", "s38", "s39", "s40", "s41", "s42", "s43", "s44", "s45", "s46", "s47", "s48", "s49", "s50", "s51", "s52", "s53", "s54", "s55", "s56", "s57", "s58", "s59", "s60", "s61", "s62", "s63", "s64", "s65", "s66", "s67", "s68", "s69", "s70", "s71", "memory", "scc", "vcc");
}
constexpr int RC = 4, RD = 4, RE = 4;
#define ROTC ((unsigned)(mg * (RC / 4) + ng * (RC / 8)) % RC)
#define ROTD ((unsigned)(ng * (RD / 4) + (mg * RD) / 6) % RD)
#define ROTE ((unsigned)(ng * (RE / 4) + (mg * RE) / 8) % RE)
#define STAMP0 unsigned t1, t2, rt
#define STAMP1(k)
#define XS(x) (x)
#define WS(x) (x)
template <int TM, int TN, int REG, int NQ>
DI void reduce_waves(const f32x16 (&acc)[NQ], f32x4 (&r)[TM * TN / 4], char* lds, int w, int lane) {
  constexpr int OW = TN / 4;
  LAS char* mine = (LAS char*)lds + w * REG + lane * 16;
#define TILE(t) f32x4{acc[(t) >> 2][((t) & 3) * 4], acc[(t) >> 2][((t) & 3) * 4 + 1], acc[(t) >> 2][((t) & 3) * 4 + 2], acc[(t) >> 2][((t) & 3) * 4 + 3]}
#pragma unroll
  for (int i = 0; i < TM; ++i)
#pragma unroll
    for (int p = OW; p < TN; ++p) *(LAS f32x4*)(mine + (i * (TN - OW) + p - OW) * 1024) = TILE(i * TN + p);
  asm volatile("s_waitcnt lgkmcnt(0)\n s_barrier" ::: "memory");
  f32x4 v[3][TM * OW];
#pragma unroll
  for (int i = 0; i < TM; ++i)
#pragma unroll
    for (int p = 0; p < OW; ++p)
#pragma unroll
      for (int x = 1; x < 4; ++x) v[x - 1][i * OW + p] = *(LAS const f32x4*)((LAS const char*)lds + (w ^ x) * REG + lane * 16 + (i * (TN - OW) + (x * OW + p) - OW) * 1024);
  __builtin_amdgcn_sched_barrier(0);
#pragma unroll
  for (int i = 0; i < TM; ++i)
#pragma unroll
    for (int p = 0; p < OW; ++p) r[i * OW + p] = (TILE(i * TN + p) + v[0][i * OW + p]) + (v[1][i * OW + p] + v[2][i * OW + p]);
#undef TILE
}
DI f32x4 unpk(u32x2 v) { return f32x4{lo16(v[0]), hi16(v[0]), lo16(v[1]), hi16(v[1])}; }
DI u32x2 pk4(f32x4 v) { return u32x2{cvtpk(v[0], v[1]), cvtpk(v[2], v[3])}; }
DI f32x4 gelu4(f32x4 u) {
  const f32x4 a = u * ((u * u) * -0.1029432397f - 2.302208198f);
  const f32x4 d = f32x4{__builtin_amdgcn_exp2f(a[0]), __builtin_amdgcn_exp2f(a[1]), __builtin_amdgcn_exp2f(a[2]), __builtin_amdgcn_exp2f(a[3])} + 1.f;
  return u * f32x4{__builtin_amdgcn_rcpf(d[0]), __builtin_amdgcn_rcpf(d[1]), __builtin_amdgcn_rcpf(d[2]), __builtin_amdgcn_rcpf(d[3])};
}
DI float ln_center(f32x4 (&v)[6], bool act) {
  const f32x4 s4 = ((v[0] + v[1]) + (v[2] + v[3])) + (v[4] + v[5]);
  const float mean = sum32(act ? (s4[0] + s4[1]) + (s4[2] + s4[3]) : 0.f) * (1.f / M);
  f32x4 q4 = {0.f, 0.f, 0.f, 0.f};
#pragma unroll
  for (int i = 0; i < 6; ++i) { v[i] -= mean; q4 += v[i] * v[i]; }
  return __builtin_amdgcn_rsqf(sum32(act ? (q4[0] + q4[1]) + (q4[2] + q4[3]) : 0.f) * (1.f / M) + 1e-5f);
}
#define SLP 1
typedef __attribute__((address_space(1))) unsigned gu32;
#define RSRC(p, n) __builtin_amdgcn_make_buffer_rsrc((void*)(p), (short)0, (int)(n), 0x00020000)
DI void publish(unsigned* f, unsigned tag) {
  asm volatile("s_waitcnt vmcnt(0) lgkmcnt(0)\n s_barrier" ::: "memory");
  if (threadIdx.x == 0) __hip_atomic_store((gu32*)f, tag, __ATOMIC_RELAXED, __HIP_MEMORY_SCOPE_AGENT);
}
DI void wait_flag(const unsigned* f, unsigned tag) {
  for (int i = 0; i < 0x40000; ++i) { if (__all(__hip_atomic_load((gu32*)f, __ATOMIC_RELAXED, __HIP_MEMORY_SCOPE_AGENT) == tag)) break; __builtin_amdgcn_s_sleep(SLP); }
  __builtin_amdgcn_fence(__ATOMIC_ACQUIRE, "wavefront");
}
DI void bar() { asm volatile("s_waitcnt lgkmcnt(0)\n s_barrier" ::: "memory"); }
constexpr int FS = 32, FX1 = 2048, FH3 = FX1 + 256 * FS, FU = FH3 + 256 * FS, FCT = FU + 256 * FS;
#define BST(i)
DI void back_half(char* lds, const u16* x, const u16* ctx, const u16* wo, const u16* w1, const u16* b1, const u16* w2, const u16* b2, const float* mod, u16* x1, u16* h3T, u16* u, u16* out, unsigned* flags, unsigned tag) {
  const int b = blockIdx.x, xcd = b & 7, idx = b >> 3;
  const int tid = threadIdx.x, lane = tid & 63, w = __builtin_amdgcn_readfirstlane(tid >> 6);
  const unsigned lw = (unsigned)(size_t)lds + w * 40960;
  unsigned t1, t2; unsigned bsv = 0;
  BST(0);
  if (idx < 30 && xcd * 30 + idx < 234) {
    const int L = xcd * 30 + idx, mg = L / 9, ng = L - mg * 9, mt0 = mg < 25 ? mg * 5 : 123;
    const int m = ng * 64 + w * 16 + (lane >> 4) * 4, s0 = mt0 * 16 + (lane & 15);
    f32x4 ga = *(const f32x4*)(mod + 2 * M + m);
    u32x2 xr[5];
#pragma unroll
    for (int i = 0; i < 5; ++i) xr[i] = *(const u32x2*)(x + (size_t)(s0 + 16 * i) * M + m);
    f32x16 acc[5];
    { const int qb = (mt0 + (lane & 8 ? 4 : 0)) >> 3, j = qb < 8 ? qb : 15 - qb, h = 8 * w + (lane & 7);
      gemm_c(acc, (const char*)ctx + ((size_t)mt0 * 128 + w * 32) * 1024, (const char*)wo + (size_t)w * 1024 * 1152 + ng * 128, lw, ROTC, tid, t1, t2, flags + FCT, (h * 8 + j) * FS * 4, 0, tag); }
    BST(1);
    asm volatile("" : "+v"(ga), "+v"(xr[0]), "+v"(xr[1]), "+v"(xr[2]), "+v"(xr[3]), "+v"(xr[4]));
    f32x4 r[5];
    reduce_waves<5, 4, 40960>(acc, r, lds, w, lane);
    BST(9);
    const auto rx = RSRC(x1, S * M * 2);
#pragma unroll
    for (int i = 0; i < 5; ++i) if (mg < 25 || i >= 2) __builtin_amdgcn_raw_buffer_store_b64(pk4(unpk(xr[i]) + ga * r[i]), rx, (s0 * M + m) * 2, i * 16 * M * 2, 16);
    BST(10);
    publish(flags + FX1 + L * FS, tag);
  }
  BST(2);
  { LAS char* tl = (LAS char*)(lds + 16384);
    const int c = lane & 31, cc = c < 24 ? c : 23, r = tid >> 5, row = b * 8 + r, rt = b >> 1;
    f32x4 ma[6], mb[6];
#pragma unroll
    for (int i = 0; i < 3; ++i) { ma[2 * i] = *(const f32x4*)(mod + 4 * M + cc * 8 + 192 * i); ma[2 * i + 1] = *(const f32x4*)(mod + 4 * M + cc * 8 + 192 * i + 4);
      mb[2 * i] = *(const f32x4*)(mod + 3 * M + cc * 8 + 192 * i); mb[2 * i + 1] = *(const f32x4*)(mod + 3 * M + cc * 8 + 192 * i + 4); }
    wait_flag(flags + FX1 + ((rt / 5) * 9 + min(lane, 8)) * FS, tag);
    BST(3);
    const auto rx = RSRC(x1, S * M * 2);
    u32x4 xv[3];
#pragma unroll
    for (int i = 0; i < 3; ++i) xv[i] = __builtin_amdgcn_raw_buffer_load_b128(rx, (row * M + cc * 8 + 192 * i) * 2, 0, 16);
    BST(11);
    f32x4 v[6];
#pragma unroll
    for (int i = 0; i < 6; ++i) v[i] = unpk(u32x2{xv[i >> 1][(i & 1) * 2], xv[i >> 1][(i & 1) * 2 + 1]});
    float rs = ln_center(v, c < 24);
#pragma unroll
    for (int i = 0; i < 6; ++i) v[i] = (v[i] * rs) * ma[i] + mb[i];
    rs = ln_center(v, c < 24);
#pragma unroll
    for (int i = 0; i < 6; ++i) v[i] *= rs;
#pragma unroll
    for (int i = 0; i < 24; i += 2) { const unsigned p = cvtpk(v[i >> 2][i & 3], v[i >> 2][(i & 3) + 1]);
      auto sw = __builtin_amdgcn_permlane32_swap(p, p, false, false);
      const unsigned o = __builtin_amdgcn_perm(sw[1], sw[0], lane < 32 ? 0x05040100u : 0x07060302u);
      *(LAS unsigned*)(tl + (cc + 24 * (i >> 3)) * 144 + ((i & 7) + (lane >> 5)) * 16 + w * 4) = o; }
    BST(12);
    bar();
    const auto rh = RSRC(h3T, S * M * 2);
#pragma unroll
    for (int d = tid; d < M; d += 256) __builtin_amdgcn_raw_buffer_store_b128(*(LAS const u32x4*)(tl + (d >> 3) * 144 + (d & 7) * 16), rh, ((d >> 4) * 64 + (b >> 2)) * 1024 + ((d & 15) + 16 * (b & 3)) * 16, 0, 16);
    BST(13);
    publish(flags + FH3 + b * FS, tag);
  }
  BST(4);
  if (idx < 27) {
    const int L = xcd * 27 + idx, ng = L / 6, mg = L - ng * 6;
    const int f = ng * 128 + w * 32 + (lane >> 4) * 4;
    u32x2 bA = *(const u32x2*)(b1 + f), bB = *(const u32x2*)(b1 + f + 16);
    f32x16 acc[12];
    gemm_d(acc, (const char*)h3T + ((size_t)mg * 6 * 64 + w * 16) * 1024, (const char*)w1 + (size_t)w * 512 * 9216 + ng * 256, lw, ROTD, tid, t1, t2, flags + FH3, (w * 64 + lane) * FS * 4, 0, tag);
    BST(5);
    asm volatile("" : "+v"(bA), "+v"(bB));
    f32x4 r[12];
    reduce_waves<6, 8, 40960>(acc, r, lds, w, lane);
    BST(14);
    const auto ru = RSRC(u, M * F * 2);
    const f32x4 b0 = unpk(bA), b1v = unpk(bB);
#pragma unroll
    for (int i = 0; i < 6; ++i) { const u32x2 lo = pk4(gelu4(r[2 * i] + b0)), hi = pk4(gelu4(r[2 * i + 1] + b1v));
      __builtin_amdgcn_raw_buffer_store_b128(u32x4{lo[0], lo[1], hi[0], hi[1]}, ru, ((mg * 6 + i) * 144 + ng * 4 + w) * 1024 + lane * 16, 0, 16); }
    BST(15);
    publish(flags + FU + L * FS, tag);
  }
  BST(6);
  { const int L = xcd * 32 + idx, ng = L >> 3, mg = L & 7, mt0 = mg < 7 ? mg * 5 : 31;
    f32x16 acc[5];
    const unsigned fo = ((w * 9 + min(lane & 15, 8)) * 6 + (mt0 + (lane & 16 ? 4 : 0)) / 6) * FS * 4;
    gemm_e(acc, (const char*)u + ((size_t)mt0 * 144 + w * 36) * 1024, (const char*)w2 + (size_t)w * 1152 * 4096 + ng * 128, lw, ROTE, tid, t1, t2, flags + FU, fo, 0, tag);
    BST(7);
    const int s = ng * 64 + w * 16 + (lane & 15), d0 = mt0 * 16 + (lane >> 4) * 4;
    const float bb = bf2f(b2[s]);
    const auto rx = RSRC(x1, S * M * 2);
    u32x2 xr[5]; f32x4 gb[5], r[5];
#pragma unroll
    for (int i = 0; i < 5; ++i) { xr[i] = __builtin_amdgcn_raw_buffer_load_b64(rx, (s * M + d0) * 2, 32 * i, 16); gb[i] = *(const f32x4*)(mod + 5 * M + d0 + 16 * i); }
    reduce_waves<5, 4, 40960>(acc, r, lds, w, lane);
    u32x2 o[5];
#pragma unroll
    for (int i = 0; i < 5; ++i) o[i] = pk4(unpk(xr[i]) + gb[i] * (r[i] + bb));
    __builtin_amdgcn_sched_barrier(0);
#pragma unroll
    for (int i = 0; i < 5; ++i) if (mg < 7 || i == 4) *(u32x2*)(out + (size_t)s * M + d0 + 16 * i) = o[i];
  }
  BST(8);
}
DI void pre_asm(const void* ab, const void* bb, unsigned wid, unsigned tid) {
  asm volatile(
"s_mov_b64 s[20:21], %[ab]\n"
"s_mov_b64 s[22:23], %[bb]\n"
"s_lshl_b32 s24, %[wid], 13\n"
"s_add_u32 m0, s24, 4096\n"
"v_and_b32 v2, 63, %[tid]\n"
"v_lshrrev_b32 v3, 3, v2\n"
"v_and_b32 v4, 7, v2\n"
"v_lshrrev_b32 v5, 4, v2\n"
"v_xor_b32 v6, v4, v5\n"
"v_lshlrev_b32 v6, 4, v6\n"
"v_lshl_add_u32 v6, v3, 10, v6\n"
"v_xor_b32 v7, 64, v6\n"
"v_add_u32 v8, 4096, v6\n"
"v_add_u32 v9, 11264, v7\n"
"v_add_u32 v10, 18432, v6\n"
"v_add_u32 v11, 25600, v7\n"
"v_add_u32 v12, 32768, v6\n"
"v_add_u32 v13, 39936, v7\n"
"v_add_u32 v14, 47104, v6\n"
"v_add_u32 v15, 54272, v7\n"
"v_and_b32 v6, 1, v5\n"
"v_lshlrev_b32 v6, 1, v6\n"
"v_xor_b32 v6, v6, v4\n"
"v_lshlrev_b32 v6, 4, v6\n"
"v_lshl_add_u32 v6, v3, 13, v6\n"
"v_xor_b32 v7, 64, v6\n"
"v_add_u32 v16, 4096, v6\n"
"v_add_u32 v17, 68608, v7\n"
"v_add_u32 v18, 133120, v6\n"
"v_add_u32 v19, 197632, v7\n"
"v_add_u32 v20, 262144, v6\n"
"v_add_u32 v21, 326656, v7\n"
"v_add_u32 v22, 391168, v6\n"
"v_add_u32 v23, 455680, v7\n"
"global_load_lds_dwordx4 v8, s[20:21] offset:-4096\n"
"global_load_lds_dwordx4 v9, s[20:21] offset:-3072\n"
"global_load_lds_dwordx4 v10, s[20:21] offset:-2048\n"
"global_load_lds_dwordx4 v11, s[20:21] offset:-1024\n"
"global_load_lds_dwordx4 v12, s[20:21] offset:0\n"
"global_load_lds_dwordx4 v13, s[20:21] offset:1024\n"
"global_load_lds_dwordx4 v14, s[20:21] offset:2048\n"
"global_load_lds_dwordx4 v15, s[20:21] offset:3072\n"
"s_add_u32 m0, s24, 0x11000\n"
"s_nop 0\n"
"global_load_lds_dwordx4 v16, s[22:23] offset:-4096\n"
"global_load_lds_dwordx4 v17, s[22:23] offset:-3072\n"
"global_load_lds_dwordx4 v18, s[22:23] offset:-2048\n"
"global_load_lds_dwordx4 v19, s[22:23] offset:-1024\n"
"global_load_lds_dwordx4 v20, s[22:23] offset:0\n"
"global_load_lds_dwordx4 v21, s[22:23] offset:1024\n"
"global_load_lds_dwordx4 v22, s[22:23] offset:2048\n"
"global_load_lds_dwordx4 v23, s[22:23] offset:3072\n"
  :: [ab] "s"(ab), [bb] "s"(bb), [wid] "s"(wid), [tid] "v"(tid) : "v2", "v3", "v4", "v5", "v6", "v7", "v8", "v9", "v10", "v11", "v12", "v13", "v14", "v15", "v16", "v17", "v18", "v19", "v20", "v21", "v22", "v23", "s20", "s21", "s22", "s23", "s24", "scc", "memory");
}
DI void f_asm(const void* ab, const void* bb, const void* wq, const void* fh, const void* hl, const void* hs, const void* fp, const void* kvb, const void* fk, const void* fq, const void* kv, const void* cl, const void* cs, const void* pf, unsigned fd, unsigned ntl, unsigned rot, unsigned wid, unsigned tid, unsigned& ts) {
  asm volatile(
"s_mov_b64 s[20:21], %[ab]\n"
"s_mov_b64 s[22:23], %[bb]\n"
"s_mov_b32 s27, %[wid]\n"
"s_mov_b32 s30, %[rot]\n"
"s_mov_b32 s31, %[rot]\n"
"s_mov_b64 s[58:59], %[wq]\n"
"s_mov_b64 s[60:61], %[fh]\n"
"v_and_b32 v2, 63, %[tid]\n"
"v_lshrrev_b32 v3, 3, v2\n"
"v_and_b32 v4, 7, v2\n"
"v_lshrrev_b32 v5, 4, v2\n"
"v_xor_b32 v6, v4, v5\n"
"v_lshlrev_b32 v6, 4, v6\n"
"v_lshl_add_u32 v6, v3, 10, v6\n"
"v_xor_b32 v7, 64, v6\n"
"v_add_u32 v8, 4096, v6\n"
"v_add_u32 v9, 11264, v7\n"
"v_add_u32 v10, 18432, v6\n"
"v_add_u32 v11, 25600, v7\n"
"v_add_u32 v12, 32768, v6\n"
"v_add_u32 v13, 39936, v7\n"
"v_add_u32 v14, 47104, v6\n"
"v_add_u32 v15, 54272, v7\n"
"v_and_b32 v6, 1, v5\n"
"v_lshlrev_b32 v6, 1, v6\n"
"v_xor_b32 v6, v6, v4\n"
"v_lshlrev_b32 v6, 4, v6\n"
"v_lshl_add_u32 v6, v3, 13, v6\n"
"v_xor_b32 v7, 64, v6\n"
"v_add_u32 v16, 4096, v6\n"
"v_add_u32 v17, 68608, v7\n"
"v_add_u32 v18, 133120, v6\n"
"v_add_u32 v19, 197632, v7\n"
"v_add_u32 v20, 262144, v6\n"
"v_add_u32 v21, 326656, v7\n"
"v_add_u32 v22, 391168, v6\n"
"v_add_u32 v23, 455680, v7\n"
"s_lshl_b32 s24, s27, 13\n"
"s_add_u32 s24, s24, 4096\n"
"s_add_u32 s25, s24, 0x10000\n"
"s_add_u32 s30, s30, 1\n"
"s_cmp_eq_u32 s30, 8\n"
"s_cselect_b32 s30, 0, s30\n"
"s_cselect_b32 s28, -7, 1\n"
"s_cselect_b32 s29, -1, 0\n"
"s_lshl_b32 s28, s28, 7\n"
"s_add_u32 s20, s20, s28\n"
"s_addc_u32 s21, s21, s29\n"
"s_xor_b32 s24, s24, 0x8000\n"
"s_add_u32 s31, s31, 1\n"
"s_cmp_eq_u32 s31, 8\n"
"s_cselect_b32 s31, 0, s31\n"
"s_cselect_b32 s28, -7, 1\n"
"s_cselect_b32 s29, -1, 0\n"
"s_lshl_b32 s28, s28, 19\n"
"s_add_u32 s22, s22, s28\n"
"s_addc_u32 s23, s23, s29\n"
"s_add_u32 s25, s25, 0x8000\n"
"s_cmp_ge_u32 s25, 0x28000\n"
"s_cselect_b32 s28, 0x18000, 0\n"
"s_sub_u32 s25, s25, s28\n"
"v_and_b32 v3, 15, v2\n"
"v_lshrrev_b32 v4, 1, v3\n"
"v_lshrrev_b32 v5, 4, v2\n"
"v_xor_b32 v4, v4, v5\n"
"v_lshlrev_b32 v4, 4, v4\n"
"v_lshl_add_u32 v4, v3, 7, v4\n"
"s_lshr_b32 s28, s27, 1\n"
"s_lshl_b32 s28, s28, 14\n"
"v_add_u32 v24, s28, v4\n"
"v_xor_b32 v25, 64, v24\n"
"v_lshlrev_b32 v3, 10, v5\n"
"v_bfe_u32 v4, v2, 2, 2\n"
"v_lshl_add_u32 v3, v4, 7, v3\n"
"v_and_b32 v4, 3, v2\n"
"v_lshl_add_u32 v3, v4, 3, v3\n"
"s_and_b32 s28, s27, 1\n"
"s_lshl_b32 s28, s28, 13\n"
"s_add_u32 s28, s28, 0x10000\n"
"v_add_u32 v3, s28, v3\n"
"v_bfe_u32 v4, v2, 3, 2\n"
"v_xor_b32 v5, 0, v4\n"
"v_lshl_add_u32 v26, v5, 5, v3\n"
"v_xor_b32 v5, 1, v4\n"
"v_lshl_add_u32 v27, v5, 5, v3\n"
"v_xor_b32 v5, 2, v4\n"
"v_lshl_add_u32 v28, v5, 5, v3\n"
"v_xor_b32 v5, 3, v4\n"
"v_lshl_add_u32 v29, v5, 5, v3\n"
"s_mov_b64 s[56:57], %[fp]\n"
"v_and_b32 v30, 15, v2\n"
"v_lshlrev_b32 v30, 2, v30\n"
"v_bfe_u32 v7, v2, 4, 1\n"
"v_mul_u32_u24 v7, %[fd], v7\n"
"v_add_u32 v30, v30, v7\n"
"s_mov_b32 s26, 0\n"
"s_mov_b32 s33, 0\n"
"s_mov_b32 s32, 0x8000\n"
"s_mov_b64 s[50:51], %[pf]\n"
"v_lshlrev_b32 v96, 7, v2\n"
"s_waitcnt vmcnt(3)\n"
"s_barrier\n"
"ds_read_b64_tr_b16 a[160:161], v26 offset:0\n"
"ds_read_b64_tr_b16 a[162:163], v26 offset:512\n"
"ds_read_b128 a[128:131], v24 offset:0\n"
"ds_read_b64_tr_b16 a[164:165], v27 offset:0\n"
"ds_read_b64_tr_b16 a[166:167], v27 offset:512\n"
"ds_read_b128 a[132:135], v24 offset:2048\n"
"ds_read_b64_tr_b16 a[168:169], v28 offset:0\n"
"ds_read_b64_tr_b16 a[170:171], v28 offset:512\n"
"ds_read_b128 a[136:139], v24 offset:4096\n"
"ds_read_b64_tr_b16 a[172:173], v29 offset:0\n"
"ds_read_b64_tr_b16 a[174:175], v29 offset:512\n"
"ds_read_b128 a[140:143], v24 offset:6144\n"
"ds_read_b64_tr_b16 a[176:177], v26 offset:16384\n"
"ds_read_b64_tr_b16 a[178:179], v26 offset:16896\n"
"ds_read_b128 a[144:147], v24 offset:8192\n"
"ds_read_b64_tr_b16 a[180:181], v27 offset:16384\n"
"ds_read_b64_tr_b16 a[182:183], v27 offset:16896\n"
"ds_read_b128 a[148:151], v24 offset:10240\n"
"ds_read_b64_tr_b16 a[184:185], v28 offset:16384\n"
"ds_read_b64_tr_b16 a[186:187], v28 offset:16896\n"
"ds_read_b128 a[152:155], v24 offset:12288\n"
"ds_read_b64_tr_b16 a[188:189], v29 offset:16384\n"
"ds_read_b64_tr_b16 a[190:191], v29 offset:16896\n"
"ds_read_b128 a[156:159], v24 offset:14336\n"
"s_waitcnt lgkmcnt(0)\n"
"s_mov_b32 m0, s25\n"
"v_mfma_f32_16x16x32_bf16 v[128:131], a[160:163], a[128:131], 0\n"
"ds_read_b64_tr_b16 a[224:225], v26 offset:4096\n"
"v_mfma_f32_16x16x32_bf16 v[132:135], a[164:167], a[128:131], 0\n"
"v_mfma_f32_16x16x32_bf16 v[136:139], a[168:171], a[128:131], 0\n"
"ds_read_b64_tr_b16 a[226:227], v26 offset:4608\n"
"global_load_lds_dwordx4 v16, s[22:23] offset:-4096\n"
"v_mfma_f32_16x16x32_bf16 v[140:143], a[172:175], a[128:131], 0\n"
"v_mfma_f32_16x16x32_bf16 a[0:3], a[128:131], a[176:179], 0\n"
"ds_read_b128 a[192:195], v25 offset:0\n"
"global_load_lds_dwordx4 v17, s[22:23] offset:-3072\n"
"v_mfma_f32_16x16x32_bf16 a[4:7], a[128:131], a[180:183], 0\n"
"v_mfma_f32_16x16x32_bf16 a[8:11], a[128:131], a[184:187], 0\n"
"ds_read_b64_tr_b16 a[228:229], v27 offset:4096\n"
"v_mfma_f32_16x16x32_bf16 a[12:15], a[128:131], a[188:191], 0\n"
"global_load_lds_dwordx4 v18, s[22:23] offset:-2048\n"
"v_mfma_f32_16x16x32_bf16 v[144:147], a[160:163], a[132:135], 0\n"
"ds_read_b64_tr_b16 a[230:231], v27 offset:4608\n"
"v_mfma_f32_16x16x32_bf16 v[148:151], a[164:167], a[132:135], 0\n"
"global_load_lds_dwordx4 v19, s[22:23] offset:-1024\n"
"v_mfma_f32_16x16x32_bf16 v[152:155], a[168:171], a[132:135], 0\n"
"v_mfma_f32_16x16x32_bf16 v[156:159], a[172:175], a[132:135], 0\n"
"ds_read_b128 a[196:199], v25 offset:2048\n"
"v_mfma_f32_16x16x32_bf16 a[16:19], a[132:135], a[176:179], 0\n"
"global_load_lds_dwordx4 v20, s[22:23] offset:0\n"
"v_mfma_f32_16x16x32_bf16 a[20:23], a[132:135], a[180:183], 0\n"
"ds_read_b64_tr_b16 a[232:233], v28 offset:4096\n"
"v_mfma_f32_16x16x32_bf16 a[24:27], a[132:135], a[184:187], 0\n"
"global_load_lds_dwordx4 v21, s[22:23] offset:1024\n"
"v_mfma_f32_16x16x32_bf16 a[28:31], a[132:135], a[188:191], 0\n"
"ds_read_b64_tr_b16 a[234:235], v28 offset:4608\n"
"v_mfma_f32_16x16x32_bf16 v[160:163], a[160:163], a[136:139], 0\n"
"v_mfma_f32_16x16x32_bf16 v[164:167], a[164:167], a[136:139], 0\n"
"ds_read_b128 a[200:203], v25 offset:4096\n"
"global_load_lds_dwordx4 v22, s[22:23] offset:2048\n"
"v_mfma_f32_16x16x32_bf16 v[168:171], a[168:171], a[136:139], 0\n"
"v_mfma_f32_16x16x32_bf16 v[172:175], a[172:175], a[136:139], 0\n"
"ds_read_b64_tr_b16 a[236:237], v29 offset:4096\n"
"global_load_lds_dwordx4 v23, s[22:23] offset:3072\n"
"s_add_u32 s31, s31, 1\n"
"s_cmp_eq_u32 s31, 8\n"
"s_cselect_b32 s31, 0, s31\n"
"s_cselect_b32 s28, -7, 1\n"
"s_cselect_b32 s29, -1, 0\n"
"s_lshl_b32 s28, s28, 19\n"
"s_add_u32 s22, s22, s28\n"
"s_addc_u32 s23, s23, s29\n"
"s_mov_b32 m0, s24\n"
"v_mfma_f32_16x16x32_bf16 a[32:35], a[136:139], a[176:179], 0\n"
"v_mfma_f32_16x16x32_bf16 a[36:39], a[136:139], a[180:183], 0\n"
"v_mfma_f32_16x16x32_bf16 a[40:43], a[136:139], a[184:187], 0\n"
"ds_read_b64_tr_b16 a[238:239], v29 offset:4608\n"
"global_load_lds_dwordx4 v8, s[20:21] offset:-4096\n"
"v_mfma_f32_16x16x32_bf16 a[44:47], a[136:139], a[188:191], 0\n"
"v_mfma_f32_16x16x32_bf16 v[176:179], a[160:163], a[140:143], 0\n"
"ds_read_b128 a[204:207], v25 offset:6144\n"
"global_load_lds_dwordx4 v9, s[20:21] offset:-3072\n"
"v_mfma_f32_16x16x32_bf16 v[180:183], a[164:167], a[140:143], 0\n"
"v_mfma_f32_16x16x32_bf16 v[184:187], a[168:171], a[140:143], 0\n"
"ds_read_b64_tr_b16 a[240:241], v26 offset:20480\n"
"v_mfma_f32_16x16x32_bf16 v[188:191], a[172:175], a[140:143], 0\n"
"global_load_lds_dwordx4 v10, s[20:21] offset:-2048\n"
"v_mfma_f32_16x16x32_bf16 a[48:51], a[140:143], a[176:179], 0\n"
"ds_read_b64_tr_b16 a[242:243], v26 offset:20992\n"
"v_mfma_f32_16x16x32_bf16 a[52:55], a[140:143], a[180:183], 0\n"
"global_load_lds_dwordx4 v11, s[20:21] offset:-1024\n"
"v_mfma_f32_16x16x32_bf16 a[56:59], a[140:143], a[184:187], 0\n"
"ds_read_b128 a[208:211], v25 offset:8192\n"
"v_mfma_f32_16x16x32_bf16 a[60:63], a[140:143], a[188:191], 0\n"
"v_mfma_f32_16x16x32_bf16 v[192:195], a[160:163], a[144:147], 0\n"
"global_load_lds_dwordx4 v12, s[20:21] offset:0\n"
"v_mfma_f32_16x16x32_bf16 v[196:199], a[164:167], a[144:147], 0\n"
"ds_read_b64_tr_b16 a[244:245], v27 offset:20480\n"
"v_mfma_f32_16x16x32_bf16 v[200:203], a[168:171], a[144:147], 0\n"
"global_load_lds_dwordx4 v13, s[20:21] offset:1024\n"
"v_mfma_f32_16x16x32_bf16 v[204:207], a[172:175], a[144:147], 0\n"
"ds_read_b64_tr_b16 a[246:247], v27 offset:20992\n"
"v_mfma_f32_16x16x32_bf16 a[64:67], a[144:147], a[176:179], 0\n"
"v_mfma_f32_16x16x32_bf16 a[68:71], a[144:147], a[180:183], 0\n"
"ds_read_b128 a[212:215], v25 offset:10240\n"
"global_load_lds_dwordx4 v14, s[20:21] offset:2048\n"
"v_mfma_f32_16x16x32_bf16 a[72:75], a[144:147], a[184:187], 0\n"
"v_mfma_f32_16x16x32_bf16 a[76:79], a[144:147], a[188:191], 0\n"
"ds_read_b64_tr_b16 a[248:249], v28 offset:20480\n"
"global_load_lds_dwordx4 v15, s[20:21] offset:3072\n"
"s_add_u32 s25, s25, 0x8000\n"
"s_cmp_ge_u32 s25, 0x28000\n"
"s_cselect_b32 s28, 0x18000, 0\n"
"s_sub_u32 s25, s25, s28\n"
"s_mov_b32 m0, s25\n"
"v_mfma_f32_16x16x32_bf16 v[208:211], a[160:163], a[148:151], 0\n"
"v_mfma_f32_16x16x32_bf16 v[212:215], a[164:167], a[148:151], 0\n"
"ds_read_b64_tr_b16 a[250:251], v28 offset:20992\n"
"v_mfma_f32_16x16x32_bf16 v[216:219], a[168:171], a[148:151], 0\n"
"global_load_lds_dwordx4 v16, s[22:23] offset:-4096\n"
"v_mfma_f32_16x16x32_bf16 v[220:223], a[172:175], a[148:151], 0\n"
"v_mfma_f32_16x16x32_bf16 a[80:83], a[148:151], a[176:179], 0\n"
"ds_read_b128 a[216:219], v25 offset:12288\n"
"global_load_lds_dwordx4 v17, s[22:23] offset:-3072\n"
"v_mfma_f32_16x16x32_bf16 a[84:87], a[148:151], a[180:183], 0\n"
"v_mfma_f32_16x16x32_bf16 a[88:91], a[148:151], a[184:187], 0\n"
"ds_read_b64_tr_b16 a[252:253], v29 offset:20480\n"
"v_mfma_f32_16x16x32_bf16 a[92:95], a[148:151], a[188:191], 0\n"
"global_load_lds_dwordx4 v18, s[22:23] offset:-2048\n"
"v_mfma_f32_16x16x32_bf16 v[224:227], a[160:163], a[152:155], 0\n"
"ds_read_b64_tr_b16 a[254:255], v29 offset:20992\n"
"v_mfma_f32_16x16x32_bf16 v[228:231], a[164:167], a[152:155], 0\n"
"global_load_lds_dwordx4 v19, s[22:23] offset:-1024\n"
"v_mfma_f32_16x16x32_bf16 v[232:235], a[168:171], a[152:155], 0\n"
"ds_read_b128 a[220:223], v25 offset:14336\n"
"v_mfma_f32_16x16x32_bf16 v[236:239], a[172:175], a[152:155], 0\n"
"v_mfma_f32_16x16x32_bf16 a[96:99], a[152:155], a[176:179], 0\n"
"global_load_lds_dwordx4 v20, s[22:23] offset:0\n"
"v_mfma_f32_16x16x32_bf16 a[100:103], a[152:155], a[180:183], 0\n"
"v_mfma_f32_16x16x32_bf16 a[104:107], a[152:155], a[184:187], 0\n"
"global_load_lds_dwordx4 v21, s[22:23] offset:1024\n"
"v_mfma_f32_16x16x32_bf16 a[108:111], a[152:155], a[188:191], 0\n"
"v_mfma_f32_16x16x32_bf16 v[240:243], a[160:163], a[156:159], 0\n"
"v_mfma_f32_16x16x32_bf16 v[244:247], a[164:167], a[156:159], 0\n"
"global_load_lds_dwordx4 v22, s[22:23] offset:2048\n"
"v_mfma_f32_16x16x32_bf16 v[248:251], a[168:171], a[156:159], 0\n"
"v_mfma_f32_16x16x32_bf16 v[252:255], a[172:175], a[156:159], 0\n"
"global_load_lds_dwordx4 v23, s[22:23] offset:3072\n"
"global_load_dword v97, v24, s[20:21]\n"
"s_add_u32 s30, s30, 1\n"
"s_cmp_eq_u32 s30, 8\n"
"s_cselect_b32 s30, 0, s30\n"
"s_cselect_b32 s28, -7, 1\n"
"s_cselect_b32 s29, -1, 0\n"
"s_lshl_b32 s28, s28, 7\n"
"s_add_u32 s20, s20, s28\n"
"s_addc_u32 s21, s21, s29\n"
"s_xor_b32 s24, s24, 0x8000\n"
"s_add_u32 s31, s31, 1\n"
"s_cmp_eq_u32 s31, 8\n"
"s_cselect_b32 s31, 0, s31\n"
"s_cselect_b32 s28, -7, 1\n"
"s_cselect_b32 s29, -1, 0\n"
"s_lshl_b32 s28, s28, 19\n"
"s_add_u32 s22, s22, s28\n"
"s_addc_u32 s23, s23, s29\n"
"s_add_u32 s25, s25, 0x8000\n"
"s_cmp_ge_u32 s25, 0x28000\n"
"s_cselect_b32 s28, 0x18000, 0\n"
"s_sub_u32 s25, s25, s28\n"
"s_bitcmp1_b32 0xe4, s26\n"
"v_mfma_f32_16x16x32_bf16 a[112:115], a[156:159], a[176:179], 0\n"
"v_mfma_f32_16x16x32_bf16 a[116:119], a[156:159], a[180:183], 0\n"
"v_mfma_f32_16x16x32_bf16 a[120:123], a[156:159], a[184:187], 0\n"
"v_mfma_f32_16x16x32_bf16 a[124:127], a[156:159], a[188:191], 0\n"
".p2align 4\n"
"76:\n"
"s_waitcnt vmcnt(9) lgkmcnt(0)\n"
"s_barrier\n"
"s_cbranch_scc1 70f\n"
"72:\n"
"v_mfma_f32_16x16x32_bf16 v[128:131], a[224:227], a[192:195], v[128:131]\n"
"s_mov_b32 m0, s24\n"
"v_xor_b32 v24, 0x8000, v24\n"
"v_xor_b32 v25, 0x8000, v25\n"
"v_mfma_f32_16x16x32_bf16 v[132:135], a[228:231], a[192:195], v[132:135]\n"
"v_add_u32 v26, s32, v26\n"
"v_add_u32 v27, s32, v27\n"
"v_mfma_f32_16x16x32_bf16 v[136:139], a[232:235], a[192:195], v[136:139]\n"
"v_add_u32 v28, s32, v28\n"
"v_add_u32 v29, s32, v29\n"
"v_mfma_f32_16x16x32_bf16 v[140:143], a[236:239], a[192:195], v[140:143]\n"
"ds_read_b64_tr_b16 a[160:161], v26 offset:0\n"
"v_mfma_f32_16x16x32_bf16 a[0:3], a[192:195], a[240:243], a[0:3]\n"
"global_load_lds_dwordx4 v8, s[20:21] offset:-4096\n"
"v_mfma_f32_16x16x32_bf16 a[4:7], a[192:195], a[244:247], a[4:7]\n"
"s_add_u32 s33, s33, 1\n"
"ds_read_b64_tr_b16 a[162:163], v26 offset:512\n"
"v_mfma_f32_16x16x32_bf16 a[8:11], a[192:195], a[248:251], a[8:11]\n"
"s_cmp_eq_u32 s33, 2\n"
"v_mfma_f32_16x16x32_bf16 a[12:15], a[192:195], a[252:255], a[12:15]\n"
"s_cselect_b32 s32, -2, 1\n"
"ds_read_b128 a[128:131], v24 offset:0\n"
"v_mfma_f32_16x16x32_bf16 v[144:147], a[224:227], a[196:199], v[144:147]\n"
"s_cselect_b32 s33, -1, s33\n"
"global_load_lds_dwordx4 v9, s[20:21] offset:-3072\n"
"v_mfma_f32_16x16x32_bf16 v[148:151], a[228:231], a[196:199], v[148:151]\n"
"s_lshl_b32 s32, s32, 15\n"
"ds_read_b64_tr_b16 a[164:165], v27 offset:0\n"
"v_mfma_f32_16x16x32_bf16 v[152:155], a[232:235], a[196:199], v[152:155]\n"
"s_add_u32 s26, s26, 1\n"
"v_mfma_f32_16x16x32_bf16 v[156:159], a[236:239], a[196:199], v[156:159]\n"
"ds_read_b64_tr_b16 a[166:167], v27 offset:512\n"
"v_mfma_f32_16x16x32_bf16 a[16:19], a[196:199], a[240:243], a[16:19]\n"
"v_mfma_f32_16x16x32_bf16 a[20:23], a[196:199], a[244:247], a[20:23]\n"
"ds_read_b128 a[132:135], v24 offset:2048\n"
"v_mfma_f32_16x16x32_bf16 a[24:27], a[196:199], a[248:251], a[24:27]\n"
"global_load_lds_dwordx4 v10, s[20:21] offset:-2048\n"
"v_mfma_f32_16x16x32_bf16 a[28:31], a[196:199], a[252:255], a[28:31]\n"
"ds_read_b64_tr_b16 a[168:169], v28 offset:0\n"
"v_mfma_f32_16x16x32_bf16 v[160:163], a[224:227], a[200:203], v[160:163]\n"
"v_mfma_f32_16x16x32_bf16 v[164:167], a[228:231], a[200:203], v[164:167]\n"
"ds_read_b64_tr_b16 a[170:171], v28 offset:512\n"
"v_mfma_f32_16x16x32_bf16 v[168:171], a[232:235], a[200:203], v[168:171]\n"
"v_mfma_f32_16x16x32_bf16 v[172:175], a[236:239], a[200:203], v[172:175]\n"
"ds_read_b128 a[136:139], v24 offset:4096\n"
"v_mfma_f32_16x16x32_bf16 a[32:35], a[200:203], a[240:243], a[32:35]\n"
"global_load_lds_dwordx4 v11, s[20:21] offset:-1024\n"
"v_mfma_f32_16x16x32_bf16 a[36:39], a[200:203], a[244:247], a[36:39]\n"
"ds_read_b64_tr_b16 a[172:173], v29 offset:0\n"
"v_mfma_f32_16x16x32_bf16 a[40:43], a[200:203], a[248:251], a[40:43]\n"
"v_mfma_f32_16x16x32_bf16 a[44:47], a[200:203], a[252:255], a[44:47]\n"
"ds_read_b64_tr_b16 a[174:175], v29 offset:512\n"
"v_mfma_f32_16x16x32_bf16 v[176:179], a[224:227], a[204:207], v[176:179]\n"
"v_mfma_f32_16x16x32_bf16 v[180:183], a[228:231], a[204:207], v[180:183]\n"
"ds_read_b128 a[140:143], v24 offset:6144\n"
"v_mfma_f32_16x16x32_bf16 v[184:187], a[232:235], a[204:207], v[184:187]\n"
"global_load_lds_dwordx4 v12, s[20:21] offset:0\n"
"v_mfma_f32_16x16x32_bf16 v[188:191], a[236:239], a[204:207], v[188:191]\n"
"v_mfma_f32_16x16x32_bf16 a[48:51], a[204:207], a[240:243], a[48:51]\n"
"ds_read_b64_tr_b16 a[176:177], v26 offset:16384\n"
"v_mfma_f32_16x16x32_bf16 a[52:55], a[204:207], a[244:247], a[52:55]\n"
"v_mfma_f32_16x16x32_bf16 a[56:59], a[204:207], a[248:251], a[56:59]\n"
"ds_read_b64_tr_b16 a[178:179], v26 offset:16896\n"
"v_mfma_f32_16x16x32_bf16 a[60:63], a[204:207], a[252:255], a[60:63]\n"
"global_load_lds_dwordx4 v13, s[20:21] offset:1024\n"
"v_mfma_f32_16x16x32_bf16 v[192:195], a[224:227], a[208:211], v[192:195]\n"
"ds_read_b128 a[144:147], v24 offset:8192\n"
"v_mfma_f32_16x16x32_bf16 v[196:199], a[228:231], a[208:211], v[196:199]\n"
"v_mfma_f32_16x16x32_bf16 v[200:203], a[232:235], a[208:211], v[200:203]\n"
"ds_read_b64_tr_b16 a[180:181], v27 offset:16384\n"
"v_mfma_f32_16x16x32_bf16 v[204:207], a[236:239], a[208:211], v[204:207]\n"
"v_mfma_f32_16x16x32_bf16 a[64:67], a[208:211], a[240:243], a[64:67]\n"
"ds_read_b64_tr_b16 a[182:183], v27 offset:16896\n"
"v_mfma_f32_16x16x32_bf16 a[68:71], a[208:211], a[244:247], a[68:71]\n"
"global_load_lds_dwordx4 v14, s[20:21] offset:2048\n"
"v_mfma_f32_16x16x32_bf16 a[72:75], a[208:211], a[248:251], a[72:75]\n"
"ds_read_b128 a[148:151], v24 offset:10240\n"
"v_mfma_f32_16x16x32_bf16 a[76:79], a[208:211], a[252:255], a[76:79]\n"
"v_mfma_f32_16x16x32_bf16 v[208:211], a[224:227], a[212:215], v[208:211]\n"
"ds_read_b64_tr_b16 a[184:185], v28 offset:16384\n"
"v_mfma_f32_16x16x32_bf16 v[212:215], a[228:231], a[212:215], v[212:215]\n"
"v_mfma_f32_16x16x32_bf16 v[216:219], a[232:235], a[212:215], v[216:219]\n"
"ds_read_b64_tr_b16 a[186:187], v28 offset:16896\n"
"v_mfma_f32_16x16x32_bf16 v[220:223], a[236:239], a[212:215], v[220:223]\n"
"global_load_lds_dwordx4 v15, s[20:21] offset:3072\n"
"v_mfma_f32_16x16x32_bf16 a[80:83], a[212:215], a[240:243], a[80:83]\n"
"ds_read_b128 a[152:155], v24 offset:12288\n"
"v_mfma_f32_16x16x32_bf16 a[84:87], a[212:215], a[244:247], a[84:87]\n"
"v_mfma_f32_16x16x32_bf16 a[88:91], a[212:215], a[248:251], a[88:91]\n"
"ds_read_b64_tr_b16 a[188:189], v29 offset:16384\n"
"v_mfma_f32_16x16x32_bf16 a[92:95], a[212:215], a[252:255], a[92:95]\n"
"v_mfma_f32_16x16x32_bf16 v[224:227], a[224:227], a[216:219], v[224:227]\n"
"ds_read_b64_tr_b16 a[190:191], v29 offset:16896\n"
"v_mfma_f32_16x16x32_bf16 v[228:231], a[228:231], a[216:219], v[228:231]\n"
"v_mfma_f32_16x16x32_bf16 v[232:235], a[232:235], a[216:219], v[232:235]\n"
"ds_read_b128 a[156:159], v24 offset:14336\n"
"v_mfma_f32_16x16x32_bf16 v[236:239], a[236:239], a[216:219], v[236:239]\n"
"v_mfma_f32_16x16x32_bf16 a[96:99], a[216:219], a[240:243], a[96:99]\n"
"s_add_u32 s30, s30, 1\n"
"v_mfma_f32_16x16x32_bf16 a[100:103], a[216:219], a[244:247], a[100:103]\n"
"s_cmp_eq_u32 s30, 8\n"
"v_mfma_f32_16x16x32_bf16 a[104:107], a[216:219], a[248:251], a[104:107]\n"
"s_cselect_b32 s30, 0, s30\n"
"v_mfma_f32_16x16x32_bf16 a[108:111], a[216:219], a[252:255], a[108:111]\n"
"s_cselect_b32 s28, -7, 1\n"
"v_mfma_f32_16x16x32_bf16 v[240:243], a[224:227], a[220:223], v[240:243]\n"
"s_cselect_b32 s29, -1, 0\n"
"v_mfma_f32_16x16x32_bf16 v[244:247], a[228:231], a[220:223], v[244:247]\n"
"s_lshl_b32 s28, s28, 7\n"
"v_mfma_f32_16x16x32_bf16 v[248:251], a[232:235], a[220:223], v[248:251]\n"
"s_add_u32 s20, s20, s28\n"
"v_mfma_f32_16x16x32_bf16 v[252:255], a[236:239], a[220:223], v[252:255]\n"
"s_addc_u32 s21, s21, s29\n"
"v_mfma_f32_16x16x32_bf16 a[112:115], a[220:223], a[240:243], a[112:115]\n"
"s_xor_b32 s24, s24, 0x8000\n"
"v_mfma_f32_16x16x32_bf16 a[116:119], a[220:223], a[244:247], a[116:119]\n"
"s_cmp_eq_u32 s26, 8\n"
"v_mfma_f32_16x16x32_bf16 a[120:123], a[220:223], a[248:251], a[120:123]\n"
"v_mfma_f32_16x16x32_bf16 a[124:127], a[220:223], a[252:255], a[124:127]\n"
"s_waitcnt lgkmcnt(0)\n"
"s_cbranch_scc1 75f\n"
"v_mfma_f32_16x16x32_bf16 v[128:131], a[160:163], a[128:131], v[128:131]\n"
"s_mov_b32 m0, s25\n"
"ds_read_b64_tr_b16 a[224:225], v26 offset:4096\n"
"v_mfma_f32_16x16x32_bf16 v[132:135], a[164:167], a[128:131], v[132:135]\n"
"v_mfma_f32_16x16x32_bf16 v[136:139], a[168:171], a[128:131], v[136:139]\n"
"ds_read_b64_tr_b16 a[226:227], v26 offset:4608\n"
"v_mfma_f32_16x16x32_bf16 v[140:143], a[172:175], a[128:131], v[140:143]\n"
"global_load_lds_dwordx4 v16, s[22:23] offset:-4096\n"
"v_mfma_f32_16x16x32_bf16 a[0:3], a[128:131], a[176:179], a[0:3]\n"
"ds_read_b128 a[192:195], v25 offset:0\n"
"v_mfma_f32_16x16x32_bf16 a[4:7], a[128:131], a[180:183], a[4:7]\n"
"v_mfma_f32_16x16x32_bf16 a[8:11], a[128:131], a[184:187], a[8:11]\n"
"ds_read_b64_tr_b16 a[228:229], v27 offset:4096\n"
"v_mfma_f32_16x16x32_bf16 a[12:15], a[128:131], a[188:191], a[12:15]\n"
"global_load_lds_dwordx4 v17, s[22:23] offset:-3072\n"
"v_mfma_f32_16x16x32_bf16 v[144:147], a[160:163], a[132:135], v[144:147]\n"
"ds_read_b64_tr_b16 a[230:231], v27 offset:4608\n"
"v_mfma_f32_16x16x32_bf16 v[148:151], a[164:167], a[132:135], v[148:151]\n"
"v_mfma_f32_16x16x32_bf16 v[152:155], a[168:171], a[132:135], v[152:155]\n"
"v_mfma_f32_16x16x32_bf16 v[156:159], a[172:175], a[132:135], v[156:159]\n"
"ds_read_b128 a[196:199], v25 offset:2048\n"
"v_mfma_f32_16x16x32_bf16 a[16:19], a[132:135], a[176:179], a[16:19]\n"
"v_mfma_f32_16x16x32_bf16 a[20:23], a[132:135], a[180:183], a[20:23]\n"
"ds_read_b64_tr_b16 a[232:233], v28 offset:4096\n"
"v_mfma_f32_16x16x32_bf16 a[24:27], a[132:135], a[184:187], a[24:27]\n"
"global_load_lds_dwordx4 v18, s[22:23] offset:-2048\n"
"v_mfma_f32_16x16x32_bf16 a[28:31], a[132:135], a[188:191], a[28:31]\n"
"ds_read_b64_tr_b16 a[234:235], v28 offset:4608\n"
"v_mfma_f32_16x16x32_bf16 v[160:163], a[160:163], a[136:139], v[160:163]\n"
"v_mfma_f32_16x16x32_bf16 v[164:167], a[164:167], a[136:139], v[164:167]\n"
"ds_read_b128 a[200:203], v25 offset:4096\n"
"v_mfma_f32_16x16x32_bf16 v[168:171], a[168:171], a[136:139], v[168:171]\n"
"v_mfma_f32_16x16x32_bf16 v[172:175], a[172:175], a[136:139], v[172:175]\n"
"ds_read_b64_tr_b16 a[236:237], v29 offset:4096\n"
"v_mfma_f32_16x16x32_bf16 a[32:35], a[136:139], a[176:179], a[32:35]\n"
"global_load_lds_dwordx4 v19, s[22:23] offset:-1024\n"
"v_mfma_f32_16x16x32_bf16 a[36:39], a[136:139], a[180:183], a[36:39]\n"
"v_mfma_f32_16x16x32_bf16 a[40:43], a[136:139], a[184:187], a[40:43]\n"
"ds_read_b64_tr_b16 a[238:239], v29 offset:4608\n"
"v_mfma_f32_16x16x32_bf16 a[44:47], a[136:139], a[188:191], a[44:47]\n"
"v_mfma_f32_16x16x32_bf16 v[176:179], a[160:163], a[140:143], v[176:179]\n"
"ds_read_b128 a[204:207], v25 offset:6144\n"
"v_mfma_f32_16x16x32_bf16 v[180:183], a[164:167], a[140:143], v[180:183]\n"
"global_load_lds_dwordx4 v20, s[22:23] offset:0\n"
"v_mfma_f32_16x16x32_bf16 v[184:187], a[168:171], a[140:143], v[184:187]\n"
"ds_read_b64_tr_b16 a[240:241], v26 offset:20480\n"
"v_mfma_f32_16x16x32_bf16 v[188:191], a[172:175], a[140:143], v[188:191]\n"
"v_mfma_f32_16x16x32_bf16 a[48:51], a[140:143], a[176:179], a[48:51]\n"
"ds_read_b64_tr_b16 a[242:243], v26 offset:20992\n"
"v_mfma_f32_16x16x32_bf16 a[52:55], a[140:143], a[180:183], a[52:55]\n"
"v_mfma_f32_16x16x32_bf16 a[56:59], a[140:143], a[184:187], a[56:59]\n"
"ds_read_b128 a[208:211], v25 offset:8192\n"
"v_mfma_f32_16x16x32_bf16 a[60:63], a[140:143], a[188:191], a[60:63]\n"
"global_load_lds_dwordx4 v21, s[22:23] offset:1024\n"
"v_mfma_f32_16x16x32_bf16 v[192:195], a[160:163], a[144:147], v[192:195]\n"
"v_mfma_f32_16x16x32_bf16 v[196:199], a[164:167], a[144:147], v[196:199]\n"
"ds_read_b64_tr_b16 a[244:245], v27 offset:20480\n"
"v_mfma_f32_16x16x32_bf16 v[200:203], a[168:171], a[144:147], v[200:203]\n"
"v_mfma_f32_16x16x32_bf16 v[204:207], a[172:175], a[144:147], v[204:207]\n"
"ds_read_b64_tr_b16 a[246:247], v27 offset:20992\n"
"v_mfma_f32_16x16x32_bf16 a[64:67], a[144:147], a[176:179], a[64:67]\n"
"global_load_lds_dwordx4 v22, s[22:23] offset:2048\n"
"v_mfma_f32_16x16x32_bf16 a[68:71], a[144:147], a[180:183], a[68:71]\n"
"ds_read_b128 a[212:215], v25 offset:10240\n"
"v_mfma_f32_16x16x32_bf16 a[72:75], a[144:147], a[184:187], a[72:75]\n"
"v_mfma_f32_16x16x32_bf16 a[76:79], a[144:147], a[188:191], a[76:79]\n"
"ds_read_b64_tr_b16 a[248:249], v28 offset:20480\n"
"v_mfma_f32_16x16x32_bf16 v[208:211], a[160:163], a[148:151], v[208:211]\n"
"v_mfma_f32_16x16x32_bf16 v[212:215], a[164:167], a[148:151], v[212:215]\n"
"ds_read_b64_tr_b16 a[250:251], v28 offset:20992\n"
"v_mfma_f32_16x16x32_bf16 v[216:219], a[168:171], a[148:151], v[216:219]\n"
"global_load_lds_dwordx4 v23, s[22:23] offset:3072\n"
"v_mfma_f32_16x16x32_bf16 v[220:223], a[172:175], a[148:151], v[220:223]\n"
"v_mfma_f32_16x16x32_bf16 a[80:83], a[148:151], a[176:179], a[80:83]\n"
"ds_read_b128 a[216:219], v25 offset:12288\n"
"v_mfma_f32_16x16x32_bf16 a[84:87], a[148:151], a[180:183], a[84:87]\n"
"v_mfma_f32_16x16x32_bf16 a[88:91], a[148:151], a[184:187], a[88:91]\n"
"ds_read_b64_tr_b16 a[252:253], v29 offset:20480\n"
"v_mfma_f32_16x16x32_bf16 a[92:95], a[148:151], a[188:191], a[92:95]\n"
"v_mfma_f32_16x16x32_bf16 v[224:227], a[160:163], a[152:155], v[224:227]\n"
"ds_read_b64_tr_b16 a[254:255], v29 offset:20992\n"
"global_load_dword v97, v96, s[50:51]\n"
"v_mfma_f32_16x16x32_bf16 v[228:231], a[164:167], a[152:155], v[228:231]\n"
"v_add_u32 v96, 8192, v96\n"
"v_mfma_f32_16x16x32_bf16 v[232:235], a[168:171], a[152:155], v[232:235]\n"
"s_add_u32 s31, s31, 1\n"
"ds_read_b128 a[220:223], v25 offset:14336\n"
"v_mfma_f32_16x16x32_bf16 v[236:239], a[172:175], a[152:155], v[236:239]\n"
"s_cmp_eq_u32 s31, 8\n"
"v_mfma_f32_16x16x32_bf16 a[96:99], a[152:155], a[176:179], a[96:99]\n"
"s_cselect_b32 s31, 0, s31\n"
"v_mfma_f32_16x16x32_bf16 a[100:103], a[152:155], a[180:183], a[100:103]\n"
"s_cselect_b32 s28, -7, 1\n"
"v_mfma_f32_16x16x32_bf16 a[104:107], a[152:155], a[184:187], a[104:107]\n"
"s_cselect_b32 s29, -1, 0\n"
"v_mfma_f32_16x16x32_bf16 a[108:111], a[152:155], a[188:191], a[108:111]\n"
"s_lshl_b32 s28, s28, 19\n"
"v_mfma_f32_16x16x32_bf16 v[240:243], a[160:163], a[156:159], v[240:243]\n"
"s_add_u32 s22, s22, s28\n"
"v_mfma_f32_16x16x32_bf16 v[244:247], a[164:167], a[156:159], v[244:247]\n"
"s_addc_u32 s23, s23, s29\n"
"v_mfma_f32_16x16x32_bf16 v[248:251], a[168:171], a[156:159], v[248:251]\n"
"s_add_u32 s25, s25, 0x8000\n"
"v_mfma_f32_16x16x32_bf16 v[252:255], a[172:175], a[156:159], v[252:255]\n"
"s_cmp_ge_u32 s25, 0x28000\n"
"v_mfma_f32_16x16x32_bf16 a[112:115], a[156:159], a[176:179], a[112:115]\n"
"s_cselect_b32 s28, 0x18000, 0\n"
"v_mfma_f32_16x16x32_bf16 a[116:119], a[156:159], a[180:183], a[116:119]\n"
"s_sub_u32 s25, s25, s28\n"
"v_mfma_f32_16x16x32_bf16 a[120:123], a[156:159], a[184:187], a[120:123]\n"
"s_bitcmp1_b32 0xe4, s26\n"
"v_mfma_f32_16x16x32_bf16 a[124:127], a[156:159], a[188:191], a[124:127]\n"
"s_branch 76b\n"
"75:\n"
"s_mov_b64 s[44:45], %[hl]\n"
"s_mov_b64 s[46:47], %[hs]\n"
"s_mov_b32 s70, 0x40000\n"
"80:\n"
"v_cmp_ne_u32 vcc, 1, v31\n"
"s_cbranch_vccnz 801f\n"
"802:\n"
"s_and_b32 s28, s27, 2\n"
"s_lshl_b32 s28, s28, 18\n"
"s_sub_u32 s28, 0x300000, s28\n"
"s_add_u32 s22, s58, s28\n"
"s_addc_u32 s23, s59, 0\n"
"s_lshl_b32 s28, s27, 13\n"
"s_add_u32 m0, s28, 0x19000\n"
"v_lshlrev_b32 v30, 4, v2\n"
"v_add_u32 v30, 4096, v30\n"
"v_add_u32 v31, 18432, v30\n"
"global_load_lds_dwordx4 v16, s[22:23] offset:-4096\n"
"global_load_lds_dwordx4 v17, s[22:23] offset:-3072\n"
"global_load_lds_dwordx4 v18, s[22:23] offset:-2048\n"
"global_load_lds_dwordx4 v19, s[22:23] offset:-1024\n"
"global_load_lds_dwordx4 v20, s[22:23] offset:0\n"
"global_load_lds_dwordx4 v21, s[22:23] offset:1024\n"
"global_load_lds_dwordx4 v22, s[22:23] offset:2048\n"
"global_load_lds_dwordx4 v23, s[22:23] offset:3072\n"
"global_load_dwordx4 a[192:195], v30, s[44:45] offset:-4096 sc1\n"
"global_load_dwordx4 a[196:199], v31, s[44:45] offset:-4096 sc1\n"
"global_load_dwordx4 a[200:203], v30, s[46:47] offset:-4096 sc1\n"
"global_load_dwordx4 a[204:207], v31, s[46:47] offset:-4096 sc1\n"
"global_load_dwordx4 a[208:211], v30, s[44:45] offset:-3072 sc1\n"
"global_load_dwordx4 a[212:215], v31, s[44:45] offset:-3072 sc1\n"
"global_load_dwordx4 a[216:219], v30, s[46:47] offset:-3072 sc1\n"
"global_load_dwordx4 a[220:223], v31, s[46:47] offset:-3072 sc1\n"
"global_load_dwordx4 a[224:227], v30, s[44:45] offset:-2048 sc1\n"
"global_load_dwordx4 a[228:231], v31, s[44:45] offset:-2048 sc1\n"
"global_load_dwordx4 a[232:235], v30, s[46:47] offset:-2048 sc1\n"
"global_load_dwordx4 a[236:239], v31, s[46:47] offset:-2048 sc1\n"
"global_load_dwordx4 a[240:243], v30, s[44:45] offset:-1024 sc1\n"
"global_load_dwordx4 a[244:247], v31, s[44:45] offset:-1024 sc1\n"
"global_load_dwordx4 a[248:251], v30, s[46:47] offset:-1024 sc1\n"
"global_load_dwordx4 a[252:255], v31, s[46:47] offset:-1024 sc1\n"
"v_cvt_pk_bf16_f32 v32, v128, v129\n"
"v_cvt_pk_bf16_f32 v33, v130, v131\n"
"v_cvt_pk_bf16_f32 v34, v132, v133\n"
"v_cvt_pk_bf16_f32 v35, v134, v135\n"
"v_cvt_pk_bf16_f32 v36, v136, v137\n"
"v_cvt_pk_bf16_f32 v37, v138, v139\n"
"v_cvt_pk_bf16_f32 v38, v140, v141\n"
"v_cvt_pk_bf16_f32 v39, v142, v143\n"
"v_cvt_pk_bf16_f32 v40, v144, v145\n"
"v_cvt_pk_bf16_f32 v41, v146, v147\n"
"v_cvt_pk_bf16_f32 v42, v148, v149\n"
"v_cvt_pk_bf16_f32 v43, v150, v151\n"
"v_cvt_pk_bf16_f32 v44, v152, v153\n"
"v_cvt_pk_bf16_f32 v45, v154, v155\n"
"v_cvt_pk_bf16_f32 v46, v156, v157\n"
"v_cvt_pk_bf16_f32 v47, v158, v159\n"
"v_cvt_pk_bf16_f32 v48, v160, v161\n"
"v_cvt_pk_bf16_f32 v49, v162, v163\n"
"v_cvt_pk_bf16_f32 v50, v164, v165\n"
"v_cvt_pk_bf16_f32 v51, v166, v167\n"
"v_cvt_pk_bf16_f32 v52, v168, v169\n"
"v_cvt_pk_bf16_f32 v53, v170, v171\n"
"v_cvt_pk_bf16_f32 v54, v172, v173\n"
"v_cvt_pk_bf16_f32 v55, v174, v175\n"
"v_cvt_pk_bf16_f32 v56, v176, v177\n"
"v_cvt_pk_bf16_f32 v57, v178, v179\n"
"v_cvt_pk_bf16_f32 v58, v180, v181\n"
"v_cvt_pk_bf16_f32 v59, v182, v183\n"
"v_cvt_pk_bf16_f32 v60, v184, v185\n"
"v_cvt_pk_bf16_f32 v61, v186, v187\n"
"v_cvt_pk_bf16_f32 v62, v188, v189\n"
"v_cvt_pk_bf16_f32 v63, v190, v191\n"
"v_cvt_pk_bf16_f32 v64, v192, v193\n"
"v_cvt_pk_bf16_f32 v65, v194, v195\n"
"v_cvt_pk_bf16_f32 v66, v196, v197\n"
"v_cvt_pk_bf16_f32 v67, v198, v199\n"
"v_cvt_pk_bf16_f32 v68, v200, v201\n"
"v_cvt_pk_bf16_f32 v69, v202, v203\n"
"v_cvt_pk_bf16_f32 v70, v204, v205\n"
"v_cvt_pk_bf16_f32 v71, v206, v207\n"
"v_cvt_pk_bf16_f32 v72, v208, v209\n"
"v_cvt_pk_bf16_f32 v73, v210, v211\n"
"v_cvt_pk_bf16_f32 v74, v212, v213\n"
"v_cvt_pk_bf16_f32 v75, v214, v215\n"
"v_cvt_pk_bf16_f32 v76, v216, v217\n"
"v_cvt_pk_bf16_f32 v77, v218, v219\n"
"v_cvt_pk_bf16_f32 v78, v220, v221\n"
"v_cvt_pk_bf16_f32 v79, v222, v223\n"
"v_cvt_pk_bf16_f32 v80, v224, v225\n"
"v_cvt_pk_bf16_f32 v81, v226, v227\n"
"v_cvt_pk_bf16_f32 v82, v228, v229\n"
"v_cvt_pk_bf16_f32 v83, v230, v231\n"
"v_cvt_pk_bf16_f32 v84, v232, v233\n"
"v_cvt_pk_bf16_f32 v85, v234, v235\n"
"v_cvt_pk_bf16_f32 v86, v236, v237\n"
"v_cvt_pk_bf16_f32 v87, v238, v239\n"
"v_cvt_pk_bf16_f32 v88, v240, v241\n"
"v_cvt_pk_bf16_f32 v89, v242, v243\n"
"v_cvt_pk_bf16_f32 v90, v244, v245\n"
"v_cvt_pk_bf16_f32 v91, v246, v247\n"
"v_cvt_pk_bf16_f32 v92, v248, v249\n"
"v_cvt_pk_bf16_f32 v93, v250, v251\n"
"v_cvt_pk_bf16_f32 v94, v252, v253\n"
"v_cvt_pk_bf16_f32 v95, v254, v255\n"
"s_mov_b64 s[62:63], %[kvb]\n"
"s_and_b32 s28, s27, 1\n"
"s_mul_i32 s29, s28, 6144\n"
"s_add_u32 s29, s29, 16384\n"
"s_add_u32 s64, s62, s29\n"
"s_addc_u32 s65, s63, 0\n"
"s_mov_b64 s[66:67], %[fk]\n"
"s_mov_b64 s[68:69], %[fq]\n"
"s_lshl_b32 s28, s28, 11\n"
"s_lshr_b32 s30, s27, 1\n"
"s_lshl_b32 s30, s30, 16\n"
"s_add_u32 s28, s28, s30\n"
"v_lshlrev_b32 v16, 4, v2\n"
"v_add_u32 v16, s28, v16\n"
"v_add_u32 v17, s29, v16\n"
"v_add_u32 v8, 0, v30\n"
"v_add_u32 v9, 8192, v30\n"
"v_add_u32 v10, 16384, v30\n"
"v_add_u32 v11, 24576, v30\n"
"v_add_u32 v12, 32768, v30\n"
"v_add_u32 v13, 40960, v30\n"
"v_add_u32 v14, 49152, v30\n"
"v_add_u32 v15, 57344, v30\n"
"v_lshrrev_b32 v5, 4, v2\n"
"v_lshlrev_b32 v3, 10, v5\n"
"v_bfe_u32 v4, v2, 2, 2\n"
"v_lshl_add_u32 v3, v4, 7, v3\n"
"v_and_b32 v4, 3, v2\n"
"v_lshl_add_u32 v3, v4, 3, v3\n"
"v_add_u32 v3, 0x0, v3\n"
"v_bfe_u32 v4, v2, 3, 2\n"
"v_xor_b32 v5, 0, v4\n"
"v_lshl_add_u32 v26, v5, 5, v3\n"
"v_xor_b32 v5, 1, v4\n"
"v_lshl_add_u32 v27, v5, 5, v3\n"
"v_xor_b32 v5, 2, v4\n"
"v_lshl_add_u32 v28, v5, 5, v3\n"
"v_xor_b32 v5, 3, v4\n"
"v_lshl_add_u32 v29, v5, 5, v3\n"
"ds_read_b64_tr_b16 a[128:129], v26 offset:0\n"
"ds_read_b64_tr_b16 a[130:131], v26 offset:512\n"
"ds_read_b64_tr_b16 a[132:133], v27 offset:0\n"
"ds_read_b64_tr_b16 a[134:135], v27 offset:512\n"
"ds_read_b64_tr_b16 a[136:137], v28 offset:0\n"
"ds_read_b64_tr_b16 a[138:139], v28 offset:512\n"
"ds_read_b64_tr_b16 a[140:141], v29 offset:0\n"
"ds_read_b64_tr_b16 a[142:143], v29 offset:512\n"
"ds_read_b64_tr_b16 a[144:145], v26 offset:8192\n"
"ds_read_b64_tr_b16 a[146:147], v26 offset:8704\n"
"ds_read_b64_tr_b16 a[148:149], v27 offset:8192\n"
"ds_read_b64_tr_b16 a[150:151], v27 offset:8704\n"
"ds_read_b64_tr_b16 a[152:153], v28 offset:8192\n"
"ds_read_b64_tr_b16 a[154:155], v28 offset:8704\n"
"ds_read_b64_tr_b16 a[156:157], v29 offset:8192\n"
"ds_read_b64_tr_b16 a[158:159], v29 offset:8704\n"
"s_waitcnt vmcnt(12) lgkmcnt(0)\n"
"v_mfma_f32_16x16x32_bf16 v[128:131], a[128:131], a[192:195], 0\n"
"ds_read_b64_tr_b16 a[160:161], v26 offset:4096\n"
"v_accvgpr_read_b32 v96, a0\n"
"v_mfma_f32_16x16x32_bf16 v[132:135], a[132:135], a[192:195], 0\n"
"ds_read_b64_tr_b16 a[162:163], v26 offset:4608\n"
"v_accvgpr_read_b32 v97, a1\n"
"v_mfma_f32_16x16x32_bf16 v[136:139], a[136:139], a[192:195], 0\n"
"ds_read_b64_tr_b16 a[164:165], v27 offset:4096\n"
"v_accvgpr_read_b32 v98, a2\n"
"v_mfma_f32_16x16x32_bf16 v[140:143], a[140:143], a[192:195], 0\n"
"global_store_dwordx4 v8, v[32:35], s[62:63] offset:-4096 sc1\n"
"v_mfma_f32_16x16x32_bf16 v[144:147], a[144:147], a[192:195], 0\n"
"v_accvgpr_read_b32 v99, a3\n"
"v_accvgpr_read_b32 v100, a16\n"
"v_mfma_f32_16x16x32_bf16 v[148:151], a[148:151], a[192:195], 0\n"
"ds_read_b64_tr_b16 a[166:167], v27 offset:4608\n"
"v_accvgpr_read_b32 v101, a17\n"
"v_mfma_f32_16x16x32_bf16 v[152:155], a[152:155], a[192:195], 0\n"
"ds_read_b64_tr_b16 a[168:169], v28 offset:4096\n"
"v_accvgpr_read_b32 v102, a18\n"
"v_mfma_f32_16x16x32_bf16 v[156:159], a[156:159], a[192:195], 0\n"
"global_load_dwordx4 a[192:195], v30, s[44:45] offset:0 sc1\n"
"v_mfma_f32_16x16x32_bf16 v[160:163], a[128:131], a[196:199], 0\n"
"v_accvgpr_read_b32 v103, a19\n"
"v_cvt_pk_bf16_f32 v104, v96, v97\n"
"v_mfma_f32_16x16x32_bf16 v[164:167], a[132:135], a[196:199], 0\n"
"ds_read_b64_tr_b16 a[170:171], v28 offset:4608\n"
"v_cvt_pk_bf16_f32 v105, v98, v99\n"
"v_mfma_f32_16x16x32_bf16 v[168:171], a[136:139], a[196:199], 0\n"
"ds_read_b64_tr_b16 a[172:173], v29 offset:4096\n"
"v_cvt_pk_bf16_f32 v106, v100, v101\n"
"v_mfma_f32_16x16x32_bf16 v[172:175], a[140:143], a[196:199], 0\n"
"global_store_dwordx4 v8, v[36:39], s[62:63] offset:-3072 sc1\n"
"v_mfma_f32_16x16x32_bf16 v[176:179], a[144:147], a[196:199], 0\n"
"v_cvt_pk_bf16_f32 v107, v102, v103\n"
"v_accvgpr_write_b32 a0, v104\n"
"v_mfma_f32_16x16x32_bf16 v[180:183], a[148:151], a[196:199], 0\n"
"ds_read_b64_tr_b16 a[174:175], v29 offset:4608\n"
"v_accvgpr_write_b32 a1, v105\n"
"v_mfma_f32_16x16x32_bf16 v[184:187], a[152:155], a[196:199], 0\n"
"ds_read_b64_tr_b16 a[176:177], v26 offset:12288\n"
"v_accvgpr_write_b32 a2, v106\n"
"v_mfma_f32_16x16x32_bf16 v[188:191], a[156:159], a[196:199], 0\n"
"global_load_dwordx4 a[196:199], v31, s[44:45] offset:0 sc1\n"
"v_mfma_f32_16x16x32_bf16 v[192:195], a[128:131], a[200:203], 0\n"
"ds_read_b64_tr_b16 a[178:179], v26 offset:12800\n"
"v_accvgpr_write_b32 a3, v107\n"
"v_mfma_f32_16x16x32_bf16 v[196:199], a[132:135], a[200:203], 0\n"
"v_accvgpr_read_b32 v112, a4\n"
"v_accvgpr_read_b32 v113, a5\n"
"v_mfma_f32_16x16x32_bf16 v[200:203], a[136:139], a[200:203], 0\n"
"ds_read_b64_tr_b16 a[180:181], v27 offset:12288\n"
"v_accvgpr_read_b32 v114, a6\n"
"v_mfma_f32_16x16x32_bf16 v[204:207], a[140:143], a[200:203], 0\n"
"ds_read_b64_tr_b16 a[182:183], v27 offset:12800\n"
"v_accvgpr_read_b32 v115, a7\n"
"v_mfma_f32_16x16x32_bf16 v[208:211], a[144:147], a[200:203], 0\n"
"v_accvgpr_read_b32 v116, a20\n"
"v_accvgpr_read_b32 v117, a21\n"
"v_mfma_f32_16x16x32_bf16 v[212:215], a[148:151], a[200:203], 0\n"
"ds_read_b64_tr_b16 a[184:185], v28 offset:12288\n"
"v_accvgpr_read_b32 v118, a22\n"
"v_mfma_f32_16x16x32_bf16 v[216:219], a[152:155], a[200:203], 0\n"
"ds_read_b64_tr_b16 a[186:187], v28 offset:12800\n"
"v_accvgpr_read_b32 v119, a23\n"
"v_mfma_f32_16x16x32_bf16 v[220:223], a[156:159], a[200:203], 0\n"
"global_load_dwordx4 a[200:203], v30, s[46:47] offset:0 sc1\n"
"v_mfma_f32_16x16x32_bf16 v[224:227], a[128:131], a[204:207], 0\n"
"v_cvt_pk_bf16_f32 v120, v112, v113\n"
"v_cvt_pk_bf16_f32 v121, v114, v115\n"
"v_mfma_f32_16x16x32_bf16 v[228:231], a[132:135], a[204:207], 0\n"
"ds_read_b64_tr_b16 a[188:189], v29 offset:12288\n"
"v_cvt_pk_bf16_f32 v122, v116, v117\n"
"v_mfma_f32_16x16x32_bf16 v[232:235], a[136:139], a[204:207], 0\n"
"ds_read_b64_tr_b16 a[190:191], v29 offset:12800\n"
"v_cvt_pk_bf16_f32 v123, v118, v119\n"
"v_mfma_f32_16x16x32_bf16 v[236:239], a[140:143], a[204:207], 0\n"
"v_accvgpr_write_b32 a4, v120\n"
"v_accvgpr_write_b32 a5, v121\n"
"v_mfma_f32_16x16x32_bf16 v[240:243], a[144:147], a[204:207], 0\n"
"v_accvgpr_write_b32 a6, v122\n"
"v_accvgpr_write_b32 a7, v123\n"
"v_mfma_f32_16x16x32_bf16 v[244:247], a[148:151], a[204:207], 0\n"
"v_mfma_f32_16x16x32_bf16 v[248:251], a[152:155], a[204:207], 0\n"
"v_mfma_f32_16x16x32_bf16 v[252:255], a[156:159], a[204:207], 0\n"
"global_load_dwordx4 a[204:207], v31, s[46:47] offset:0 sc1\n"
"s_waitcnt vmcnt(14) lgkmcnt(0)\n"
"v_mfma_f32_16x16x32_bf16 v[128:131], a[160:163], a[208:211], v[128:131]\n"
"ds_read_b64_tr_b16 a[128:129], v26 offset:16384\n"
"v_accvgpr_read_b32 v96, a8\n"
"v_mfma_f32_16x16x32_bf16 v[132:135], a[164:167], a[208:211], v[132:135]\n"
"ds_read_b64_tr_b16 a[130:131], v26 offset:16896\n"
"v_accvgpr_read_b32 v97, a9\n"
"v_mfma_f32_16x16x32_bf16 v[136:139], a[168:171], a[208:211], v[136:139]\n"
"v_accvgpr_read_b32 v98, a10\n"
"v_accvgpr_read_b32 v99, a11\n"
"v_mfma_f32_16x16x32_bf16 v[140:143], a[172:175], a[208:211], v[140:143]\n"
"global_store_dwordx4 v8, v[40:43], s[62:63] offset:0 sc1\n"
"v_mfma_f32_16x16x32_bf16 v[144:147], a[176:179], a[208:211], v[144:147]\n"
"ds_read_b64_tr_b16 a[132:133], v27 offset:16384\n"
"v_accvgpr_read_b32 v100, a24\n"
"v_mfma_f32_16x16x32_bf16 v[148:151], a[180:183], a[208:211], v[148:151]\n"
"ds_read_b64_tr_b16 a[134:135], v27 offset:16896\n"
"v_accvgpr_read_b32 v101, a25\n"
"v_mfma_f32_16x16x32_bf16 v[152:155], a[184:187], a[208:211], v[152:155]\n"
"v_accvgpr_read_b32 v102, a26\n"
"v_accvgpr_read_b32 v103, a27\n"
"v_mfma_f32_16x16x32_bf16 v[156:159], a[188:191], a[208:211], v[156:159]\n"
"global_load_dwordx4 a[208:211], v30, s[44:45] offset:1024 sc1\n"
"v_mfma_f32_16x16x32_bf16 v[160:163], a[160:163], a[212:215], v[160:163]\n"
"ds_read_b64_tr_b16 a[136:137], v28 offset:16384\n"
"v_cvt_pk_bf16_f32 v104, v96, v97\n"
"v_mfma_f32_16x16x32_bf16 v[164:167], a[164:167], a[212:215], v[164:167]\n"
"ds_read_b64_tr_b16 a[138:139], v28 offset:16896\n"
"v_cvt_pk_bf16_f32 v105, v98, v99\n"
"v_mfma_f32_16x16x32_bf16 v[168:171], a[168:171], a[212:215], v[168:171]\n"
"v_cvt_pk_bf16_f32 v106, v100, v101\n"
"v_cvt_pk_bf16_f32 v107, v102, v103\n"
"v_mfma_f32_16x16x32_bf16 v[172:175], a[172:175], a[212:215], v[172:175]\n"
"ds_read_b64_tr_b16 a[140:141], v29 offset:16384\n"
"v_accvgpr_write_b32 a8, v104\n"
"v_mfma_f32_16x16x32_bf16 v[176:179], a[176:179], a[212:215], v[176:179]\n"
"ds_read_b64_tr_b16 a[142:143], v29 offset:16896\n"
"v_accvgpr_write_b32 a9, v105\n"
"v_mfma_f32_16x16x32_bf16 v[180:183], a[180:183], a[212:215], v[180:183]\n"
"v_accvgpr_write_b32 a10, v106\n"
"v_accvgpr_write_b32 a11, v107\n"
"v_mfma_f32_16x16x32_bf16 v[184:187], a[184:187], a[212:215], v[184:187]\n"
"ds_read_b64_tr_b16 a[144:145], v26 offset:24576\n"
"v_accvgpr_read_b32 v112, a12\n"
"v_mfma_f32_16x16x32_bf16 v[188:191], a[188:191], a[212:215], v[188:191]\n"
"global_load_dwordx4 a[212:215], v31, s[44:45] offset:1024 sc1\n"
"v_mfma_f32_16x16x32_bf16 v[192:195], a[160:163], a[216:219], v[192:195]\n"
"ds_read_b64_tr_b16 a[146:147], v26 offset:25088\n"
"v_accvgpr_read_b32 v113, a13\n"
"v_mfma_f32_16x16x32_bf16 v[196:199], a[164:167], a[216:219], v[196:199]\n"
"v_accvgpr_read_b32 v114, a14\n"
"v_accvgpr_read_b32 v115, a15\n"
"v_mfma_f32_16x16x32_bf16 v[200:203], a[168:171], a[216:219], v[200:203]\n"
"ds_read_b64_tr_b16 a[148:149], v27 offset:24576\n"
"v_accvgpr_read_b32 v116, a28\n"
"v_mfma_f32_16x16x32_bf16 v[204:207], a[172:175], a[216:219], v[204:207]\n"
"ds_read_b64_tr_b16 a[150:151], v27 offset:25088\n"
"v_accvgpr_read_b32 v117, a29\n"
"v_mfma_f32_16x16x32_bf16 v[208:211], a[176:179], a[216:219], v[208:211]\n"
"v_accvgpr_read_b32 v118, a30\n"
"v_accvgpr_read_b32 v119, a31\n"
"v_mfma_f32_16x16x32_bf16 v[212:215], a[180:183], a[216:219], v[212:215]\n"
"ds_read_b64_tr_b16 a[152:153], v28 offset:24576\n"
"v_cvt_pk_bf16_f32 v120, v112, v113\n"
"v_mfma_f32_16x16x32_bf16 v[216:219], a[184:187], a[216:219], v[216:219]\n"
"ds_read_b64_tr_b16 a[154:155], v28 offset:25088\n"
"v_cvt_pk_bf16_f32 v121, v114, v115\n"
"v_mfma_f32_16x16x32_bf16 v[220:223], a[188:191], a[216:219], v[220:223]\n"
"global_load_dwordx4 a[216:219], v30, s[46:47] offset:1024 sc1\n"
"v_mfma_f32_16x16x32_bf16 v[224:227], a[160:163], a[220:223], v[224:227]\n"
"v_cvt_pk_bf16_f32 v122, v116, v117\n"
"v_cvt_pk_bf16_f32 v123, v118, v119\n"
"v_mfma_f32_16x16x32_bf16 v[228:231], a[164:167], a[220:223], v[228:231]\n"
"ds_read_b64_tr_b16 a[156:157], v29 offset:24576\n"
"v_accvgpr_write_b32 a12, v120\n"
"v_mfma_f32_16x16x32_bf16 v[232:235], a[168:171], a[220:223], v[232:235]\n"
"ds_read_b64_tr_b16 a[158:159], v29 offset:25088\n"
"v_accvgpr_write_b32 a13, v121\n"
"v_mfma_f32_16x16x32_bf16 v[236:239], a[172:175], a[220:223], v[236:239]\n"
"v_accvgpr_write_b32 a14, v122\n"
"v_accvgpr_write_b32 a15, v123\n"
"v_mfma_f32_16x16x32_bf16 v[240:243], a[176:179], a[220:223], v[240:243]\n"
"v_mfma_f32_16x16x32_bf16 v[244:247], a[180:183], a[220:223], v[244:247]\n"
"v_mfma_f32_16x16x32_bf16 v[248:251], a[184:187], a[220:223], v[248:251]\n"
"v_mfma_f32_16x16x32_bf16 v[252:255], a[188:191], a[220:223], v[252:255]\n"
"global_load_dwordx4 a[220:223], v31, s[46:47] offset:1024 sc1\n"
"s_waitcnt vmcnt(15) lgkmcnt(0)\n"
"s_barrier\n"
"v_mfma_f32_16x16x32_bf16 v[128:131], a[128:131], a[224:227], v[128:131]\n"
"ds_read_b64_tr_b16 a[160:161], v26 offset:20480\n"
"v_accvgpr_read_b32 v96, a32\n"
"v_mfma_f32_16x16x32_bf16 v[132:135], a[132:135], a[224:227], v[132:135]\n"
"ds_read_b64_tr_b16 a[162:163], v26 offset:20992\n"
"v_accvgpr_read_b32 v97, a33\n"
"v_mfma_f32_16x16x32_bf16 v[136:139], a[136:139], a[224:227], v[136:139]\n"
"v_accvgpr_read_b32 v98, a34\n"
"v_accvgpr_read_b32 v99, a35\n"
"v_mfma_f32_16x16x32_bf16 v[140:143], a[140:143], a[224:227], v[140:143]\n"
"global_store_dwordx4 v8, v[44:47], s[62:63] offset:1024 sc1\n"
"v_mfma_f32_16x16x32_bf16 v[144:147], a[144:147], a[224:227], v[144:147]\n"
"ds_read_b64_tr_b16 a[164:165], v27 offset:20480\n"
"v_accvgpr_read_b32 v100, a48\n"
"v_mfma_f32_16x16x32_bf16 v[148:151], a[148:151], a[224:227], v[148:151]\n"
"ds_read_b64_tr_b16 a[166:167], v27 offset:20992\n"
"v_accvgpr_read_b32 v101, a49\n"
"v_mfma_f32_16x16x32_bf16 v[152:155], a[152:155], a[224:227], v[152:155]\n"
"v_accvgpr_read_b32 v102, a50\n"
"v_accvgpr_read_b32 v103, a51\n"
"v_mfma_f32_16x16x32_bf16 v[156:159], a[156:159], a[224:227], v[156:159]\n"
"global_load_dwordx4 a[224:227], v30, s[44:45] offset:2048 sc1\n"
"v_mfma_f32_16x16x32_bf16 v[160:163], a[128:131], a[228:231], v[160:163]\n"
"ds_read_b64_tr_b16 a[168:169], v28 offset:20480\n"
"v_cvt_pk_bf16_f32 v104, v96, v97\n"
"v_mfma_f32_16x16x32_bf16 v[164:167], a[132:135], a[228:231], v[164:167]\n"
"ds_read_b64_tr_b16 a[170:171], v28 offset:20992\n"
"v_cvt_pk_bf16_f32 v105, v98, v99\n"
"v_mfma_f32_16x16x32_bf16 v[168:171], a[136:139], a[228:231], v[168:171]\n"
"v_cvt_pk_bf16_f32 v106, v100, v101\n"
"v_cvt_pk_bf16_f32 v107, v102, v103\n"
"v_mfma_f32_16x16x32_bf16 v[172:175], a[140:143], a[228:231], v[172:175]\n"
"ds_read_b64_tr_b16 a[172:173], v29 offset:20480\n"
"v_accvgpr_write_b32 a32, v104\n"
"v_mfma_f32_16x16x32_bf16 v[176:179], a[144:147], a[228:231], v[176:179]\n"
"ds_read_b64_tr_b16 a[174:175], v29 offset:20992\n"
"v_accvgpr_write_b32 a33, v105\n"
"v_mfma_f32_16x16x32_bf16 v[180:183], a[148:151], a[228:231], v[180:183]\n"
"v_accvgpr_write_b32 a34, v106\n"
"v_accvgpr_write_b32 a35, v107\n"
"v_mfma_f32_16x16x32_bf16 v[184:187], a[152:155], a[228:231], v[184:187]\n"
"ds_read_b64_tr_b16 a[176:177], v26 offset:28672\n"
"v_accvgpr_read_b32 v112, a36\n"
"v_mfma_f32_16x16x32_bf16 v[188:191], a[156:159], a[228:231], v[188:191]\n"
"global_load_dwordx4 a[228:231], v31, s[44:45] offset:2048 sc1\n"
"v_mfma_f32_16x16x32_bf16 v[192:195], a[128:131], a[232:235], v[192:195]\n"
"ds_read_b64_tr_b16 a[178:179], v26 offset:29184\n"
"v_accvgpr_read_b32 v113, a37\n"
"v_mfma_f32_16x16x32_bf16 v[196:199], a[132:135], a[232:235], v[196:199]\n"
"v_accvgpr_read_b32 v114, a38\n"
"v_accvgpr_read_b32 v115, a39\n"
"v_mfma_f32_16x16x32_bf16 v[200:203], a[136:139], a[232:235], v[200:203]\n"
"ds_read_b64_tr_b16 a[180:181], v27 offset:28672\n"
"v_accvgpr_read_b32 v116, a52\n"
"v_mfma_f32_16x16x32_bf16 v[204:207], a[140:143], a[232:235], v[204:207]\n"
"ds_read_b64_tr_b16 a[182:183], v27 offset:29184\n"
"v_accvgpr_read_b32 v117, a53\n"
"v_mfma_f32_16x16x32_bf16 v[208:211], a[144:147], a[232:235], v[208:211]\n"
"v_accvgpr_read_b32 v118, a54\n"
"v_accvgpr_read_b32 v119, a55\n"
"v_mfma_f32_16x16x32_bf16 v[212:215], a[148:151], a[232:235], v[212:215]\n"
"ds_read_b64_tr_b16 a[184:185], v28 offset:28672\n"
"v_cvt_pk_bf16_f32 v120, v112, v113\n"
"v_mfma_f32_16x16x32_bf16 v[216:219], a[152:155], a[232:235], v[216:219]\n"
"ds_read_b64_tr_b16 a[186:187], v28 offset:29184\n"
"v_cvt_pk_bf16_f32 v121, v114, v115\n"
"v_mfma_f32_16x16x32_bf16 v[220:223], a[156:159], a[232:235], v[220:223]\n"
"global_load_dwordx4 a[232:235], v30, s[46:47] offset:2048 sc1\n"
"v_mfma_f32_16x16x32_bf16 v[224:227], a[128:131], a[236:239], v[224:227]\n"
"v_cvt_pk_bf16_f32 v122, v116, v117\n"
"v_cvt_pk_bf16_f32 v123, v118, v119\n"
"v_mfma_f32_16x16x32_bf16 v[228:231], a[132:135], a[236:239], v[228:231]\n"
"ds_read_b64_tr_b16 a[188:189], v29 offset:28672\n"
"v_accvgpr_write_b32 a36, v120\n"
"v_mfma_f32_16x16x32_bf16 v[232:235], a[136:139], a[236:239], v[232:235]\n"
"ds_read_b64_tr_b16 a[190:191], v29 offset:29184\n"
"v_accvgpr_write_b32 a37, v121\n"
"v_mfma_f32_16x16x32_bf16 v[236:239], a[140:143], a[236:239], v[236:239]\n"
"v_accvgpr_write_b32 a38, v122\n"
"v_accvgpr_write_b32 a39, v123\n"
"v_mfma_f32_16x16x32_bf16 v[240:243], a[144:147], a[236:239], v[240:243]\n"
"v_mfma_f32_16x16x32_bf16 v[244:247], a[148:151], a[236:239], v[244:247]\n"
"v_mfma_f32_16x16x32_bf16 v[248:251], a[152:155], a[236:239], v[248:251]\n"
"v_mfma_f32_16x16x32_bf16 v[252:255], a[156:159], a[236:239], v[252:255]\n"
"global_load_dwordx4 a[236:239], v31, s[46:47] offset:2048 sc1\n"
"s_waitcnt vmcnt(16) lgkmcnt(0)\n"
"v_add_u32 v26, 0x10000, v26\n"
"v_add_u32 v27, 0x10000, v27\n"
"v_add_u32 v28, 0x10000, v28\n"
"v_add_u32 v29, 0x10000, v29\n"
"v_mfma_f32_16x16x32_bf16 v[128:131], a[160:163], a[240:243], v[128:131]\n"
"ds_read_b64_tr_b16 a[128:129], v26 offset:0\n"
"v_accvgpr_read_b32 v96, a40\n"
"v_mfma_f32_16x16x32_bf16 v[132:135], a[164:167], a[240:243], v[132:135]\n"
"ds_read_b64_tr_b16 a[130:131], v26 offset:512\n"
"v_accvgpr_read_b32 v97, a41\n"
"v_mfma_f32_16x16x32_bf16 v[136:139], a[168:171], a[240:243], v[136:139]\n"
"ds_read_b64_tr_b16 a[132:133], v27 offset:0\n"
"v_accvgpr_read_b32 v98, a42\n"
"v_mfma_f32_16x16x32_bf16 v[140:143], a[172:175], a[240:243], v[140:143]\n"
"global_store_dwordx4 v9, v[48:51], s[62:63] offset:-4096 sc1\n"
"v_mfma_f32_16x16x32_bf16 v[144:147], a[176:179], a[240:243], v[144:147]\n"
"v_accvgpr_read_b32 v99, a43\n"
"v_accvgpr_read_b32 v100, a56\n"
"v_mfma_f32_16x16x32_bf16 v[148:151], a[180:183], a[240:243], v[148:151]\n"
"ds_read_b64_tr_b16 a[134:135], v27 offset:512\n"
"v_accvgpr_read_b32 v101, a57\n"
"v_mfma_f32_16x16x32_bf16 v[152:155], a[184:187], a[240:243], v[152:155]\n"
"ds_read_b64_tr_b16 a[136:137], v28 offset:0\n"
"v_accvgpr_read_b32 v102, a58\n"
"v_mfma_f32_16x16x32_bf16 v[156:159], a[188:191], a[240:243], v[156:159]\n"
"global_load_dwordx4 a[240:243], v30, s[44:45] offset:3072 sc1\n"
"v_mfma_f32_16x16x32_bf16 v[160:163], a[160:163], a[244:247], v[160:163]\n"
"v_accvgpr_read_b32 v103, a59\n"
"v_cvt_pk_bf16_f32 v104, v96, v97\n"
"v_mfma_f32_16x16x32_bf16 v[164:167], a[164:167], a[244:247], v[164:167]\n"
"ds_read_b64_tr_b16 a[138:139], v28 offset:512\n"
"v_cvt_pk_bf16_f32 v105, v98, v99\n"
"v_mfma_f32_16x16x32_bf16 v[168:171], a[168:171], a[244:247], v[168:171]\n"
"ds_read_b64_tr_b16 a[140:141], v29 offset:0\n"
"v_cvt_pk_bf16_f32 v106, v100, v101\n"
"v_mfma_f32_16x16x32_bf16 v[172:175], a[172:175], a[244:247], v[172:175]\n"
"global_store_dwordx4 v9, v[52:55], s[62:63] offset:-3072 sc1\n"
"v_mfma_f32_16x16x32_bf16 v[176:179], a[176:179], a[244:247], v[176:179]\n"
"v_cvt_pk_bf16_f32 v107, v102, v103\n"
"v_accvgpr_write_b32 a40, v104\n"
"v_mfma_f32_16x16x32_bf16 v[180:183], a[180:183], a[244:247], v[180:183]\n"
"ds_read_b64_tr_b16 a[142:143], v29 offset:512\n"
"v_accvgpr_write_b32 a41, v105\n"
"v_mfma_f32_16x16x32_bf16 v[184:187], a[184:187], a[244:247], v[184:187]\n"
"ds_read_b64_tr_b16 a[144:145], v26 offset:8192\n"
"v_accvgpr_write_b32 a42, v106\n"
"v_mfma_f32_16x16x32_bf16 v[188:191], a[188:191], a[244:247], v[188:191]\n"
"global_load_dwordx4 a[244:247], v31, s[44:45] offset:3072 sc1\n"
"s_add_u32 s44, s44, 8192\n"
"s_addc_u32 s45, s45, 0\n"
"v_mfma_f32_16x16x32_bf16 v[192:195], a[160:163], a[248:251], v[192:195]\n"
"ds_read_b64_tr_b16 a[146:147], v26 offset:8704\n"
"v_accvgpr_write_b32 a43, v107\n"
"v_mfma_f32_16x16x32_bf16 v[196:199], a[164:167], a[248:251], v[196:199]\n"
"v_accvgpr_read_b32 v112, a44\n"
"v_accvgpr_read_b32 v113, a45\n"
"v_mfma_f32_16x16x32_bf16 v[200:203], a[168:171], a[248:251], v[200:203]\n"
"ds_read_b64_tr_b16 a[148:149], v27 offset:8192\n"
"v_accvgpr_read_b32 v114, a46\n"
"v_mfma_f32_16x16x32_bf16 v[204:207], a[172:175], a[248:251], v[204:207]\n"
"ds_read_b64_tr_b16 a[150:151], v27 offset:8704\n"
"v_accvgpr_read_b32 v115, a47\n"
"v_mfma_f32_16x16x32_bf16 v[208:211], a[176:179], a[248:251], v[208:211]\n"
"v_accvgpr_read_b32 v116, a60\n"
"v_accvgpr_read_b32 v117, a61\n"
"v_mfma_f32_16x16x32_bf16 v[212:215], a[180:183], a[248:251], v[212:215]\n"
"ds_read_b64_tr_b16 a[152:153], v28 offset:8192\n"
"v_accvgpr_read_b32 v118, a62\n"
"v_mfma_f32_16x16x32_bf16 v[216:219], a[184:187], a[248:251], v[216:219]\n"
"ds_read_b64_tr_b16 a[154:155], v28 offset:8704\n"
"v_accvgpr_read_b32 v119, a63\n"
"v_mfma_f32_16x16x32_bf16 v[220:223], a[188:191], a[248:251], v[220:223]\n"
"global_load_dwordx4 a[248:251], v30, s[46:47] offset:3072 sc1\n"
"v_mfma_f32_16x16x32_bf16 v[224:227], a[160:163], a[252:255], v[224:227]\n"
"v_cvt_pk_bf16_f32 v120, v112, v113\n"
"v_cvt_pk_bf16_f32 v121, v114, v115\n"
"v_mfma_f32_16x16x32_bf16 v[228:231], a[164:167], a[252:255], v[228:231]\n"
"ds_read_b64_tr_b16 a[156:157], v29 offset:8192\n"
"v_cvt_pk_bf16_f32 v122, v116, v117\n"
"v_mfma_f32_16x16x32_bf16 v[232:235], a[168:171], a[252:255], v[232:235]\n"
"ds_read_b64_tr_b16 a[158:159], v29 offset:8704\n"
"v_cvt_pk_bf16_f32 v123, v118, v119\n"
"v_mfma_f32_16x16x32_bf16 v[236:239], a[172:175], a[252:255], v[236:239]\n"
"v_accvgpr_write_b32 a44, v120\n"
"v_accvgpr_write_b32 a45, v121\n"
"v_mfma_f32_16x16x32_bf16 v[240:243], a[176:179], a[252:255], v[240:243]\n"
"v_accvgpr_write_b32 a46, v122\n"
"v_accvgpr_write_b32 a47, v123\n"
"v_mfma_f32_16x16x32_bf16 v[244:247], a[180:183], a[252:255], v[244:247]\n"
"v_mfma_f32_16x16x32_bf16 v[248:251], a[184:187], a[252:255], v[248:251]\n"
"v_mfma_f32_16x16x32_bf16 v[252:255], a[188:191], a[252:255], v[252:255]\n"
"global_load_dwordx4 a[252:255], v31, s[46:47] offset:3072 sc1\n"
"s_add_u32 s46, s46, 8192\n"
"s_addc_u32 s47, s47, 0\n"
"s_waitcnt vmcnt(16) lgkmcnt(0)\n"
"v_mfma_f32_16x16x32_bf16 v[128:131], a[128:131], a[192:195], v[128:131]\n"
"ds_read_b64_tr_b16 a[160:161], v26 offset:4096\n"
"v_accvgpr_read_b32 v96, a64\n"
"v_mfma_f32_16x16x32_bf16 v[132:135], a[132:135], a[192:195], v[132:135]\n"
"ds_read_b64_tr_b16 a[162:163], v26 offset:4608\n"
"v_accvgpr_read_b32 v97, a65\n"
"v_mfma_f32_16x16x32_bf16 v[136:139], a[136:139], a[192:195], v[136:139]\n"
"v_accvgpr_read_b32 v98, a66\n"
"v_accvgpr_read_b32 v99, a67\n"
"v_mfma_f32_16x16x32_bf16 v[140:143], a[140:143], a[192:195], v[140:143]\n"
"global_store_dwordx4 v9, v[56:59], s[62:63] offset:0 sc1\n"
"v_mfma_f32_16x16x32_bf16 v[144:147], a[144:147], a[192:195], v[144:147]\n"
"ds_read_b64_tr_b16 a[164:165], v27 offset:4096\n"
"v_accvgpr_read_b32 v100, a80\n"
"v_mfma_f32_16x16x32_bf16 v[148:151], a[148:151], a[192:195], v[148:151]\n"
"ds_read_b64_tr_b16 a[166:167], v27 offset:4608\n"
"v_accvgpr_read_b32 v101, a81\n"
"v_mfma_f32_16x16x32_bf16 v[152:155], a[152:155], a[192:195], v[152:155]\n"
"v_accvgpr_read_b32 v102, a82\n"
"v_accvgpr_read_b32 v103, a83\n"
"v_mfma_f32_16x16x32_bf16 v[156:159], a[156:159], a[192:195], v[156:159]\n"
"global_load_dwordx4 a[192:195], v30, s[44:45] offset:-4096 sc1\n"
"v_mfma_f32_16x16x32_bf16 v[160:163], a[128:131], a[196:199], v[160:163]\n"
"ds_read_b64_tr_b16 a[168:169], v28 offset:4096\n"
"v_cvt_pk_bf16_f32 v104, v96, v97\n"
"v_mfma_f32_16x16x32_bf16 v[164:167], a[132:135], a[196:199], v[164:167]\n"
"ds_read_b64_tr_b16 a[170:171], v28 offset:4608\n"
"v_cvt_pk_bf16_f32 v105, v98, v99\n"
"v_mfma_f32_16x16x32_bf16 v[168:171], a[136:139], a[196:199], v[168:171]\n"
"v_cvt_pk_bf16_f32 v106, v100, v101\n"
"v_cvt_pk_bf16_f32 v107, v102, v103\n"
"v_mfma_f32_16x16x32_bf16 v[172:175], a[140:143], a[196:199], v[172:175]\n"
"ds_read_b64_tr_b16 a[172:173], v29 offset:4096\n"
"v_accvgpr_write_b32 a64, v104\n"
"v_mfma_f32_16x16x32_bf16 v[176:179], a[144:147], a[196:199], v[176:179]\n"
"ds_read_b64_tr_b16 a[174:175], v29 offset:4608\n"
"v_accvgpr_write_b32 a65, v105\n"
"v_mfma_f32_16x16x32_bf16 v[180:183], a[148:151], a[196:199], v[180:183]\n"
"v_accvgpr_write_b32 a66, v106\n"
"v_accvgpr_write_b32 a67, v107\n"
"v_mfma_f32_16x16x32_bf16 v[184:187], a[152:155], a[196:199], v[184:187]\n"
"ds_read_b64_tr_b16 a[176:177], v26 offset:12288\n"
"v_accvgpr_read_b32 v112, a68\n"
"v_mfma_f32_16x16x32_bf16 v[188:191], a[156:159], a[196:199], v[188:191]\n"
"global_load_dwordx4 a[196:199], v31, s[44:45] offset:-4096 sc1\n"
"v_mfma_f32_16x16x32_bf16 v[192:195], a[128:131], a[200:203], v[192:195]\n"
"ds_read_b64_tr_b16 a[178:179], v26 offset:12800\n"
"v_accvgpr_read_b32 v113, a69\n"
"v_mfma_f32_16x16x32_bf16 v[196:199], a[132:135], a[200:203], v[196:199]\n"
"v_accvgpr_read_b32 v114, a70\n"
"v_accvgpr_read_b32 v115, a71\n"
"v_mfma_f32_16x16x32_bf16 v[200:203], a[136:139], a[200:203], v[200:203]\n"
"ds_read_b64_tr_b16 a[180:181], v27 offset:12288\n"
"v_accvgpr_read_b32 v116, a84\n"
"v_mfma_f32_16x16x32_bf16 v[204:207], a[140:143], a[200:203], v[204:207]\n"
"ds_read_b64_tr_b16 a[182:183], v27 offset:12800\n"
"v_accvgpr_read_b32 v117, a85\n"
"v_mfma_f32_16x16x32_bf16 v[208:211], a[144:147], a[200:203], v[208:211]\n"
"v_accvgpr_read_b32 v118, a86\n"
"v_accvgpr_read_b32 v119, a87\n"
"v_mfma_f32_16x16x32_bf16 v[212:215], a[148:151], a[200:203], v[212:215]\n"
"ds_read_b64_tr_b16 a[184:185], v28 offset:12288\n"
"v_cvt_pk_bf16_f32 v120, v112, v113\n"
"v_mfma_f32_16x16x32_bf16 v[216:219], a[152:155], a[200:203], v[216:219]\n"
"ds_read_b64_tr_b16 a[186:187], v28 offset:12800\n"
"v_cvt_pk_bf16_f32 v121, v114, v115\n"
"v_mfma_f32_16x16x32_bf16 v[220:223], a[156:159], a[200:203], v[220:223]\n"
"global_load_dwordx4 a[200:203], v30, s[46:47] offset:-4096 sc1\n"
"v_mfma_f32_16x16x32_bf16 v[224:227], a[128:131], a[204:207], v[224:227]\n"
"v_cvt_pk_bf16_f32 v122, v116, v117\n"
"v_cvt_pk_bf16_f32 v123, v118, v119\n"
"v_mfma_f32_16x16x32_bf16 v[228:231], a[132:135], a[204:207], v[228:231]\n"
"ds_read_b64_tr_b16 a[188:189], v29 offset:12288\n"
"v_accvgpr_write_b32 a68, v120\n"
"v_mfma_f32_16x16x32_bf16 v[232:235], a[136:139], a[204:207], v[232:235]\n"
"ds_read_b64_tr_b16 a[190:191], v29 offset:12800\n"
"v_accvgpr_write_b32 a69, v121\n"
"v_mfma_f32_16x16x32_bf16 v[236:239], a[140:143], a[204:207], v[236:239]\n"
"v_accvgpr_write_b32 a70, v122\n"
"v_accvgpr_write_b32 a71, v123\n"
"v_mfma_f32_16x16x32_bf16 v[240:243], a[144:147], a[204:207], v[240:243]\n"
"v_mfma_f32_16x16x32_bf16 v[244:247], a[148:151], a[204:207], v[244:247]\n"
"v_mfma_f32_16x16x32_bf16 v[248:251], a[152:155], a[204:207], v[248:251]\n"
"v_mfma_f32_16x16x32_bf16 v[252:255], a[156:159], a[204:207], v[252:255]\n"
"global_load_dwordx4 a[204:207], v31, s[46:47] offset:-4096 sc1\n"
"s_waitcnt vmcnt(16) lgkmcnt(0)\n"
"v_mfma_f32_16x16x32_bf16 v[128:131], a[160:163], a[208:211], v[128:131]\n"
"ds_read_b64_tr_b16 a[128:129], v26 offset:16384\n"
"v_accvgpr_read_b32 v96, a72\n"
"v_mfma_f32_16x16x32_bf16 v[132:135], a[164:167], a[208:211], v[132:135]\n"
"ds_read_b64_tr_b16 a[130:131], v26 offset:16896\n"
"v_accvgpr_read_b32 v97, a73\n"
"v_mfma_f32_16x16x32_bf16 v[136:139], a[168:171], a[208:211], v[136:139]\n"
"v_accvgpr_read_b32 v98, a74\n"
"v_accvgpr_read_b32 v99, a75\n"
"v_mfma_f32_16x16x32_bf16 v[140:143], a[172:175], a[208:211], v[140:143]\n"
"global_store_dwordx4 v9, v[60:63], s[62:63] offset:1024 sc1\n"
"v_mfma_f32_16x16x32_bf16 v[144:147], a[176:179], a[208:211], v[144:147]\n"
"ds_read_b64_tr_b16 a[132:133], v27 offset:16384\n"
"v_accvgpr_read_b32 v100, a88\n"
"v_mfma_f32_16x16x32_bf16 v[148:151], a[180:183], a[208:211], v[148:151]\n"
"ds_read_b64_tr_b16 a[134:135], v27 offset:16896\n"
"v_accvgpr_read_b32 v101, a89\n"
"v_mfma_f32_16x16x32_bf16 v[152:155], a[184:187], a[208:211], v[152:155]\n"
"v_accvgpr_read_b32 v102, a90\n"
"v_accvgpr_read_b32 v103, a91\n"
"v_mfma_f32_16x16x32_bf16 v[156:159], a[188:191], a[208:211], v[156:159]\n"
"global_load_dwordx4 a[208:211], v30, s[44:45] offset:-3072 sc1\n"
"v_mfma_f32_16x16x32_bf16 v[160:163], a[160:163], a[212:215], v[160:163]\n"
"ds_read_b64_tr_b16 a[136:137], v28 offset:16384\n"
"v_cvt_pk_bf16_f32 v104, v96, v97\n"
"v_mfma_f32_16x16x32_bf16 v[164:167], a[164:167], a[212:215], v[164:167]\n"
"ds_read_b64_tr_b16 a[138:139], v28 offset:16896\n"
"v_cvt_pk_bf16_f32 v105, v98, v99\n"
"v_mfma_f32_16x16x32_bf16 v[168:171], a[168:171], a[212:215], v[168:171]\n"
"v_cvt_pk_bf16_f32 v106, v100, v101\n"
"v_cvt_pk_bf16_f32 v107, v102, v103\n"
"v_mfma_f32_16x16x32_bf16 v[172:175], a[172:175], a[212:215], v[172:175]\n"
"ds_read_b64_tr_b16 a[140:141], v29 offset:16384\n"
"v_accvgpr_write_b32 a72, v104\n"
"v_mfma_f32_16x16x32_bf16 v[176:179], a[176:179], a[212:215], v[176:179]\n"
"ds_read_b64_tr_b16 a[142:143], v29 offset:16896\n"
"v_accvgpr_write_b32 a73, v105\n"
"v_mfma_f32_16x16x32_bf16 v[180:183], a[180:183], a[212:215], v[180:183]\n"
"v_accvgpr_write_b32 a74, v106\n"
"v_accvgpr_write_b32 a75, v107\n"
"v_mfma_f32_16x16x32_bf16 v[184:187], a[184:187], a[212:215], v[184:187]\n"
"ds_read_b64_tr_b16 a[144:145], v26 offset:24576\n"
"v_accvgpr_read_b32 v112, a76\n"
"v_mfma_f32_16x16x32_bf16 v[188:191], a[188:191], a[212:215], v[188:191]\n"
"global_load_dwordx4 a[212:215], v31, s[44:45] offset:-3072 sc1\n"
"v_mfma_f32_16x16x32_bf16 v[192:195], a[160:163], a[216:219], v[192:195]\n"
"ds_read_b64_tr_b16 a[146:147], v26 offset:25088\n"
"v_accvgpr_read_b32 v113, a77\n"
"v_mfma_f32_16x16x32_bf16 v[196:199], a[164:167], a[216:219], v[196:199]\n"
"v_accvgpr_read_b32 v114, a78\n"
"v_accvgpr_read_b32 v115, a79\n"
"v_mfma_f32_16x16x32_bf16 v[200:203], a[168:171], a[216:219], v[200:203]\n"
"ds_read_b64_tr_b16 a[148:149], v27 offset:24576\n"
"v_accvgpr_read_b32 v116, a92\n"
"v_mfma_f32_16x16x32_bf16 v[204:207], a[172:175], a[216:219], v[204:207]\n"
"ds_read_b64_tr_b16 a[150:151], v27 offset:25088\n"
"v_accvgpr_read_b32 v117, a93\n"
"v_mfma_f32_16x16x32_bf16 v[208:211], a[176:179], a[216:219], v[208:211]\n"
"v_accvgpr_read_b32 v118, a94\n"
"v_accvgpr_read_b32 v119, a95\n"
"v_mfma_f32_16x16x32_bf16 v[212:215], a[180:183], a[216:219], v[212:215]\n"
"ds_read_b64_tr_b16 a[152:153], v28 offset:24576\n"
"v_cvt_pk_bf16_f32 v120, v112, v113\n"
"v_mfma_f32_16x16x32_bf16 v[216:219], a[184:187], a[216:219], v[216:219]\n"
"ds_read_b64_tr_b16 a[154:155], v28 offset:25088\n"
"v_cvt_pk_bf16_f32 v121, v114, v115\n"
"v_mfma_f32_16x16x32_bf16 v[220:223], a[188:191], a[216:219], v[220:223]\n"
"global_load_dwordx4 a[216:219], v30, s[46:47] offset:-3072 sc1\n"
"v_mfma_f32_16x16x32_bf16 v[224:227], a[160:163], a[220:223], v[224:227]\n"
"v_cvt_pk_bf16_f32 v122, v116, v117\n"
"v_cvt_pk_bf16_f32 v123, v118, v119\n"
"v_mfma_f32_16x16x32_bf16 v[228:231], a[164:167], a[220:223], v[228:231]\n"
"ds_read_b64_tr_b16 a[156:157], v29 offset:24576\n"
"v_accvgpr_write_b32 a76, v120\n"
"v_mfma_f32_16x16x32_bf16 v[232:235], a[168:171], a[220:223], v[232:235]\n"
"ds_read_b64_tr_b16 a[158:159], v29 offset:25088\n"
"v_accvgpr_write_b32 a77, v121\n"
"v_mfma_f32_16x16x32_bf16 v[236:239], a[172:175], a[220:223], v[236:239]\n"
"v_accvgpr_write_b32 a78, v122\n"
"v_accvgpr_write_b32 a79, v123\n"
"v_mfma_f32_16x16x32_bf16 v[240:243], a[176:179], a[220:223], v[240:243]\n"
"v_mfma_f32_16x16x32_bf16 v[244:247], a[180:183], a[220:223], v[244:247]\n"
"v_mfma_f32_16x16x32_bf16 v[248:251], a[184:187], a[220:223], v[248:251]\n"
"v_mfma_f32_16x16x32_bf16 v[252:255], a[188:191], a[220:223], v[252:255]\n"
"global_load_dwordx4 a[220:223], v31, s[46:47] offset:-3072 sc1\n"
"s_waitcnt vmcnt(16) lgkmcnt(0)\n"
"v_mfma_f32_16x16x32_bf16 v[128:131], a[128:131], a[224:227], v[128:131]\n"
"ds_read_b64_tr_b16 a[160:161], v26 offset:20480\n"
"v_accvgpr_read_b32 v96, a96\n"
"v_mfma_f32_16x16x32_bf16 v[132:135], a[132:135], a[224:227], v[132:135]\n"
"ds_read_b64_tr_b16 a[162:163], v26 offset:20992\n"
"v_accvgpr_read_b32 v97, a97\n"
"v_mfma_f32_16x16x32_bf16 v[136:139], a[136:139], a[224:227], v[136:139]\n"
"v_accvgpr_read_b32 v98, a98\n"
"v_accvgpr_read_b32 v99, a99\n"
"v_mfma_f32_16x16x32_bf16 v[140:143], a[140:143], a[224:227], v[140:143]\n"
"global_store_dwordx4 v12, v[64:67], s[62:63] offset:-4096 sc1\n"
"v_mfma_f32_16x16x32_bf16 v[144:147], a[144:147], a[224:227], v[144:147]\n"
"ds_read_b64_tr_b16 a[164:165], v27 offset:20480\n"
"v_accvgpr_read_b32 v100, a112\n"
"v_mfma_f32_16x16x32_bf16 v[148:151], a[148:151], a[224:227], v[148:151]\n"
"ds_read_b64_tr_b16 a[166:167], v27 offset:20992\n"
"v_accvgpr_read_b32 v101, a113\n"
"v_mfma_f32_16x16x32_bf16 v[152:155], a[152:155], a[224:227], v[152:155]\n"
"v_accvgpr_read_b32 v102, a114\n"
"v_accvgpr_read_b32 v103, a115\n"
"v_mfma_f32_16x16x32_bf16 v[156:159], a[156:159], a[224:227], v[156:159]\n"
"global_load_dwordx4 a[224:227], v30, s[44:45] offset:-2048 sc1\n"
"v_mfma_f32_16x16x32_bf16 v[160:163], a[128:131], a[228:231], v[160:163]\n"
"ds_read_b64_tr_b16 a[168:169], v28 offset:20480\n"
"v_cvt_pk_bf16_f32 v104, v96, v97\n"
"v_mfma_f32_16x16x32_bf16 v[164:167], a[132:135], a[228:231], v[164:167]\n"
"ds_read_b64_tr_b16 a[170:171], v28 offset:20992\n"
"v_cvt_pk_bf16_f32 v105, v98, v99\n"
"v_mfma_f32_16x16x32_bf16 v[168:171], a[136:139], a[228:231], v[168:171]\n"
"v_cvt_pk_bf16_f32 v106, v100, v101\n"
"v_cvt_pk_bf16_f32 v107, v102, v103\n"
"v_mfma_f32_16x16x32_bf16 v[172:175], a[140:143], a[228:231], v[172:175]\n"
"ds_read_b64_tr_b16 a[172:173], v29 offset:20480\n"
"v_accvgpr_write_b32 a96, v104\n"
"v_mfma_f32_16x16x32_bf16 v[176:179], a[144:147], a[228:231], v[176:179]\n"
"ds_read_b64_tr_b16 a[174:175], v29 offset:20992\n"
"v_accvgpr_write_b32 a97, v105\n"
"v_mfma_f32_16x16x32_bf16 v[180:183], a[148:151], a[228:231], v[180:183]\n"
"v_accvgpr_write_b32 a98, v106\n"
"v_accvgpr_write_b32 a99, v107\n"
"v_mfma_f32_16x16x32_bf16 v[184:187], a[152:155], a[228:231], v[184:187]\n"
"ds_read_b64_tr_b16 a[176:177], v26 offset:28672\n"
"v_accvgpr_read_b32 v112, a100\n"
"v_mfma_f32_16x16x32_bf16 v[188:191], a[156:159], a[228:231], v[188:191]\n"
"global_load_dwordx4 a[228:231], v31, s[44:45] offset:-2048 sc1\n"
"v_mfma_f32_16x16x32_bf16 v[192:195], a[128:131], a[232:235], v[192:195]\n"
"ds_read_b64_tr_b16 a[178:179], v26 offset:29184\n"
"v_accvgpr_read_b32 v113, a101\n"
"v_mfma_f32_16x16x32_bf16 v[196:199], a[132:135], a[232:235], v[196:199]\n"
"v_accvgpr_read_b32 v114, a102\n"
"v_accvgpr_read_b32 v115, a103\n"
"v_mfma_f32_16x16x32_bf16 v[200:203], a[136:139], a[232:235], v[200:203]\n"
"ds_read_b64_tr_b16 a[180:181], v27 offset:28672\n"
"v_accvgpr_read_b32 v116, a116\n"
"v_mfma_f32_16x16x32_bf16 v[204:207], a[140:143], a[232:235], v[204:207]\n"
"ds_read_b64_tr_b16 a[182:183], v27 offset:29184\n"
"v_accvgpr_read_b32 v117, a117\n"
"v_mfma_f32_16x16x32_bf16 v[208:211], a[144:147], a[232:235], v[208:211]\n"
"v_accvgpr_read_b32 v118, a118\n"
"v_accvgpr_read_b32 v119, a119\n"
"v_mfma_f32_16x16x32_bf16 v[212:215], a[148:151], a[232:235], v[212:215]\n"
"ds_read_b64_tr_b16 a[184:185], v28 offset:28672\n"
"v_cvt_pk_bf16_f32 v120, v112, v113\n"
"v_mfma_f32_16x16x32_bf16 v[216:219], a[152:155], a[232:235], v[216:219]\n"
"ds_read_b64_tr_b16 a[186:187], v28 offset:29184\n"
"v_cvt_pk_bf16_f32 v121, v114, v115\n"
"v_mfma_f32_16x16x32_bf16 v[220:223], a[156:159], a[232:235], v[220:223]\n"
"global_load_dwordx4 a[232:235], v30, s[46:47] offset:-2048 sc1\n"
"v_mfma_f32_16x16x32_bf16 v[224:227], a[128:131], a[236:239], v[224:227]\n"
"v_cvt_pk_bf16_f32 v122, v116, v117\n"
"v_cvt_pk_bf16_f32 v123, v118, v119\n"
"v_mfma_f32_16x16x32_bf16 v[228:231], a[132:135], a[236:239], v[228:231]\n"
"ds_read_b64_tr_b16 a[188:189], v29 offset:28672\n"
"v_accvgpr_write_b32 a100, v120\n"
"v_mfma_f32_16x16x32_bf16 v[232:235], a[136:139], a[236:239], v[232:235]\n"
"ds_read_b64_tr_b16 a[190:191], v29 offset:29184\n"
"v_accvgpr_write_b32 a101, v121\n"
"v_mfma_f32_16x16x32_bf16 v[236:239], a[140:143], a[236:239], v[236:239]\n"
"v_accvgpr_write_b32 a102, v122\n"
"v_accvgpr_write_b32 a103, v123\n"
"v_mfma_f32_16x16x32_bf16 v[240:243], a[144:147], a[236:239], v[240:243]\n"
"v_mfma_f32_16x16x32_bf16 v[244:247], a[148:151], a[236:239], v[244:247]\n"
"v_mfma_f32_16x16x32_bf16 v[248:251], a[152:155], a[236:239], v[248:251]\n"
"v_mfma_f32_16x16x32_bf16 v[252:255], a[156:159], a[236:239], v[252:255]\n"
"global_load_dwordx4 a[236:239], v31, s[46:47] offset:-2048 sc1\n"
"s_waitcnt vmcnt(15) lgkmcnt(0)\n"
"v_add_u32 v26, 0xffff0000, v26\n"
"v_add_u32 v27, 0xffff0000, v27\n"
"v_add_u32 v28, 0xffff0000, v28\n"
"v_add_u32 v29, 0xffff0000, v29\n"
"v_mfma_f32_16x16x32_bf16 v[128:131], a[160:163], a[240:243], v[128:131]\n"
"ds_read_b64_tr_b16 a[128:129], v26 offset:32768\n"
"v_accvgpr_read_b32 v96, a104\n"
"v_mfma_f32_16x16x32_bf16 v[132:135], a[164:167], a[240:243], v[132:135]\n"
"ds_read_b64_tr_b16 a[130:131], v26 offset:33280\n"
"v_accvgpr_read_b32 v97, a105\n"
"v_mfma_f32_16x16x32_bf16 v[136:139], a[168:171], a[240:243], v[136:139]\n"
"ds_read_b64_tr_b16 a[132:133], v27 offset:32768\n"
"v_accvgpr_read_b32 v98, a106\n"
"v_mfma_f32_16x16x32_bf16 v[140:143], a[172:175], a[240:243], v[140:143]\n"
"global_store_dwordx4 v12, v[68:71], s[62:63] offset:-3072 sc1\n"
"v_mfma_f32_16x16x32_bf16 v[144:147], a[176:179], a[240:243], v[144:147]\n"
"v_accvgpr_read_b32 v99, a107\n"
"v_accvgpr_read_b32 v100, a120\n"
"v_mfma_f32_16x16x32_bf16 v[148:151], a[180:183], a[240:243], v[148:151]\n"
"ds_read_b64_tr_b16 a[134:135], v27 offset:33280\n"
"v_accvgpr_read_b32 v101, a121\n"
"v_mfma_f32_16x16x32_bf16 v[152:155], a[184:187], a[240:243], v[152:155]\n"
"ds_read_b64_tr_b16 a[136:137], v28 offset:32768\n"
"v_accvgpr_read_b32 v102, a122\n"
"v_mfma_f32_16x16x32_bf16 v[156:159], a[188:191], a[240:243], v[156:159]\n"
"global_load_dwordx4 a[240:243], v30, s[44:45] offset:-1024 sc1\n"
"v_mfma_f32_16x16x32_bf16 v[160:163], a[160:163], a[244:247], v[160:163]\n"
"v_accvgpr_read_b32 v103, a123\n"
"v_cvt_pk_bf16_f32 v104, v96, v97\n"
"v_mfma_f32_16x16x32_bf16 v[164:167], a[164:167], a[244:247], v[164:167]\n"
"ds_read_b64_tr_b16 a[138:139], v28 offset:33280\n"
"v_cvt_pk_bf16_f32 v105, v98, v99\n"
"v_mfma_f32_16x16x32_bf16 v[168:171], a[168:171], a[244:247], v[168:171]\n"
"ds_read_b64_tr_b16 a[140:141], v29 offset:32768\n"
"v_cvt_pk_bf16_f32 v106, v100, v101\n"
"v_mfma_f32_16x16x32_bf16 v[172:175], a[172:175], a[244:247], v[172:175]\n"
"global_store_dwordx4 v12, v[72:75], s[62:63] offset:0 sc1\n"
"v_mfma_f32_16x16x32_bf16 v[176:179], a[176:179], a[244:247], v[176:179]\n"
"v_cvt_pk_bf16_f32 v107, v102, v103\n"
"v_accvgpr_write_b32 a104, v104\n"
"v_mfma_f32_16x16x32_bf16 v[180:183], a[180:183], a[244:247], v[180:183]\n"
"ds_read_b64_tr_b16 a[142:143], v29 offset:33280\n"
"v_accvgpr_write_b32 a105, v105\n"
"v_mfma_f32_16x16x32_bf16 v[184:187], a[184:187], a[244:247], v[184:187]\n"
"ds_read_b64_tr_b16 a[144:145], v26 offset:40960\n"
"v_accvgpr_write_b32 a106, v106\n"
"v_mfma_f32_16x16x32_bf16 v[188:191], a[188:191], a[244:247], v[188:191]\n"
"global_load_dwordx4 a[244:247], v31, s[44:45] offset:-1024 sc1\n"
"v_mfma_f32_16x16x32_bf16 v[192:195], a[160:163], a[248:251], v[192:195]\n"
"ds_read_b64_tr_b16 a[146:147], v26 offset:41472\n"
"v_accvgpr_write_b32 a107, v107\n"
"v_mfma_f32_16x16x32_bf16 v[196:199], a[164:167], a[248:251], v[196:199]\n"
"v_accvgpr_read_b32 v112, a108\n"
"v_accvgpr_read_b32 v113, a109\n"
"v_mfma_f32_16x16x32_bf16 v[200:203], a[168:171], a[248:251], v[200:203]\n"
"ds_read_b64_tr_b16 a[148:149], v27 offset:40960\n"
"v_accvgpr_read_b32 v114, a110\n"
"v_mfma_f32_16x16x32_bf16 v[204:207], a[172:175], a[248:251], v[204:207]\n"
"ds_read_b64_tr_b16 a[150:151], v27 offset:41472\n"
"v_accvgpr_read_b32 v115, a111\n"
"v_mfma_f32_16x16x32_bf16 v[208:211], a[176:179], a[248:251], v[208:211]\n"
"v_accvgpr_read_b32 v116, a124\n"
"v_accvgpr_read_b32 v117, a125\n"
"v_mfma_f32_16x16x32_bf16 v[212:215], a[180:183], a[248:251], v[212:215]\n"
"ds_read_b64_tr_b16 a[152:153], v28 offset:40960\n"
"v_accvgpr_read_b32 v118, a126\n"
"v_mfma_f32_16x16x32_bf16 v[216:219], a[184:187], a[248:251], v[216:219]\n"
"ds_read_b64_tr_b16 a[154:155], v28 offset:41472\n"
"v_accvgpr_read_b32 v119, a127\n"
"v_mfma_f32_16x16x32_bf16 v[220:223], a[188:191], a[248:251], v[220:223]\n"
"global_load_dwordx4 a[248:251], v30, s[46:47] offset:-1024 sc1\n"
"v_mfma_f32_16x16x32_bf16 v[224:227], a[160:163], a[252:255], v[224:227]\n"
"v_cvt_pk_bf16_f32 v120, v112, v113\n"
"v_cvt_pk_bf16_f32 v121, v114, v115\n"
"v_mfma_f32_16x16x32_bf16 v[228:231], a[164:167], a[252:255], v[228:231]\n"
"ds_read_b64_tr_b16 a[156:157], v29 offset:40960\n"
"v_cvt_pk_bf16_f32 v122, v116, v117\n"
"v_mfma_f32_16x16x32_bf16 v[232:235], a[168:171], a[252:255], v[232:235]\n"
"ds_read_b64_tr_b16 a[158:159], v29 offset:41472\n"
"v_cvt_pk_bf16_f32 v123, v118, v119\n"
"v_mfma_f32_16x16x32_bf16 v[236:239], a[172:175], a[252:255], v[236:239]\n"
"v_accvgpr_write_b32 a108, v120\n"
"v_accvgpr_write_b32 a109, v121\n"
"v_mfma_f32_16x16x32_bf16 v[240:243], a[176:179], a[252:255], v[240:243]\n"
"v_accvgpr_write_b32 a110, v122\n"
"v_accvgpr_write_b32 a111, v123\n"
"v_mfma_f32_16x16x32_bf16 v[244:247], a[180:183], a[252:255], v[244:247]\n"
"v_mfma_f32_16x16x32_bf16 v[248:251], a[184:187], a[252:255], v[248:251]\n"
"v_mfma_f32_16x16x32_bf16 v[252:255], a[188:191], a[252:255], v[252:255]\n"
"global_load_dwordx4 a[252:255], v31, s[46:47] offset:-1024 sc1\n"
"s_waitcnt vmcnt(16) lgkmcnt(0)\n"
"s_barrier\n"
"v_mfma_f32_16x16x32_bf16 v[128:131], a[128:131], a[192:195], v[128:131]\n"
"ds_read_b64_tr_b16 a[160:161], v26 offset:36864\n"
"v_mfma_f32_16x16x32_bf16 v[132:135], a[132:135], a[192:195], v[132:135]\n"
"ds_write_b128 v16, v[32:35] offset:0\n"
"v_mfma_f32_16x16x32_bf16 v[136:139], a[136:139], a[192:195], v[136:139]\n"
"ds_read_b64_tr_b16 a[162:163], v26 offset:37376\n"
"v_mfma_f32_16x16x32_bf16 v[140:143], a[140:143], a[192:195], v[140:143]\n"
"global_store_dwordx4 v12, v[76:79], s[62:63] offset:1024 sc1\n"
"v_mfma_f32_16x16x32_bf16 v[144:147], a[144:147], a[192:195], v[144:147]\n"
"ds_read_b64_tr_b16 a[164:165], v27 offset:36864\n"
"v_mfma_f32_16x16x32_bf16 v[148:151], a[148:151], a[192:195], v[148:151]\n"
"ds_read_b64_tr_b16 a[166:167], v27 offset:37376\n"
"v_mfma_f32_16x16x32_bf16 v[152:155], a[152:155], a[192:195], v[152:155]\n"
"v_mfma_f32_16x16x32_bf16 v[156:159], a[156:159], a[192:195], v[156:159]\n"
"global_load_dwordx4 a[192:195], v30, s[44:45] offset:0 sc1\n"
"v_mfma_f32_16x16x32_bf16 v[160:163], a[128:131], a[196:199], v[160:163]\n"
"ds_write_b128 v16, v[36:39] offset:1024\n"
"v_mfma_f32_16x16x32_bf16 v[164:167], a[132:135], a[196:199], v[164:167]\n"
"ds_read_b64_tr_b16 a[168:169], v28 offset:36864\n"
"v_mfma_f32_16x16x32_bf16 v[168:171], a[136:139], a[196:199], v[168:171]\n"
"ds_read_b64_tr_b16 a[170:171], v28 offset:37376\n"
"v_mfma_f32_16x16x32_bf16 v[172:175], a[140:143], a[196:199], v[172:175]\n"
"ds_read_b64_tr_b16 a[172:173], v29 offset:36864\n"
"v_mfma_f32_16x16x32_bf16 v[176:179], a[144:147], a[196:199], v[176:179]\n"
"ds_write_b128 v16, v[40:43] offset:4096\n"
"v_mfma_f32_16x16x32_bf16 v[180:183], a[148:151], a[196:199], v[180:183]\n"
"v_mfma_f32_16x16x32_bf16 v[184:187], a[152:155], a[196:199], v[184:187]\n"
"ds_read_b64_tr_b16 a[174:175], v29 offset:37376\n"
"v_mfma_f32_16x16x32_bf16 v[188:191], a[156:159], a[196:199], v[188:191]\n"
"global_load_dwordx4 a[196:199], v31, s[44:45] offset:0 sc1\n"
"v_mfma_f32_16x16x32_bf16 v[192:195], a[128:131], a[200:203], v[192:195]\n"
"ds_read_b64_tr_b16 a[176:177], v26 offset:45056\n"
"v_mfma_f32_16x16x32_bf16 v[196:199], a[132:135], a[200:203], v[196:199]\n"
"ds_read_b64_tr_b16 a[178:179], v26 offset:45568\n"
"v_mfma_f32_16x16x32_bf16 v[200:203], a[136:139], a[200:203], v[200:203]\n"
"ds_write_b128 v16, v[44:47] offset:5120\n"
"v_mfma_f32_16x16x32_bf16 v[204:207], a[140:143], a[200:203], v[204:207]\n"
"ds_read_b64_tr_b16 a[180:181], v27 offset:45056\n"
"v_mfma_f32_16x16x32_bf16 v[208:211], a[144:147], a[200:203], v[208:211]\n"
"v_mfma_f32_16x16x32_bf16 v[212:215], a[148:151], a[200:203], v[212:215]\n"
"ds_read_b64_tr_b16 a[182:183], v27 offset:45568\n"
"v_mfma_f32_16x16x32_bf16 v[216:219], a[152:155], a[200:203], v[216:219]\n"
"ds_read_b64_tr_b16 a[184:185], v28 offset:45056\n"
"v_mfma_f32_16x16x32_bf16 v[220:223], a[156:159], a[200:203], v[220:223]\n"
"global_load_dwordx4 a[200:203], v30, s[46:47] offset:0 sc1\n"
"v_mfma_f32_16x16x32_bf16 v[224:227], a[128:131], a[204:207], v[224:227]\n"
"ds_read_b64_tr_b16 a[186:187], v28 offset:45568\n"
"v_mfma_f32_16x16x32_bf16 v[228:231], a[132:135], a[204:207], v[228:231]\n"
"ds_read_b64_tr_b16 a[188:189], v29 offset:45056\n"
"v_mfma_f32_16x16x32_bf16 v[232:235], a[136:139], a[204:207], v[232:235]\n"
"ds_read_b64_tr_b16 a[190:191], v29 offset:45568\n"
"v_mfma_f32_16x16x32_bf16 v[236:239], a[140:143], a[204:207], v[236:239]\n"
"v_mfma_f32_16x16x32_bf16 v[240:243], a[144:147], a[204:207], v[240:243]\n"
"v_mfma_f32_16x16x32_bf16 v[244:247], a[148:151], a[204:207], v[244:247]\n"
"v_mfma_f32_16x16x32_bf16 v[248:251], a[152:155], a[204:207], v[248:251]\n"
"v_mfma_f32_16x16x32_bf16 v[252:255], a[156:159], a[204:207], v[252:255]\n"
"global_load_dwordx4 a[204:207], v31, s[46:47] offset:0 sc1\n"
"s_waitcnt vmcnt(16) lgkmcnt(0)\n"
"v_mfma_f32_16x16x32_bf16 v[128:131], a[160:163], a[208:211], v[128:131]\n"
"ds_read_b64_tr_b16 a[128:129], v26 offset:49152\n"
"v_mfma_f32_16x16x32_bf16 v[132:135], a[164:167], a[208:211], v[132:135]\n"
"ds_write_b128 v16, v[48:51] offset:8192\n"
"v_mfma_f32_16x16x32_bf16 v[136:139], a[168:171], a[208:211], v[136:139]\n"
"ds_read_b64_tr_b16 a[130:131], v26 offset:49664\n"
"v_mfma_f32_16x16x32_bf16 v[140:143], a[172:175], a[208:211], v[140:143]\n"
"global_store_dwordx4 v13, v[80:83], s[62:63] offset:-4096 sc1\n"
"v_mfma_f32_16x16x32_bf16 v[144:147], a[176:179], a[208:211], v[144:147]\n"
"ds_read_b64_tr_b16 a[132:133], v27 offset:49152\n"
"v_mfma_f32_16x16x32_bf16 v[148:151], a[180:183], a[208:211], v[148:151]\n"
"v_mfma_f32_16x16x32_bf16 v[152:155], a[184:187], a[208:211], v[152:155]\n"
"ds_read_b64_tr_b16 a[134:135], v27 offset:49664\n"
"v_mfma_f32_16x16x32_bf16 v[156:159], a[188:191], a[208:211], v[156:159]\n"
"global_load_dwordx4 a[208:211], v30, s[44:45] offset:1024 sc1\n"
"v_mfma_f32_16x16x32_bf16 v[160:163], a[160:163], a[212:215], v[160:163]\n"
"ds_write_b128 v16, v[52:55] offset:9216\n"
"v_mfma_f32_16x16x32_bf16 v[164:167], a[164:167], a[212:215], v[164:167]\n"
"ds_read_b64_tr_b16 a[136:137], v28 offset:49152\n"
"v_mfma_f32_16x16x32_bf16 v[168:171], a[168:171], a[212:215], v[168:171]\n"
"ds_read_b64_tr_b16 a[138:139], v28 offset:49664\n"
"v_mfma_f32_16x16x32_bf16 v[172:175], a[172:175], a[212:215], v[172:175]\n"
"v_mfma_f32_16x16x32_bf16 v[176:179], a[176:179], a[212:215], v[176:179]\n"
"ds_read_b64_tr_b16 a[140:141], v29 offset:49152\n"
"v_mfma_f32_16x16x32_bf16 v[180:183], a[180:183], a[212:215], v[180:183]\n"
"ds_write_b128 v16, v[56:59] offset:12288\n"
"v_mfma_f32_16x16x32_bf16 v[184:187], a[184:187], a[212:215], v[184:187]\n"
"ds_read_b64_tr_b16 a[142:143], v29 offset:49664\n"
"v_mfma_f32_16x16x32_bf16 v[188:191], a[188:191], a[212:215], v[188:191]\n"
"global_load_dwordx4 a[212:215], v31, s[44:45] offset:1024 sc1\n"
"v_mfma_f32_16x16x32_bf16 v[192:195], a[160:163], a[216:219], v[192:195]\n"
"ds_read_b64_tr_b16 a[144:145], v26 offset:57344\n"
"v_mfma_f32_16x16x32_bf16 v[196:199], a[164:167], a[216:219], v[196:199]\n"
"v_mfma_f32_16x16x32_bf16 v[200:203], a[168:171], a[216:219], v[200:203]\n"
"ds_read_b64_tr_b16 a[146:147], v26 offset:57856\n"
"v_mfma_f32_16x16x32_bf16 v[204:207], a[172:175], a[216:219], v[204:207]\n"
"ds_read_b64_tr_b16 a[148:149], v27 offset:57344\n"
"v_mfma_f32_16x16x32_bf16 v[208:211], a[176:179], a[216:219], v[208:211]\n"
"ds_read_b64_tr_b16 a[150:151], v27 offset:57856\n"
"v_mfma_f32_16x16x32_bf16 v[212:215], a[180:183], a[216:219], v[212:215]\n"
"ds_read_b64_tr_b16 a[152:153], v28 offset:57344\n"
"v_mfma_f32_16x16x32_bf16 v[216:219], a[184:187], a[216:219], v[216:219]\n"
"v_mfma_f32_16x16x32_bf16 v[220:223], a[188:191], a[216:219], v[220:223]\n"
"global_load_dwordx4 a[216:219], v30, s[46:47] offset:1024 sc1\n"
"v_mfma_f32_16x16x32_bf16 v[224:227], a[160:163], a[220:223], v[224:227]\n"
"ds_read_b64_tr_b16 a[154:155], v28 offset:57856\n"
"v_mfma_f32_16x16x32_bf16 v[228:231], a[164:167], a[220:223], v[228:231]\n"
"ds_read_b64_tr_b16 a[156:157], v29 offset:57344\n"
"v_mfma_f32_16x16x32_bf16 v[232:235], a[168:171], a[220:223], v[232:235]\n"
"ds_read_b64_tr_b16 a[158:159], v29 offset:57856\n"
"v_mfma_f32_16x16x32_bf16 v[236:239], a[172:175], a[220:223], v[236:239]\n"
"v_mfma_f32_16x16x32_bf16 v[240:243], a[176:179], a[220:223], v[240:243]\n"
"v_mfma_f32_16x16x32_bf16 v[244:247], a[180:183], a[220:223], v[244:247]\n"
"v_mfma_f32_16x16x32_bf16 v[248:251], a[184:187], a[220:223], v[248:251]\n"
"v_mfma_f32_16x16x32_bf16 v[252:255], a[188:191], a[220:223], v[252:255]\n"
"global_load_dwordx4 a[220:223], v31, s[46:47] offset:1024 sc1\n"
"s_waitcnt vmcnt(16) lgkmcnt(0)\n"
"v_mfma_f32_16x16x32_bf16 v[128:131], a[128:131], a[224:227], v[128:131]\n"
"ds_read_b64_tr_b16 a[160:161], v26 offset:53248\n"
"v_mfma_f32_16x16x32_bf16 v[132:135], a[132:135], a[224:227], v[132:135]\n"
"ds_write_b128 v16, v[60:63] offset:13312\n"
"v_mfma_f32_16x16x32_bf16 v[136:139], a[136:139], a[224:227], v[136:139]\n"
"ds_read_b64_tr_b16 a[162:163], v26 offset:53760\n"
"v_mfma_f32_16x16x32_bf16 v[140:143], a[140:143], a[224:227], v[140:143]\n"
"global_store_dwordx4 v13, v[84:87], s[62:63] offset:-3072 sc1\n"
"v_mfma_f32_16x16x32_bf16 v[144:147], a[144:147], a[224:227], v[144:147]\n"
"ds_read_b64_tr_b16 a[164:165], v27 offset:53248\n"
"v_mfma_f32_16x16x32_bf16 v[148:151], a[148:151], a[224:227], v[148:151]\n"
"ds_read_b64_tr_b16 a[166:167], v27 offset:53760\n"
"v_mfma_f32_16x16x32_bf16 v[152:155], a[152:155], a[224:227], v[152:155]\n"
"v_mfma_f32_16x16x32_bf16 v[156:159], a[156:159], a[224:227], v[156:159]\n"
"global_load_dwordx4 a[224:227], v30, s[44:45] offset:2048 sc1\n"
"v_mfma_f32_16x16x32_bf16 v[160:163], a[128:131], a[228:231], v[160:163]\n"
"ds_write_b128 v17, a[0:3] offset:0\n"
"v_mfma_f32_16x16x32_bf16 v[164:167], a[132:135], a[228:231], v[164:167]\n"
"ds_read_b64_tr_b16 a[168:169], v28 offset:53248\n"
"v_mfma_f32_16x16x32_bf16 v[168:171], a[136:139], a[228:231], v[168:171]\n"
"ds_read_b64_tr_b16 a[170:171], v28 offset:53760\n"
"v_mfma_f32_16x16x32_bf16 v[172:175], a[140:143], a[228:231], v[172:175]\n"
"global_store_dwordx4 v13, v[88:91], s[62:63] offset:0 sc1\n"
"v_mfma_f32_16x16x32_bf16 v[176:179], a[144:147], a[228:231], v[176:179]\n"
"ds_read_b64_tr_b16 a[172:173], v29 offset:53248\n"
"v_mfma_f32_16x16x32_bf16 v[180:183], a[148:151], a[228:231], v[180:183]\n"
"ds_write_b128 v17, a[4:7] offset:2048\n"
"v_mfma_f32_16x16x32_bf16 v[184:187], a[152:155], a[228:231], v[184:187]\n"
"v_mfma_f32_16x16x32_bf16 v[188:191], a[156:159], a[228:231], v[188:191]\n"
"global_load_dwordx4 a[228:231], v31, s[44:45] offset:2048 sc1\n"
"v_mfma_f32_16x16x32_bf16 v[192:195], a[128:131], a[232:235], v[192:195]\n"
"ds_read_b64_tr_b16 a[174:175], v29 offset:53760\n"
"v_mfma_f32_16x16x32_bf16 v[196:199], a[132:135], a[232:235], v[196:199]\n"
"ds_read_b64_tr_b16 a[176:177], v26 offset:61440\n"
"v_mfma_f32_16x16x32_bf16 v[200:203], a[136:139], a[232:235], v[200:203]\n"
"ds_read_b64_tr_b16 a[178:179], v26 offset:61952\n"
"v_mfma_f32_16x16x32_bf16 v[204:207], a[140:143], a[232:235], v[204:207]\n"
"ds_read_b64_tr_b16 a[180:181], v27 offset:61440\n"
"v_mfma_f32_16x16x32_bf16 v[208:211], a[144:147], a[232:235], v[208:211]\n"
"ds_read_b64_tr_b16 a[182:183], v27 offset:61952\n"
"v_mfma_f32_16x16x32_bf16 v[212:215], a[148:151], a[232:235], v[212:215]\n"
"v_mfma_f32_16x16x32_bf16 v[216:219], a[152:155], a[232:235], v[216:219]\n"
"ds_read_b64_tr_b16 a[184:185], v28 offset:61440\n"
"v_mfma_f32_16x16x32_bf16 v[220:223], a[156:159], a[232:235], v[220:223]\n"
"global_load_dwordx4 a[232:235], v30, s[46:47] offset:2048 sc1\n"
"v_mfma_f32_16x16x32_bf16 v[224:227], a[128:131], a[236:239], v[224:227]\n"
"ds_read_b64_tr_b16 a[186:187], v28 offset:61952\n"
"v_mfma_f32_16x16x32_bf16 v[228:231], a[132:135], a[236:239], v[228:231]\n"
"ds_read_b64_tr_b16 a[188:189], v29 offset:61440\n"
"v_mfma_f32_16x16x32_bf16 v[232:235], a[136:139], a[236:239], v[232:235]\n"
"ds_read_b64_tr_b16 a[190:191], v29 offset:61952\n"
"v_mfma_f32_16x16x32_bf16 v[236:239], a[140:143], a[236:239], v[236:239]\n"
"v_mfma_f32_16x16x32_bf16 v[240:243], a[144:147], a[236:239], v[240:243]\n"
"v_mfma_f32_16x16x32_bf16 v[244:247], a[148:151], a[236:239], v[244:247]\n"
"v_mfma_f32_16x16x32_bf16 v[248:251], a[152:155], a[236:239], v[248:251]\n"
"v_mfma_f32_16x16x32_bf16 v[252:255], a[156:159], a[236:239], v[252:255]\n"
"global_load_dwordx4 a[236:239], v31, s[46:47] offset:2048 sc1\n"
"s_waitcnt vmcnt(16) lgkmcnt(0)\n"
"v_add_u32 v26, 0x10000, v26\n"
"v_add_u32 v27, 0x10000, v27\n"
"v_add_u32 v28, 0x10000, v28\n"
"v_add_u32 v29, 0x10000, v29\n"
"v_mfma_f32_16x16x32_bf16 v[128:131], a[160:163], a[240:243], v[128:131]\n"
"ds_read_b64_tr_b16 a[128:129], v26 offset:32768\n"
"v_mfma_f32_16x16x32_bf16 v[132:135], a[164:167], a[240:243], v[132:135]\n"
"ds_write_b128 v17, a[8:11] offset:4096\n"
"v_mfma_f32_16x16x32_bf16 v[136:139], a[168:171], a[240:243], v[136:139]\n"
"ds_read_b64_tr_b16 a[130:131], v26 offset:33280\n"
"v_mfma_f32_16x16x32_bf16 v[140:143], a[172:175], a[240:243], v[140:143]\n"
"global_store_dwordx4 v13, v[92:95], s[62:63] offset:1024 sc1\n"
"v_mfma_f32_16x16x32_bf16 v[144:147], a[176:179], a[240:243], v[144:147]\n"
"ds_read_b64_tr_b16 a[132:133], v27 offset:32768\n"
"v_mfma_f32_16x16x32_bf16 v[148:151], a[180:183], a[240:243], v[148:151]\n"
"v_mfma_f32_16x16x32_bf16 v[152:155], a[184:187], a[240:243], v[152:155]\n"
"ds_read_b64_tr_b16 a[134:135], v27 offset:33280\n"
"v_mfma_f32_16x16x32_bf16 v[156:159], a[188:191], a[240:243], v[156:159]\n"
"global_load_dwordx4 a[240:243], v30, s[44:45] offset:3072 sc1\n"
"v_mfma_f32_16x16x32_bf16 v[160:163], a[160:163], a[244:247], v[160:163]\n"
"ds_write_b128 v17, a[12:15] offset:6144\n"
"v_mfma_f32_16x16x32_bf16 v[164:167], a[164:167], a[244:247], v[164:167]\n"
"ds_read_b64_tr_b16 a[136:137], v28 offset:32768\n"
"v_mfma_f32_16x16x32_bf16 v[168:171], a[168:171], a[244:247], v[168:171]\n"
"ds_read_b64_tr_b16 a[138:139], v28 offset:33280\n"
"v_mfma_f32_16x16x32_bf16 v[172:175], a[172:175], a[244:247], v[172:175]\n"
"v_mfma_f32_16x16x32_bf16 v[176:179], a[176:179], a[244:247], v[176:179]\n"
"ds_read_b64_tr_b16 a[140:141], v29 offset:32768\n"
"v_mfma_f32_16x16x32_bf16 v[180:183], a[180:183], a[244:247], v[180:183]\n"
"ds_write_b128 v17, a[32:35] offset:1024\n"
"v_mfma_f32_16x16x32_bf16 v[184:187], a[184:187], a[244:247], v[184:187]\n"
"ds_read_b64_tr_b16 a[142:143], v29 offset:33280\n"
"v_mfma_f32_16x16x32_bf16 v[188:191], a[188:191], a[244:247], v[188:191]\n"
"global_load_dwordx4 a[244:247], v31, s[44:45] offset:3072 sc1\n"
"s_add_u32 s44, s44, 8192\n"
"s_addc_u32 s45, s45, 0\n"
"v_mfma_f32_16x16x32_bf16 v[192:195], a[160:163], a[248:251], v[192:195]\n"
"ds_read_b64_tr_b16 a[144:145], v26 offset:40960\n"
"v_mfma_f32_16x16x32_bf16 v[196:199], a[164:167], a[248:251], v[196:199]\n"
"v_mfma_f32_16x16x32_bf16 v[200:203], a[168:171], a[248:251], v[200:203]\n"
"ds_read_b64_tr_b16 a[146:147], v26 offset:41472\n"
"v_mfma_f32_16x16x32_bf16 v[204:207], a[172:175], a[248:251], v[204:207]\n"
"ds_read_b64_tr_b16 a[148:149], v27 offset:40960\n"
"v_mfma_f32_16x16x32_bf16 v[208:211], a[176:179], a[248:251], v[208:211]\n"
"ds_read_b64_tr_b16 a[150:151], v27 offset:41472\n"
"v_mfma_f32_16x16x32_bf16 v[212:215], a[180:183], a[248:251], v[212:215]\n"
"ds_read_b64_tr_b16 a[152:153], v28 offset:40960\n"
"v_mfma_f32_16x16x32_bf16 v[216:219], a[184:187], a[248:251], v[216:219]\n"
"v_mfma_f32_16x16x32_bf16 v[220:223], a[188:191], a[248:251], v[220:223]\n"
"global_load_dwordx4 a[248:251], v30, s[46:47] offset:3072 sc1\n"
"v_mfma_f32_16x16x32_bf16 v[224:227], a[160:163], a[252:255], v[224:227]\n"
"ds_read_b64_tr_b16 a[154:155], v28 offset:41472\n"
"v_mfma_f32_16x16x32_bf16 v[228:231], a[164:167], a[252:255], v[228:231]\n"
"ds_read_b64_tr_b16 a[156:157], v29 offset:40960\n"
"v_mfma_f32_16x16x32_bf16 v[232:235], a[168:171], a[252:255], v[232:235]\n"
"ds_read_b64_tr_b16 a[158:159], v29 offset:41472\n"
"v_mfma_f32_16x16x32_bf16 v[236:239], a[172:175], a[252:255], v[236:239]\n"
"v_mfma_f32_16x16x32_bf16 v[240:243], a[176:179], a[252:255], v[240:243]\n"
"v_mfma_f32_16x16x32_bf16 v[244:247], a[180:183], a[252:255], v[244:247]\n"
"v_mfma_f32_16x16x32_bf16 v[248:251], a[184:187], a[252:255], v[248:251]\n"
"v_mfma_f32_16x16x32_bf16 v[252:255], a[188:191], a[252:255], v[252:255]\n"
"global_load_dwordx4 a[252:255], v31, s[46:47] offset:3072 sc1\n"
"s_add_u32 s46, s46, 8192\n"
"s_addc_u32 s47, s47, 0\n"
"s_waitcnt vmcnt(16) lgkmcnt(0)\n"
"v_mfma_f32_16x16x32_bf16 v[128:131], a[128:131], a[192:195], v[128:131]\n"
"ds_read_b64_tr_b16 a[160:161], v26 offset:36864\n"
"v_mfma_f32_16x16x32_bf16 v[132:135], a[132:135], a[192:195], v[132:135]\n"
"ds_write_b128 v17, a[36:39] offset:3072\n"
"v_mfma_f32_16x16x32_bf16 v[136:139], a[136:139], a[192:195], v[136:139]\n"
"ds_read_b64_tr_b16 a[162:163], v26 offset:37376\n"
"v_mfma_f32_16x16x32_bf16 v[140:143], a[140:143], a[192:195], v[140:143]\n"
"global_store_dwordx4 v8, a[0:3], s[64:65] offset:-4096 sc1\n"
"v_mfma_f32_16x16x32_bf16 v[144:147], a[144:147], a[192:195], v[144:147]\n"
"ds_read_b64_tr_b16 a[164:165], v27 offset:36864\n"
"v_mfma_f32_16x16x32_bf16 v[148:151], a[148:151], a[192:195], v[148:151]\n"
"v_mfma_f32_16x16x32_bf16 v[152:155], a[152:155], a[192:195], v[152:155]\n"
"ds_read_b64_tr_b16 a[166:167], v27 offset:37376\n"
"v_mfma_f32_16x16x32_bf16 v[156:159], a[156:159], a[192:195], v[156:159]\n"
"global_load_dwordx4 a[192:195], v30, s[44:45] offset:-4096 sc1\n"
"v_mfma_f32_16x16x32_bf16 v[160:163], a[128:131], a[196:199], v[160:163]\n"
"ds_write_b128 v17, a[40:43] offset:5120\n"
"v_mfma_f32_16x16x32_bf16 v[164:167], a[132:135], a[196:199], v[164:167]\n"
"ds_read_b64_tr_b16 a[168:169], v28 offset:36864\n"
"v_mfma_f32_16x16x32_bf16 v[168:171], a[136:139], a[196:199], v[168:171]\n"
"ds_read_b64_tr_b16 a[170:171], v28 offset:37376\n"
"v_mfma_f32_16x16x32_bf16 v[172:175], a[140:143], a[196:199], v[172:175]\n"
"v_mfma_f32_16x16x32_bf16 v[176:179], a[144:147], a[196:199], v[176:179]\n"
"ds_read_b64_tr_b16 a[172:173], v29 offset:36864\n"
"v_mfma_f32_16x16x32_bf16 v[180:183], a[148:151], a[196:199], v[180:183]\n"
"ds_write_b128 v17, a[44:47] offset:7168\n"
"v_mfma_f32_16x16x32_bf16 v[184:187], a[152:155], a[196:199], v[184:187]\n"
"ds_read_b64_tr_b16 a[174:175], v29 offset:37376\n"
"v_mfma_f32_16x16x32_bf16 v[188:191], a[156:159], a[196:199], v[188:191]\n"
"global_load_dwordx4 a[196:199], v31, s[44:45] offset:-4096 sc1\n"
"v_mfma_f32_16x16x32_bf16 v[192:195], a[128:131], a[200:203], v[192:195]\n"
"ds_read_b64_tr_b16 a[176:177], v26 offset:45056\n"
"v_mfma_f32_16x16x32_bf16 v[196:199], a[132:135], a[200:203], v[196:199]\n"
"v_mfma_f32_16x16x32_bf16 v[200:203], a[136:139], a[200:203], v[200:203]\n"
"ds_read_b64_tr_b16 a[178:179], v26 offset:45568\n"
"v_mfma_f32_16x16x32_bf16 v[204:207], a[140:143], a[200:203], v[204:207]\n"
"ds_read_b64_tr_b16 a[180:181], v27 offset:45056\n"
"v_mfma_f32_16x16x32_bf16 v[208:211], a[144:147], a[200:203], v[208:211]\n"
"ds_read_b64_tr_b16 a[182:183], v27 offset:45568\n"
"v_mfma_f32_16x16x32_bf16 v[212:215], a[148:151], a[200:203], v[212:215]\n"
"ds_read_b64_tr_b16 a[184:185], v28 offset:45056\n"
"v_mfma_f32_16x16x32_bf16 v[216:219], a[152:155], a[200:203], v[216:219]\n"
"v_mfma_f32_16x16x32_bf16 v[220:223], a[156:159], a[200:203], v[220:223]\n"
"global_load_dwordx4 a[200:203], v30, s[46:47] offset:-4096 sc1\n"
"v_mfma_f32_16x16x32_bf16 v[224:227], a[128:131], a[204:207], v[224:227]\n"
"ds_read_b64_tr_b16 a[186:187], v28 offset:45568\n"
"v_mfma_f32_16x16x32_bf16 v[228:231], a[132:135], a[204:207], v[228:231]\n"
"ds_read_b64_tr_b16 a[188:189], v29 offset:45056\n"
"v_mfma_f32_16x16x32_bf16 v[232:235], a[136:139], a[204:207], v[232:235]\n"
"ds_read_b64_tr_b16 a[190:191], v29 offset:45568\n"
"v_mfma_f32_16x16x32_bf16 v[236:239], a[140:143], a[204:207], v[236:239]\n"
"v_mfma_f32_16x16x32_bf16 v[240:243], a[144:147], a[204:207], v[240:243]\n"
"v_mfma_f32_16x16x32_bf16 v[244:247], a[148:151], a[204:207], v[244:247]\n"
"v_mfma_f32_16x16x32_bf16 v[248:251], a[152:155], a[204:207], v[248:251]\n"
"v_mfma_f32_16x16x32_bf16 v[252:255], a[156:159], a[204:207], v[252:255]\n"
"global_load_dwordx4 a[204:207], v31, s[46:47] offset:-4096 sc1\n"
"s_waitcnt vmcnt(16) lgkmcnt(0)\n"
"s_barrier\n"
"v_add_u32 v26, 0x10000, v26\n"
"v_add_u32 v27, 0x10000, v27\n"
"v_add_u32 v28, 0x10000, v28\n"
"v_add_u32 v29, 0x10000, v29\n"
"v_mfma_f32_16x16x32_bf16 v[128:131], a[160:163], a[208:211], v[128:131]\n"
"ds_read_b64_tr_b16 a[128:129], v26 offset:0\n"
"v_mfma_f32_16x16x32_bf16 v[132:135], a[164:167], a[208:211], v[132:135]\n"
"ds_write_b128 v16, v[64:67] offset:32768\n"
"v_mfma_f32_16x16x32_bf16 v[136:139], a[168:171], a[208:211], v[136:139]\n"
"ds_read_b64_tr_b16 a[130:131], v26 offset:512\n"
"v_mfma_f32_16x16x32_bf16 v[140:143], a[172:175], a[208:211], v[140:143]\n"
"global_store_dwordx4 v8, a[4:7], s[64:65] offset:-2048 sc1\n"
"v_mfma_f32_16x16x32_bf16 v[144:147], a[176:179], a[208:211], v[144:147]\n"
"ds_read_b64_tr_b16 a[132:133], v27 offset:0\n"
"v_mfma_f32_16x16x32_bf16 v[148:151], a[180:183], a[208:211], v[148:151]\n"
"ds_read_b64_tr_b16 a[134:135], v27 offset:512\n"
"v_mfma_f32_16x16x32_bf16 v[152:155], a[184:187], a[208:211], v[152:155]\n"
"v_mfma_f32_16x16x32_bf16 v[156:159], a[188:191], a[208:211], v[156:159]\n"
"global_load_dwordx4 a[208:211], v30, s[44:45] offset:-3072 sc1\n"
"v_mfma_f32_16x16x32_bf16 v[160:163], a[160:163], a[212:215], v[160:163]\n"
"ds_write_b128 v16, v[68:71] offset:33792\n"
"v_mfma_f32_16x16x32_bf16 v[164:167], a[164:167], a[212:215], v[164:167]\n"
"ds_read_b64_tr_b16 a[136:137], v28 offset:0\n"
"v_mfma_f32_16x16x32_bf16 v[168:171], a[168:171], a[212:215], v[168:171]\n"
"ds_read_b64_tr_b16 a[138:139], v28 offset:512\n"
"v_mfma_f32_16x16x32_bf16 v[172:175], a[172:175], a[212:215], v[172:175]\n"
"ds_read_b64_tr_b16 a[140:141], v29 offset:0\n"
"v_mfma_f32_16x16x32_bf16 v[176:179], a[176:179], a[212:215], v[176:179]\n"
"ds_write_b128 v16, v[72:75] offset:36864\n"
"v_mfma_f32_16x16x32_bf16 v[180:183], a[180:183], a[212:215], v[180:183]\n"
"v_mfma_f32_16x16x32_bf16 v[184:187], a[184:187], a[212:215], v[184:187]\n"
"ds_read_b64_tr_b16 a[142:143], v29 offset:512\n"
"v_mfma_f32_16x16x32_bf16 v[188:191], a[188:191], a[212:215], v[188:191]\n"
"global_load_dwordx4 a[212:215], v31, s[44:45] offset:-3072 sc1\n"
"v_mfma_f32_16x16x32_bf16 v[192:195], a[160:163], a[216:219], v[192:195]\n"
"ds_read_b64_tr_b16 a[144:145], v26 offset:8192\n"
"v_mfma_f32_16x16x32_bf16 v[196:199], a[164:167], a[216:219], v[196:199]\n"
"ds_read_b64_tr_b16 a[146:147], v26 offset:8704\n"
"v_mfma_f32_16x16x32_bf16 v[200:203], a[168:171], a[216:219], v[200:203]\n"
"ds_write_b128 v16, v[76:79] offset:37888\n"
"v_mfma_f32_16x16x32_bf16 v[204:207], a[172:175], a[216:219], v[204:207]\n"
"ds_read_b64_tr_b16 a[148:149], v27 offset:8192\n"
"v_mfma_f32_16x16x32_bf16 v[208:211], a[176:179], a[216:219], v[208:211]\n"
"v_mfma_f32_16x16x32_bf16 v[212:215], a[180:183], a[216:219], v[212:215]\n"
"ds_read_b64_tr_b16 a[150:151], v27 offset:8704\n"
"v_mfma_f32_16x16x32_bf16 v[216:219], a[184:187], a[216:219], v[216:219]\n"
"ds_read_b64_tr_b16 a[152:153], v28 offset:8192\n"
"v_mfma_f32_16x16x32_bf16 v[220:223], a[188:191], a[216:219], v[220:223]\n"
"global_load_dwordx4 a[216:219], v30, s[46:47] offset:-3072 sc1\n"
"v_mfma_f32_16x16x32_bf16 v[224:227], a[160:163], a[220:223], v[224:227]\n"
"ds_read_b64_tr_b16 a[154:155], v28 offset:8704\n"
"v_mfma_f32_16x16x32_bf16 v[228:231], a[164:167], a[220:223], v[228:231]\n"
"ds_read_b64_tr_b16 a[156:157], v29 offset:8192\n"
"v_mfma_f32_16x16x32_bf16 v[232:235], a[168:171], a[220:223], v[232:235]\n"
"ds_read_b64_tr_b16 a[158:159], v29 offset:8704\n"
"v_mfma_f32_16x16x32_bf16 v[236:239], a[172:175], a[220:223], v[236:239]\n"
"v_mfma_f32_16x16x32_bf16 v[240:243], a[176:179], a[220:223], v[240:243]\n"
"v_mfma_f32_16x16x32_bf16 v[244:247], a[180:183], a[220:223], v[244:247]\n"
"v_mfma_f32_16x16x32_bf16 v[248:251], a[184:187], a[220:223], v[248:251]\n"
"v_mfma_f32_16x16x32_bf16 v[252:255], a[188:191], a[220:223], v[252:255]\n"
"global_load_dwordx4 a[220:223], v31, s[46:47] offset:-3072 sc1\n"
"s_waitcnt vmcnt(0) lgkmcnt(0)\n"
"v_mfma_f32_16x16x32_bf16 v[128:131], a[128:131], a[224:227], v[128:131]\n"
"ds_read_b64_tr_b16 a[160:161], v26 offset:4096\n"
"v_mfma_f32_16x16x32_bf16 v[132:135], a[132:135], a[224:227], v[132:135]\n"
"ds_write_b128 v16, v[80:83] offset:40960\n"
"v_mfma_f32_16x16x32_bf16 v[136:139], a[136:139], a[224:227], v[136:139]\n"
"ds_read_b64_tr_b16 a[162:163], v26 offset:4608\n"
"v_mfma_f32_16x16x32_bf16 v[140:143], a[140:143], a[224:227], v[140:143]\n"
"global_store_dwordx4 v8, a[8:11], s[64:65] offset:0 sc1\n"
"v_mfma_f32_16x16x32_bf16 v[144:147], a[144:147], a[224:227], v[144:147]\n"
"ds_read_b64_tr_b16 a[164:165], v27 offset:4096\n"
"v_mfma_f32_16x16x32_bf16 v[148:151], a[148:151], a[224:227], v[148:151]\n"
"v_mfma_f32_16x16x32_bf16 v[152:155], a[152:155], a[224:227], v[152:155]\n"
"ds_read_b64_tr_b16 a[166:167], v27 offset:4608\n"
"v_mfma_f32_16x16x32_bf16 v[156:159], a[156:159], a[224:227], v[156:159]\n"
"ds_write_b128 v16, v[84:87] offset:41984\n"
"v_mfma_f32_16x16x32_bf16 v[160:163], a[128:131], a[228:231], v[160:163]\n"
"ds_read_b64_tr_b16 a[168:169], v28 offset:4096\n"
"v_mfma_f32_16x16x32_bf16 v[164:167], a[132:135], a[228:231], v[164:167]\n"
"ds_read_b64_tr_b16 a[170:171], v28 offset:4608\n"
"v_mfma_f32_16x16x32_bf16 v[168:171], a[136:139], a[228:231], v[168:171]\n"
"v_mfma_f32_16x16x32_bf16 v[172:175], a[140:143], a[228:231], v[172:175]\n"
"global_store_dwordx4 v8, a[12:15], s[64:65] offset:2048 sc1\n"
"v_mfma_f32_16x16x32_bf16 v[176:179], a[144:147], a[228:231], v[176:179]\n"
"ds_read_b64_tr_b16 a[172:173], v29 offset:4096\n"
"v_mfma_f32_16x16x32_bf16 v[180:183], a[148:151], a[228:231], v[180:183]\n"
"ds_write_b128 v16, v[88:91] offset:45056\n"
"v_mfma_f32_16x16x32_bf16 v[184:187], a[152:155], a[228:231], v[184:187]\n"
"ds_read_b64_tr_b16 a[174:175], v29 offset:4608\n"
"v_mfma_f32_16x16x32_bf16 v[188:191], a[156:159], a[228:231], v[188:191]\n"
"ds_read_b64_tr_b16 a[176:177], v26 offset:12288\n"
"v_mfma_f32_16x16x32_bf16 v[192:195], a[128:131], a[232:235], v[192:195]\n"
"v_mfma_f32_16x16x32_bf16 v[196:199], a[132:135], a[232:235], v[196:199]\n"
"ds_read_b64_tr_b16 a[178:179], v26 offset:12800\n"
"v_mfma_f32_16x16x32_bf16 v[200:203], a[136:139], a[232:235], v[200:203]\n"
"ds_read_b64_tr_b16 a[180:181], v27 offset:12288\n"
"v_mfma_f32_16x16x32_bf16 v[204:207], a[140:143], a[232:235], v[204:207]\n"
"global_store_dwordx4 v8, a[32:35], s[64:65] offset:-3072 sc1\n"
"v_mfma_f32_16x16x32_bf16 v[208:211], a[144:147], a[232:235], v[208:211]\n"
"ds_read_b64_tr_b16 a[182:183], v27 offset:12800\n"
"v_mfma_f32_16x16x32_bf16 v[212:215], a[148:151], a[232:235], v[212:215]\n"
"ds_read_b64_tr_b16 a[184:185], v28 offset:12288\n"
"v_mfma_f32_16x16x32_bf16 v[216:219], a[152:155], a[232:235], v[216:219]\n"
"v_mfma_f32_16x16x32_bf16 v[220:223], a[156:159], a[232:235], v[220:223]\n"
"ds_read_b64_tr_b16 a[186:187], v28 offset:12800\n"
"v_mfma_f32_16x16x32_bf16 v[224:227], a[128:131], a[236:239], v[224:227]\n"
"ds_read_b64_tr_b16 a[188:189], v29 offset:12288\n"
"v_mfma_f32_16x16x32_bf16 v[228:231], a[132:135], a[236:239], v[228:231]\n"
"ds_read_b64_tr_b16 a[190:191], v29 offset:12800\n"
"v_mfma_f32_16x16x32_bf16 v[232:235], a[136:139], a[236:239], v[232:235]\n"
"v_mfma_f32_16x16x32_bf16 v[236:239], a[140:143], a[236:239], v[236:239]\n"
"global_store_dwordx4 v8, a[36:39], s[64:65] offset:-1024 sc1\n"
"v_mfma_f32_16x16x32_bf16 v[240:243], a[144:147], a[236:239], v[240:243]\n"
"v_mfma_f32_16x16x32_bf16 v[244:247], a[148:151], a[236:239], v[244:247]\n"
"v_mfma_f32_16x16x32_bf16 v[248:251], a[152:155], a[236:239], v[248:251]\n"
"v_mfma_f32_16x16x32_bf16 v[252:255], a[156:159], a[236:239], v[252:255]\n"
"s_waitcnt lgkmcnt(0)\n"
"v_mfma_f32_16x16x32_bf16 v[128:131], a[160:163], a[240:243], v[128:131]\n"
"ds_read_b64_tr_b16 a[128:129], v26 offset:16384\n"
"v_mfma_f32_16x16x32_bf16 v[132:135], a[164:167], a[240:243], v[132:135]\n"
"ds_write_b128 v16, v[92:95] offset:46080\n"
"v_mfma_f32_16x16x32_bf16 v[136:139], a[168:171], a[240:243], v[136:139]\n"
"ds_read_b64_tr_b16 a[130:131], v26 offset:16896\n"
"v_mfma_f32_16x16x32_bf16 v[140:143], a[172:175], a[240:243], v[140:143]\n"
"global_store_dwordx4 v8, a[40:43], s[64:65] offset:1024 sc1\n"
"v_mfma_f32_16x16x32_bf16 v[144:147], a[176:179], a[240:243], v[144:147]\n"
"ds_read_b64_tr_b16 a[132:133], v27 offset:16384\n"
"v_mfma_f32_16x16x32_bf16 v[148:151], a[180:183], a[240:243], v[148:151]\n"
"v_mfma_f32_16x16x32_bf16 v[152:155], a[184:187], a[240:243], v[152:155]\n"
"ds_read_b64_tr_b16 a[134:135], v27 offset:16896\n"
"v_mfma_f32_16x16x32_bf16 v[156:159], a[188:191], a[240:243], v[156:159]\n"
"ds_write_b128 v17, a[64:67] offset:32768\n"
"v_mfma_f32_16x16x32_bf16 v[160:163], a[160:163], a[244:247], v[160:163]\n"
"ds_read_b64_tr_b16 a[136:137], v28 offset:16384\n"
"v_mfma_f32_16x16x32_bf16 v[164:167], a[164:167], a[244:247], v[164:167]\n"
"v_mfma_f32_16x16x32_bf16 v[168:171], a[168:171], a[244:247], v[168:171]\n"
"ds_read_b64_tr_b16 a[138:139], v28 offset:16896\n"
"v_mfma_f32_16x16x32_bf16 v[172:175], a[172:175], a[244:247], v[172:175]\n"
"global_store_dwordx4 v8, a[44:47], s[64:65] offset:3072 sc1\n"
"v_mfma_f32_16x16x32_bf16 v[176:179], a[176:179], a[244:247], v[176:179]\n"
"ds_read_b64_tr_b16 a[140:141], v29 offset:16384\n"
"v_mfma_f32_16x16x32_bf16 v[180:183], a[180:183], a[244:247], v[180:183]\n"
"ds_write_b128 v17, a[68:71] offset:34816\n"
"v_mfma_f32_16x16x32_bf16 v[184:187], a[184:187], a[244:247], v[184:187]\n"
"v_mfma_f32_16x16x32_bf16 v[188:191], a[188:191], a[244:247], v[188:191]\n"
"ds_read_b64_tr_b16 a[142:143], v29 offset:16896\n"
"v_mfma_f32_16x16x32_bf16 v[192:195], a[160:163], a[248:251], v[192:195]\n"
"ds_read_b64_tr_b16 a[144:145], v26 offset:24576\n"
"v_mfma_f32_16x16x32_bf16 v[196:199], a[164:167], a[248:251], v[196:199]\n"
"ds_read_b64_tr_b16 a[146:147], v26 offset:25088\n"
"v_mfma_f32_16x16x32_bf16 v[200:203], a[168:171], a[248:251], v[200:203]\n"
"v_mfma_f32_16x16x32_bf16 v[204:207], a[172:175], a[248:251], v[204:207]\n"
"global_store_dwordx4 v12, a[64:67], s[64:65] offset:-4096 sc1\n"
"v_mfma_f32_16x16x32_bf16 v[208:211], a[176:179], a[248:251], v[208:211]\n"
"ds_read_b64_tr_b16 a[148:149], v27 offset:24576\n"
"v_mfma_f32_16x16x32_bf16 v[212:215], a[180:183], a[248:251], v[212:215]\n"
"ds_read_b64_tr_b16 a[150:151], v27 offset:25088\n"
"v_mfma_f32_16x16x32_bf16 v[216:219], a[184:187], a[248:251], v[216:219]\n"
"ds_read_b64_tr_b16 a[152:153], v28 offset:24576\n"
"v_mfma_f32_16x16x32_bf16 v[220:223], a[188:191], a[248:251], v[220:223]\n"
"v_mfma_f32_16x16x32_bf16 v[224:227], a[160:163], a[252:255], v[224:227]\n"
"ds_read_b64_tr_b16 a[154:155], v28 offset:25088\n"
"v_mfma_f32_16x16x32_bf16 v[228:231], a[164:167], a[252:255], v[228:231]\n"
"ds_read_b64_tr_b16 a[156:157], v29 offset:24576\n"
"v_mfma_f32_16x16x32_bf16 v[232:235], a[168:171], a[252:255], v[232:235]\n"
"ds_read_b64_tr_b16 a[158:159], v29 offset:25088\n"
"v_mfma_f32_16x16x32_bf16 v[236:239], a[172:175], a[252:255], v[236:239]\n"
"v_mfma_f32_16x16x32_bf16 v[240:243], a[176:179], a[252:255], v[240:243]\n"
"v_mfma_f32_16x16x32_bf16 v[244:247], a[180:183], a[252:255], v[244:247]\n"
"v_mfma_f32_16x16x32_bf16 v[248:251], a[184:187], a[252:255], v[248:251]\n"
"v_mfma_f32_16x16x32_bf16 v[252:255], a[188:191], a[252:255], v[252:255]\n"
"s_waitcnt lgkmcnt(0)\n"
"v_mfma_f32_16x16x32_bf16 v[128:131], a[128:131], a[192:195], v[128:131]\n"
"ds_read_b64_tr_b16 a[160:161], v26 offset:20480\n"
"v_mfma_f32_16x16x32_bf16 v[132:135], a[132:135], a[192:195], v[132:135]\n"
"ds_write_b128 v17, a[72:75] offset:36864\n"
"v_mfma_f32_16x16x32_bf16 v[136:139], a[136:139], a[192:195], v[136:139]\n"
"ds_read_b64_tr_b16 a[162:163], v26 offset:20992\n"
"v_mfma_f32_16x16x32_bf16 v[140:143], a[140:143], a[192:195], v[140:143]\n"
"global_store_dwordx4 v12, a[68:71], s[64:65] offset:-2048 sc1\n"
"v_mfma_f32_16x16x32_bf16 v[144:147], a[144:147], a[192:195], v[144:147]\n"
"ds_read_b64_tr_b16 a[164:165], v27 offset:20480\n"
"v_mfma_f32_16x16x32_bf16 v[148:151], a[148:151], a[192:195], v[148:151]\n"
"v_mfma_f32_16x16x32_bf16 v[152:155], a[152:155], a[192:195], v[152:155]\n"
"ds_read_b64_tr_b16 a[166:167], v27 offset:20992\n"
"v_mfma_f32_16x16x32_bf16 v[156:159], a[156:159], a[192:195], v[156:159]\n"
"ds_write_b128 v17, a[76:79] offset:38912\n"
"v_mfma_f32_16x16x32_bf16 v[160:163], a[128:131], a[196:199], v[160:163]\n"
"ds_read_b64_tr_b16 a[168:169], v28 offset:20480\n"
"v_mfma_f32_16x16x32_bf16 v[164:167], a[132:135], a[196:199], v[164:167]\n"
"ds_read_b64_tr_b16 a[170:171], v28 offset:20992\n"
"v_mfma_f32_16x16x32_bf16 v[168:171], a[136:139], a[196:199], v[168:171]\n"
"v_mfma_f32_16x16x32_bf16 v[172:175], a[140:143], a[196:199], v[172:175]\n"
"global_store_dwordx4 v12, a[72:75], s[64:65] offset:0 sc1\n"
"v_mfma_f32_16x16x32_bf16 v[176:179], a[144:147], a[196:199], v[176:179]\n"
"ds_read_b64_tr_b16 a[172:173], v29 offset:20480\n"
"v_mfma_f32_16x16x32_bf16 v[180:183], a[148:151], a[196:199], v[180:183]\n"
"ds_write_b128 v17, a[96:99] offset:33792\n"
"v_mfma_f32_16x16x32_bf16 v[184:187], a[152:155], a[196:199], v[184:187]\n"
"ds_read_b64_tr_b16 a[174:175], v29 offset:20992\n"
"v_mfma_f32_16x16x32_bf16 v[188:191], a[156:159], a[196:199], v[188:191]\n"
"ds_read_b64_tr_b16 a[176:177], v26 offset:28672\n"
"v_mfma_f32_16x16x32_bf16 v[192:195], a[128:131], a[200:203], v[192:195]\n"
"v_mfma_f32_16x16x32_bf16 v[196:199], a[132:135], a[200:203], v[196:199]\n"
"ds_read_b64_tr_b16 a[178:179], v26 offset:29184\n"
"v_mfma_f32_16x16x32_bf16 v[200:203], a[136:139], a[200:203], v[200:203]\n"
"ds_read_b64_tr_b16 a[180:181], v27 offset:28672\n"
"v_mfma_f32_16x16x32_bf16 v[204:207], a[140:143], a[200:203], v[204:207]\n"
"global_store_dwordx4 v12, a[76:79], s[64:65] offset:2048 sc1\n"
"v_mfma_f32_16x16x32_bf16 v[208:211], a[144:147], a[200:203], v[208:211]\n"
"ds_read_b64_tr_b16 a[182:183], v27 offset:29184\n"
"v_mfma_f32_16x16x32_bf16 v[212:215], a[148:151], a[200:203], v[212:215]\n"
"ds_read_b64_tr_b16 a[184:185], v28 offset:28672\n"
"v_mfma_f32_16x16x32_bf16 v[216:219], a[152:155], a[200:203], v[216:219]\n"
"v_mfma_f32_16x16x32_bf16 v[220:223], a[156:159], a[200:203], v[220:223]\n"
"ds_read_b64_tr_b16 a[186:187], v28 offset:29184\n"
"v_mfma_f32_16x16x32_bf16 v[224:227], a[128:131], a[204:207], v[224:227]\n"
"ds_read_b64_tr_b16 a[188:189], v29 offset:28672\n"
"v_mfma_f32_16x16x32_bf16 v[228:231], a[132:135], a[204:207], v[228:231]\n"
"ds_read_b64_tr_b16 a[190:191], v29 offset:29184\n"
"v_mfma_f32_16x16x32_bf16 v[232:235], a[136:139], a[204:207], v[232:235]\n"
"v_mfma_f32_16x16x32_bf16 v[236:239], a[140:143], a[204:207], v[236:239]\n"
"global_store_dwordx4 v12, a[96:99], s[64:65] offset:-3072 sc1\n"
"v_mfma_f32_16x16x32_bf16 v[240:243], a[144:147], a[204:207], v[240:243]\n"
"v_mfma_f32_16x16x32_bf16 v[244:247], a[148:151], a[204:207], v[244:247]\n"
"v_mfma_f32_16x16x32_bf16 v[248:251], a[152:155], a[204:207], v[248:251]\n"
"v_mfma_f32_16x16x32_bf16 v[252:255], a[156:159], a[204:207], v[252:255]\n"
"s_waitcnt lgkmcnt(0)\n"
"v_mfma_f32_16x16x32_bf16 v[128:131], a[160:163], a[208:211], v[128:131]\n"
"ds_write_b128 v17, a[100:103] offset:35840\n"
"v_mfma_f32_16x16x32_bf16 v[132:135], a[164:167], a[208:211], v[132:135]\n"
"v_mfma_f32_16x16x32_bf16 v[136:139], a[168:171], a[208:211], v[136:139]\n"
"v_mfma_f32_16x16x32_bf16 v[140:143], a[172:175], a[208:211], v[140:143]\n"
"global_store_dwordx4 v12, a[100:103], s[64:65] offset:-1024 sc1\n"
"v_mfma_f32_16x16x32_bf16 v[144:147], a[176:179], a[208:211], v[144:147]\n"
"v_mfma_f32_16x16x32_bf16 v[148:151], a[180:183], a[208:211], v[148:151]\n"
"v_mfma_f32_16x16x32_bf16 v[152:155], a[184:187], a[208:211], v[152:155]\n"
"v_mfma_f32_16x16x32_bf16 v[156:159], a[188:191], a[208:211], v[156:159]\n"
"v_mfma_f32_16x16x32_bf16 v[160:163], a[160:163], a[212:215], v[160:163]\n"
"v_mfma_f32_16x16x32_bf16 v[164:167], a[164:167], a[212:215], v[164:167]\n"
"ds_write_b128 v17, a[104:107] offset:37888\n"
"v_mfma_f32_16x16x32_bf16 v[168:171], a[168:171], a[212:215], v[168:171]\n"
"v_mfma_f32_16x16x32_bf16 v[172:175], a[172:175], a[212:215], v[172:175]\n"
"global_store_dwordx4 v12, a[104:107], s[64:65] offset:1024 sc1\n"
"v_mfma_f32_16x16x32_bf16 v[176:179], a[176:179], a[212:215], v[176:179]\n"
"v_mfma_f32_16x16x32_bf16 v[180:183], a[180:183], a[212:215], v[180:183]\n"
"v_mfma_f32_16x16x32_bf16 v[184:187], a[184:187], a[212:215], v[184:187]\n"
"v_mfma_f32_16x16x32_bf16 v[188:191], a[188:191], a[212:215], v[188:191]\n"
"v_mfma_f32_16x16x32_bf16 v[192:195], a[160:163], a[216:219], v[192:195]\n"
"v_mfma_f32_16x16x32_bf16 v[196:199], a[164:167], a[216:219], v[196:199]\n"
"v_mfma_f32_16x16x32_bf16 v[200:203], a[168:171], a[216:219], v[200:203]\n"
"ds_write_b128 v17, a[108:111] offset:39936\n"
"v_mfma_f32_16x16x32_bf16 v[204:207], a[172:175], a[216:219], v[204:207]\n"
"global_store_dwordx4 v12, a[108:111], s[64:65] offset:3072 sc1\n"
"v_mfma_f32_16x16x32_bf16 v[208:211], a[176:179], a[216:219], v[208:211]\n"
"v_mfma_f32_16x16x32_bf16 v[212:215], a[180:183], a[216:219], v[212:215]\n"
"v_mfma_f32_16x16x32_bf16 v[216:219], a[184:187], a[216:219], v[216:219]\n"
"v_mfma_f32_16x16x32_bf16 v[220:223], a[188:191], a[216:219], v[220:223]\n"
"v_mfma_f32_16x16x32_bf16 v[224:227], a[160:163], a[220:223], v[224:227]\n"
"v_mfma_f32_16x16x32_bf16 v[228:231], a[164:167], a[220:223], v[228:231]\n"
"v_mfma_f32_16x16x32_bf16 v[232:235], a[168:171], a[220:223], v[232:235]\n"
"v_mfma_f32_16x16x32_bf16 v[236:239], a[172:175], a[220:223], v[236:239]\n"
"v_mfma_f32_16x16x32_bf16 v[240:243], a[176:179], a[220:223], v[240:243]\n"
"v_mfma_f32_16x16x32_bf16 v[244:247], a[180:183], a[220:223], v[244:247]\n"
"v_mfma_f32_16x16x32_bf16 v[248:251], a[184:187], a[220:223], v[248:251]\n"
"v_mfma_f32_16x16x32_bf16 v[252:255], a[188:191], a[220:223], v[252:255]\n"
"v_lshlrev_b32 v3, 4, v2\n"
"s_waitcnt lgkmcnt(0)\n"
"s_barrier\n"
"ds_read_b128 a[64:67], v3 offset:0\n"
"ds_read_b128 a[68:71], v3 offset:1024\n"
"ds_read_b128 a[72:75], v3 offset:2048\n"
"ds_read_b128 a[76:79], v3 offset:3072\n"
"ds_read_b128 a[80:83], v3 offset:4096\n"
"ds_read_b128 a[84:87], v3 offset:5120\n"
"ds_read_b128 a[88:91], v3 offset:6144\n"
"ds_read_b128 a[92:95], v3 offset:7168\n"
"ds_read_b128 a[96:99], v3 offset:8192\n"
"ds_read_b128 a[100:103], v3 offset:9216\n"
"ds_read_b128 a[104:107], v3 offset:10240\n"
"ds_read_b128 a[108:111], v3 offset:11264\n"
"ds_read_b128 a[112:115], v3 offset:12288\n"
"ds_read_b128 a[116:119], v3 offset:13312\n"
"ds_read_b128 a[120:123], v3 offset:14336\n"
"ds_read_b128 a[124:127], v3 offset:15360\n"
"v_cvt_pk_bf16_f32 v64, v128, v129\n"
"v_cvt_pk_bf16_f32 v65, v130, v131\n"
"v_cvt_pk_bf16_f32 v66, v132, v133\n"
"v_cvt_pk_bf16_f32 v67, v134, v135\n"
"v_cvt_pk_bf16_f32 v68, v136, v137\n"
"v_cvt_pk_bf16_f32 v69, v138, v139\n"
"v_cvt_pk_bf16_f32 v70, v140, v141\n"
"v_cvt_pk_bf16_f32 v71, v142, v143\n"
"v_cvt_pk_bf16_f32 v72, v144, v145\n"
"v_cvt_pk_bf16_f32 v73, v146, v147\n"
"v_cvt_pk_bf16_f32 v74, v148, v149\n"
"v_cvt_pk_bf16_f32 v75, v150, v151\n"
"v_cvt_pk_bf16_f32 v76, v152, v153\n"
"v_cvt_pk_bf16_f32 v77, v154, v155\n"
"v_cvt_pk_bf16_f32 v78, v156, v157\n"
"v_cvt_pk_bf16_f32 v79, v158, v159\n"
"v_cvt_pk_bf16_f32 v80, v160, v161\n"
"v_cvt_pk_bf16_f32 v81, v162, v163\n"
"v_cvt_pk_bf16_f32 v82, v164, v165\n"
"v_cvt_pk_bf16_f32 v83, v166, v167\n"
"v_cvt_pk_bf16_f32 v84, v168, v169\n"
"v_cvt_pk_bf16_f32 v85, v170, v171\n"
"v_cvt_pk_bf16_f32 v86, v172, v173\n"
"v_cvt_pk_bf16_f32 v87, v174, v175\n"
"v_cvt_pk_bf16_f32 v88, v176, v177\n"
"v_cvt_pk_bf16_f32 v89, v178, v179\n"
"v_cvt_pk_bf16_f32 v90, v180, v181\n"
"v_cvt_pk_bf16_f32 v91, v182, v183\n"
"v_cvt_pk_bf16_f32 v92, v184, v185\n"
"v_cvt_pk_bf16_f32 v93, v186, v187\n"
"v_cvt_pk_bf16_f32 v94, v188, v189\n"
"v_cvt_pk_bf16_f32 v95, v190, v191\n"
"v_cvt_pk_bf16_f32 v96, v192, v193\n"
"v_cvt_pk_bf16_f32 v97, v194, v195\n"
"v_cvt_pk_bf16_f32 v98, v196, v197\n"
"v_cvt_pk_bf16_f32 v99, v198, v199\n"
"v_cvt_pk_bf16_f32 v100, v200, v201\n"
"v_cvt_pk_bf16_f32 v101, v202, v203\n"
"v_cvt_pk_bf16_f32 v102, v204, v205\n"
"v_cvt_pk_bf16_f32 v103, v206, v207\n"
"v_cvt_pk_bf16_f32 v104, v208, v209\n"
"v_cvt_pk_bf16_f32 v105, v210, v211\n"
"v_cvt_pk_bf16_f32 v106, v212, v213\n"
"v_cvt_pk_bf16_f32 v107, v214, v215\n"
"v_cvt_pk_bf16_f32 v108, v216, v217\n"
"v_cvt_pk_bf16_f32 v109, v218, v219\n"
"v_cvt_pk_bf16_f32 v110, v220, v221\n"
"v_cvt_pk_bf16_f32 v111, v222, v223\n"
"v_cvt_pk_bf16_f32 v112, v224, v225\n"
"v_cvt_pk_bf16_f32 v113, v226, v227\n"
"v_cvt_pk_bf16_f32 v114, v228, v229\n"
"v_cvt_pk_bf16_f32 v115, v230, v231\n"
"v_cvt_pk_bf16_f32 v116, v232, v233\n"
"v_cvt_pk_bf16_f32 v117, v234, v235\n"
"v_cvt_pk_bf16_f32 v118, v236, v237\n"
"v_cvt_pk_bf16_f32 v119, v238, v239\n"
"v_cvt_pk_bf16_f32 v120, v240, v241\n"
"v_cvt_pk_bf16_f32 v121, v242, v243\n"
"v_cvt_pk_bf16_f32 v122, v244, v245\n"
"v_cvt_pk_bf16_f32 v123, v246, v247\n"
"v_cvt_pk_bf16_f32 v124, v248, v249\n"
"v_cvt_pk_bf16_f32 v125, v250, v251\n"
"v_cvt_pk_bf16_f32 v126, v252, v253\n"
"v_cvt_pk_bf16_f32 v127, v254, v255\n"
"s_mov_b64 s[22:23], %[kv]\n"
"s_mov_b32 s32, %[ntl]\n"
"s_mov_b64 s[38:39], %[cl]\n"
"s_mov_b64 s[40:41], %[cs]\n"
"s_add_u32 s20, s58, 4096\n"
"s_addc_u32 s21, s59, 0\n"
"s_lshl_b32 s25, s27, 13\n"
"s_add_u32 s25, s25, 4096\n"
"s_add_u32 s24, s25, 0x20000\n"
"s_mov_b32 s43, 64\n"
"s_mov_b32 s34, 0\n"
"s_sub_u32 s42, s32, 2\n"
"s_cmp_eq_u32 s32, 32\n"
"s_cselect_b32 s42, 2, s42\n"
"s_lshl_b64 s[30:31], 3, s42\n"
"s_sub_u32 s28, s32, 1\n"
"s_lshl_b64 s[28:29], 7, s28\n"
"s_or_b64 s[30:31], s[30:31], s[28:29]\n"
"s_or_b32 s30, s30, 6\n"
"s_or_b32 s31, s31, 3\n"
"s_mov_b32 s26, 0\n"
"s_waitcnt lgkmcnt(0)\n"
"v_mfma_f32_16x16x32_bf16 v[128:131], a[64:67], v[64:67], 0\n"
"v_lshlrev_b32 v245, 4, v2\n"
"v_and_b32 v3, 15, v2\n"
"v_lshrrev_b32 v4, 4, v2\n"
"v_mfma_f32_16x16x32_bf16 v[128:131], a[68:71], v[68:71], v[128:131]\n"
"v_lshlrev_b32 v4, 2, v4\n"
"v_sub_u32 v3, v3, v4\n"
"s_lshl_b32 s28, s27, 5\n"
"v_mfma_f32_16x16x32_bf16 v[128:131], a[72:75], v[72:75], v[128:131]\n"
"v_add_u32 v240, s28, v3\n"
"v_subrev_u32 v241, 64, v240\n"
"v_mov_b32 v242, 0xff800000\n"
"v_mfma_f32_16x16x32_bf16 v[128:131], a[76:79], v[76:79], v[128:131]\n"
"v_mov_b32 v247, 0x49800000\n"
"v_mov_b32 v200, 0\n"
"v_mov_b32 v201, 0\n"
"v_mfma_f32_16x16x32_bf16 v[132:135], a[64:67], v[80:83], 0\n"
"v_mov_b32 v202, 0\n"
"v_mov_b32 v203, 0\n"
"v_mov_b32 v204, 0\n"
"v_mfma_f32_16x16x32_bf16 v[132:135], a[68:71], v[84:87], v[132:135]\n"
"v_mov_b32 v205, 0\n"
"v_mov_b32 v206, 0\n"
"v_mov_b32 v207, 0\n"
"v_mfma_f32_16x16x32_bf16 v[132:135], a[72:75], v[88:91], v[132:135]\n"
"v_mov_b32 v208, 0\n"
"v_mov_b32 v209, 0\n"
"v_mov_b32 v210, 0\n"
"v_mfma_f32_16x16x32_bf16 v[132:135], a[76:79], v[92:95], v[132:135]\n"
"v_mov_b32 v211, 0\n"
"v_mov_b32 v212, 0\n"
"v_mov_b32 v213, 0\n"
"v_mfma_f32_16x16x32_bf16 v[136:139], a[80:83], v[64:67], 0\n"
"v_mov_b32 v214, 0\n"
"v_mov_b32 v215, 0\n"
"v_mov_b32 v234, 0\n"
"v_mfma_f32_16x16x32_bf16 v[136:139], a[84:87], v[68:71], v[136:139]\n"
"v_mov_b32 v235, 0\n"
"v_mov_b32 v243, v245\n"
"v_add_u32 v244, 0x10000, v245\n"
"v_mfma_f32_16x16x32_bf16 v[136:139], a[88:91], v[72:75], v[136:139]\n"
"v_accvgpr_write_b32 a0, 0\n"
"v_accvgpr_write_b32 a1, 0\n"
"v_accvgpr_write_b32 a2, 0\n"
"v_mfma_f32_16x16x32_bf16 v[136:139], a[92:95], v[76:79], v[136:139]\n"
"v_accvgpr_write_b32 a3, 0\n"
"v_accvgpr_write_b32 a4, 0\n"
"v_accvgpr_write_b32 a5, 0\n"
"v_mfma_f32_16x16x32_bf16 v[140:143], a[80:83], v[80:83], 0\n"
"v_accvgpr_write_b32 a6, 0\n"
"v_accvgpr_write_b32 a7, 0\n"
"v_accvgpr_write_b32 a8, 0\n"
"v_mfma_f32_16x16x32_bf16 v[140:143], a[84:87], v[84:87], v[140:143]\n"
"v_accvgpr_write_b32 a9, 0\n"
"v_accvgpr_write_b32 a10, 0\n"
"v_accvgpr_write_b32 a11, 0\n"
"v_mfma_f32_16x16x32_bf16 v[140:143], a[88:91], v[88:91], v[140:143]\n"
"v_accvgpr_write_b32 a12, 0\n"
"v_accvgpr_write_b32 a13, 0\n"
"v_accvgpr_write_b32 a14, 0\n"
"v_mfma_f32_16x16x32_bf16 v[140:143], a[92:95], v[92:95], v[140:143]\n"
"v_accvgpr_write_b32 a15, 0\n"
"v_accvgpr_write_b32 a16, 0\n"
"v_mfma_f32_16x16x32_bf16 v[144:147], a[96:99], v[64:67], 0\n"
"v_accvgpr_write_b32 a17, 0\n"
"v_accvgpr_write_b32 a18, 0\n"
"v_accvgpr_write_b32 a19, 0\n"
"v_mfma_f32_16x16x32_bf16 v[144:147], a[100:103], v[68:71], v[144:147]\n"
"v_accvgpr_write_b32 a20, 0\n"
"v_accvgpr_write_b32 a21, 0\n"
"v_accvgpr_write_b32 a22, 0\n"
"v_mfma_f32_16x16x32_bf16 v[144:147], a[104:107], v[72:75], v[144:147]\n"
"v_accvgpr_write_b32 a23, 0\n"
"v_accvgpr_write_b32 a24, 0\n"
"v_accvgpr_write_b32 a25, 0\n"
"v_mfma_f32_16x16x32_bf16 v[144:147], a[108:111], v[76:79], v[144:147]\n"
"v_accvgpr_write_b32 a26, 0\n"
"v_accvgpr_write_b32 a27, 0\n"
"v_accvgpr_write_b32 a28, 0\n"
"v_mfma_f32_16x16x32_bf16 v[148:151], a[96:99], v[80:83], 0\n"
"v_accvgpr_write_b32 a29, 0\n"
"v_accvgpr_write_b32 a30, 0\n"
"v_accvgpr_write_b32 a31, 0\n"
"v_mfma_f32_16x16x32_bf16 v[148:151], a[100:103], v[84:87], v[148:151]\n"
"v_accvgpr_write_b32 a32, 0\n"
"v_accvgpr_write_b32 a33, 0\n"
"v_accvgpr_write_b32 a34, 0\n"
"v_mfma_f32_16x16x32_bf16 v[148:151], a[104:107], v[88:91], v[148:151]\n"
"v_accvgpr_write_b32 a35, 0\n"
"v_accvgpr_write_b32 a36, 0\n"
"v_accvgpr_write_b32 a37, 0\n"
"v_mfma_f32_16x16x32_bf16 v[148:151], a[108:111], v[92:95], v[148:151]\n"
"v_accvgpr_write_b32 a38, 0\n"
"v_accvgpr_write_b32 a39, 0\n"
"v_accvgpr_write_b32 a40, 0\n"
"v_mfma_f32_16x16x32_bf16 v[152:155], a[112:115], v[64:67], 0\n"
"v_accvgpr_write_b32 a41, 0\n"
"v_accvgpr_write_b32 a42, 0\n"
"v_accvgpr_write_b32 a43, 0\n"
"v_mfma_f32_16x16x32_bf16 v[152:155], a[116:119], v[68:71], v[152:155]\n"
"v_accvgpr_write_b32 a44, 0\n"
"v_accvgpr_write_b32 a45, 0\n"
"v_accvgpr_write_b32 a46, 0\n"
"v_mfma_f32_16x16x32_bf16 v[152:155], a[120:123], v[72:75], v[152:155]\n"
"v_accvgpr_write_b32 a47, 0\n"
"v_accvgpr_write_b32 a48, 0\n"
"v_accvgpr_write_b32 a49, 0\n"
"v_mfma_f32_16x16x32_bf16 v[152:155], a[124:127], v[76:79], v[152:155]\n"
"v_accvgpr_write_b32 a50, 0\n"
"v_accvgpr_write_b32 a51, 0\n"
"v_accvgpr_write_b32 a52, 0\n"
"v_mfma_f32_16x16x32_bf16 v[156:159], a[112:115], v[80:83], 0\n"
"v_accvgpr_write_b32 a53, 0\n"
"v_accvgpr_write_b32 a54, 0\n"
"v_accvgpr_write_b32 a55, 0\n"
"v_mfma_f32_16x16x32_bf16 v[156:159], a[116:119], v[84:87], v[156:159]\n"
"v_accvgpr_write_b32 a56, 0\n"
"v_accvgpr_write_b32 a57, 0\n"
"v_accvgpr_write_b32 a58, 0\n"
"v_mfma_f32_16x16x32_bf16 v[156:159], a[120:123], v[88:91], v[156:159]\n"
"v_accvgpr_write_b32 a59, 0\n"
"v_accvgpr_write_b32 a60, 0\n"
"v_accvgpr_write_b32 a61, 0\n"
"v_mfma_f32_16x16x32_bf16 v[156:159], a[124:127], v[92:95], v[156:159]\n"
"v_accvgpr_write_b32 a62, 0\n"
"v_accvgpr_write_b32 a63, 0\n"
"s_nop 7\n"
"s_branch 43f\n"
".p2align 4\n"
"1:\n"
"v_mfma_f32_16x16x32_bf16 v[128:131], a[64:67], v[64:67], v[216:219]\n"
"ds_read_b128 a[128:131], v244 offset:49152\n"
"v_exp_f32 v226, v178\n"
"v_mfma_f32_16x16x32_bf16 v[128:131], a[68:71], v[68:71], v[128:131]\n"
"s_bitcmp1_b64 s[30:31], s26\n"
"v_exp_f32 v227, v179\n"
"v_mfma_f32_16x16x32_bf16 v[128:131], a[72:75], v[72:75], v[128:131]\n"
"ds_read_b128 a[132:135], v244 offset:50176\n"
"v_add_f32 v234, v234, v224\n"
"v_mfma_f32_16x16x32_bf16 v[128:131], a[76:79], v[76:79], v[128:131]\n"
"ds_read_b128 a[136:139], v244 offset:51200\n"
"v_add_f32 v234, v234, v225\n"
"v_mfma_f32_16x16x32_bf16 v[132:135], a[64:67], v[80:83], v[220:223]\n"
"s_cselect_b64 s[56:57], -1, 0\n"
"v_cvt_pk_bf16_f32 v208, v224, v225\n"
"v_exp_f32 v228, v180\n"
"v_mfma_f32_16x16x32_bf16 v[132:135], a[68:71], v[84:87], v[132:135]\n"
"ds_read_b128 a[140:143], v244 offset:52224\n"
"v_exp_f32 v229, v181\n"
"v_mfma_f32_16x16x32_bf16 v[132:135], a[72:75], v[88:91], v[132:135]\n"
"v_add_f32 v234, v234, v226\n"
"v_add_f32 v234, v234, v227\n"
"v_mfma_f32_16x16x32_bf16 v[132:135], a[76:79], v[92:95], v[132:135]\n"
"ds_read_b128 a[144:147], v244 offset:53248\n"
"v_cvt_pk_bf16_f32 v209, v226, v227\n"
"v_mfma_f32_16x16x32_bf16 v[136:139], a[80:83], v[64:67], v[216:219]\n"
"ds_read_b128 a[148:151], v244 offset:54272\n"
"v_exp_f32 v230, v182\n"
"v_mfma_f32_16x16x32_bf16 v[136:139], a[84:87], v[68:71], v[136:139]\n"
"v_exp_f32 v231, v183\n"
"v_mfma_f32_16x16x32_bf16 v[136:139], a[88:91], v[72:75], v[136:139]\n"
"ds_read_b128 a[152:155], v244 offset:55296\n"
"v_add_f32 v235, v235, v228\n"
"v_mfma_f32_16x16x32_bf16 v[136:139], a[92:95], v[76:79], v[136:139]\n"
"ds_read_b128 a[156:159], v244 offset:56320\n"
"v_add_f32 v235, v235, v229\n"
"v_mfma_f32_16x16x32_bf16 v[140:143], a[80:83], v[80:83], v[220:223]\n"
"v_cvt_pk_bf16_f32 v212, v228, v229\n"
"v_exp_f32 v224, v184\n"
"v_mfma_f32_16x16x32_bf16 v[140:143], a[84:87], v[84:87], v[140:143]\n"
"ds_read_b128 a[160:163], v244 offset:57344\n"
"v_exp_f32 v225, v185\n"
"v_mfma_f32_16x16x32_bf16 v[140:143], a[88:91], v[88:91], v[140:143]\n"
"ds_read_b128 a[164:167], v244 offset:58368\n"
"v_add_f32 v235, v235, v230\n"
"v_mfma_f32_16x16x32_bf16 v[140:143], a[92:95], v[92:95], v[140:143]\n"
"v_add_f32 v235, v235, v231\n"
"v_cvt_pk_bf16_f32 v213, v230, v231\n"
"v_mfma_f32_16x16x32_bf16 v[144:147], a[96:99], v[64:67], v[216:219]\n"
"ds_read_b128 a[168:171], v244 offset:59392\n"
"v_exp_f32 v226, v186\n"
"v_mfma_f32_16x16x32_bf16 v[144:147], a[100:103], v[68:71], v[144:147]\n"
"ds_read_b128 a[172:175], v244 offset:60416\n"
"v_exp_f32 v227, v187\n"
"v_mfma_f32_16x16x32_bf16 v[144:147], a[104:107], v[72:75], v[144:147]\n"
"v_add_f32 v234, v234, v224\n"
"v_add_f32 v234, v234, v225\n"
"v_mfma_f32_16x16x32_bf16 v[144:147], a[108:111], v[76:79], v[144:147]\n"
"ds_read_b128 a[176:179], v244 offset:61440\n"
"v_cvt_pk_bf16_f32 v210, v224, v225\n"
"v_mfma_f32_16x16x32_bf16 v[148:151], a[96:99], v[80:83], v[220:223]\n"
"v_exp_f32 v228, v188\n"
"v_mfma_f32_16x16x32_bf16 v[148:151], a[100:103], v[84:87], v[148:151]\n"
"ds_read_b128 a[180:183], v244 offset:62464\n"
"v_exp_f32 v229, v189\n"
"v_mfma_f32_16x16x32_bf16 v[148:151], a[104:107], v[88:91], v[148:151]\n"
"ds_read_b128 a[184:187], v244 offset:63488\n"
"v_add_f32 v234, v234, v226\n"
"v_mfma_f32_16x16x32_bf16 v[148:151], a[108:111], v[92:95], v[148:151]\n"
"v_add_f32 v234, v234, v227\n"
"v_cvt_pk_bf16_f32 v211, v226, v227\n"
"v_mfma_f32_16x16x32_bf16 v[152:155], a[112:115], v[64:67], v[216:219]\n"
"ds_read_b128 a[188:191], v244 offset:64512\n"
"v_exp_f32 v230, v190\n"
"v_mfma_f32_16x16x32_bf16 v[152:155], a[116:119], v[68:71], v[152:155]\n"
"v_exp_f32 v231, v191\n"
"v_mfma_f32_16x16x32_bf16 v[152:155], a[120:123], v[72:75], v[152:155]\n"
"v_add_f32 v235, v235, v228\n"
"v_add_f32 v235, v235, v229\n"
"v_cvt_pk_bf16_f32 v214, v228, v229\n"
"v_mfma_f32_16x16x32_bf16 v[152:155], a[124:127], v[76:79], v[152:155]\n"
"v_add_f32 v235, v235, v230\n"
"v_add_f32 v235, v235, v231\n"
"v_cvt_pk_bf16_f32 v215, v230, v231\n"
"v_mfma_f32_16x16x32_bf16 v[156:159], a[112:115], v[80:83], v[220:223]\n"
"v_max_f32 v246, v234, v235\n"
"v_mfma_f32_16x16x32_bf16 v[156:159], a[116:119], v[84:87], v[156:159]\n"
"v_cmp_nle_f32 vcc, v246, v247\n"
"v_mfma_f32_16x16x32_bf16 v[156:159], a[120:123], v[88:91], v[156:159]\n"
"s_or_b64 vcc, vcc, s[56:57]\n"
"v_mfma_f32_16x16x32_bf16 v[156:159], a[124:127], v[92:95], v[156:159]\n"
"s_cbranch_vccnz 20f\n"
"30:\n"
"s_waitcnt vmcnt(8) lgkmcnt(0)\n"
"s_barrier\n"
"33:\n"
"v_mfma_f32_16x16x32_bf16 a[0:3], a[128:131], v[200:203], a[0:3]\n"
"s_mov_b32 m0, s24\n"
"v_exp_f32 v224, v128\n"
"v_mfma_f32_16x16x32_bf16 a[0:3], a[132:135], v[208:211], a[0:3]\n"
"ds_read_b128 a[64:67], v243 offset:32768\n"
"global_load_lds_dwordx4 v245, s[20:21] offset:-4096 sc1\n"
"v_mfma_f32_16x16x32_bf16 a[4:7], a[128:131], v[204:207], a[4:7]\n"
"ds_read_b128 a[68:71], v243 offset:33792\n"
"v_exp_f32 v225, v129\n"
"v_mfma_f32_16x16x32_bf16 a[4:7], a[132:135], v[212:215], a[4:7]\n"
"s_add_u32 s26, s26, 1\n"
"v_exp_f32 v226, v130\n"
"v_mfma_f32_16x16x32_bf16 a[8:11], a[136:139], v[200:203], a[8:11]\n"
"ds_read_b128 a[72:75], v243 offset:34816\n"
"global_load_lds_dwordx4 v245, s[20:21] offset:-3072 sc1\n"
"v_mfma_f32_16x16x32_bf16 a[8:11], a[140:143], v[208:211], a[8:11]\n"
"ds_read_b128 a[76:79], v243 offset:35840\n"
"v_exp_f32 v227, v131\n"
"v_mfma_f32_16x16x32_bf16 a[12:15], a[136:139], v[204:207], a[12:15]\n"
"s_add_u32 s43, s43, 1\n"
"v_add_f32 v232, v234, v224\n"
"v_add_f32 v232, v232, v225\n"
"v_cvt_pk_bf16_f32 v192, v224, v225\n"
"v_mfma_f32_16x16x32_bf16 a[12:15], a[140:143], v[212:215], a[12:15]\n"
"ds_read_b128 a[80:83], v243 offset:36864\n"
"global_load_lds_dwordx4 v245, s[20:21] offset:-2048 sc1\n"
"v_mfma_f32_16x16x32_bf16 a[16:19], a[144:147], v[200:203], a[16:19]\n"
"ds_read_b128 a[84:87], v243 offset:37888\n"
"v_exp_f32 v228, v132\n"
"v_mfma_f32_16x16x32_bf16 a[16:19], a[148:151], v[208:211], a[16:19]\n"
"s_add_u32 s24, s24, 32768\n"
"v_exp_f32 v229, v133\n"
"v_mfma_f32_16x16x32_bf16 a[20:23], a[144:147], v[204:207], a[20:23]\n"
"ds_read_b128 a[88:91], v243 offset:38912\n"
"global_load_lds_dwordx4 v245, s[20:21] offset:-1024 sc1\n"
"v_mfma_f32_16x16x32_bf16 a[20:23], a[148:151], v[212:215], a[20:23]\n"
"s_and_b32 s24, s24, 0x1ffff\n"
"v_add_f32 v232, v232, v226\n"
"v_add_f32 v232, v232, v227\n"
"v_cvt_pk_bf16_f32 v193, v226, v227\n"
"v_mfma_f32_16x16x32_bf16 a[24:27], a[152:155], v[200:203], a[24:27]\n"
"ds_read_b128 a[92:95], v243 offset:39936\n"
"v_exp_f32 v230, v134\n"
"v_mfma_f32_16x16x32_bf16 a[24:27], a[156:159], v[208:211], a[24:27]\n"
"ds_read_b128 a[96:99], v243 offset:40960\n"
"global_load_lds_dwordx4 v245, s[20:21] offset:0 sc1\n"
"v_mfma_f32_16x16x32_bf16 a[28:31], a[152:155], v[204:207], a[28:31]\n"
"s_cmp_eq_u32 s43, s34\n"
"v_exp_f32 v231, v135\n"
"v_mfma_f32_16x16x32_bf16 a[28:31], a[156:159], v[212:215], a[28:31]\n"
"ds_read_b128 a[100:103], v243 offset:41984\n"
"v_add_f32 v233, v235, v228\n"
"v_add_f32 v233, v233, v229\n"
"v_mfma_f32_16x16x32_bf16 a[32:35], a[160:163], v[200:203], a[32:35]\n"
"s_cselect_b32 s33, 5, 1\n"
"v_cvt_pk_bf16_f32 v196, v228, v229\n"
"v_exp_f32 v224, v136\n"
"v_mfma_f32_16x16x32_bf16 a[32:35], a[164:167], v[208:211], a[32:35]\n"
"ds_read_b128 a[104:107], v243 offset:43008\n"
"global_load_lds_dwordx4 v245, s[20:21] offset:1024 sc1\n"
"v_mfma_f32_16x16x32_bf16 a[36:39], a[160:163], v[204:207], a[36:39]\n"
"ds_read_b128 a[108:111], v243 offset:44032\n"
"v_exp_f32 v225, v137\n"
"v_mfma_f32_16x16x32_bf16 a[36:39], a[164:167], v[212:215], a[36:39]\n"
"s_lshl_b32 s33, s33, 15\n"
"v_add_f32 v233, v233, v230\n"
"v_add_f32 v233, v233, v231\n"
"v_cvt_pk_bf16_f32 v197, v230, v231\n"
"v_mfma_f32_16x16x32_bf16 a[40:43], a[168:171], v[200:203], a[40:43]\n"
"ds_read_b128 a[112:115], v243 offset:45056\n"
"global_load_lds_dwordx4 v245, s[20:21] offset:2048 sc1\n"
"v_mfma_f32_16x16x32_bf16 a[40:43], a[172:175], v[208:211], a[40:43]\n"
"ds_read_b128 a[116:119], v243 offset:46080\n"
"v_exp_f32 v226, v138\n"
"v_mfma_f32_16x16x32_bf16 a[44:47], a[168:171], v[204:207], a[44:47]\n"
"v_exp_f32 v227, v139\n"
"v_mfma_f32_16x16x32_bf16 a[44:47], a[172:175], v[212:215], a[44:47]\n"
"ds_read_b128 a[120:123], v243 offset:47104\n"
"global_load_lds_dwordx4 v245, s[20:21] offset:3072 sc1\n"
"v_mfma_f32_16x16x32_bf16 a[48:51], a[176:179], v[200:203], a[48:51]\n"
"s_add_u32 s20, s20, s33\n"
"v_add_f32 v232, v232, v224\n"
"v_add_f32 v232, v232, v225\n"
"v_cvt_pk_bf16_f32 v194, v224, v225\n"
"v_mfma_f32_16x16x32_bf16 a[48:51], a[180:183], v[208:211], a[48:51]\n"
"ds_read_b128 a[124:127], v243 offset:48128\n"
"v_exp_f32 v228, v140\n"
"v_mfma_f32_16x16x32_bf16 a[52:55], a[176:179], v[204:207], a[52:55]\n"
"s_addc_u32 s21, s21, 0\n"
"v_exp_f32 v229, v141\n"
"v_mfma_f32_16x16x32_bf16 a[52:55], a[180:183], v[212:215], a[52:55]\n"
"s_cmp_eq_u32 s43, s32\n"
"v_add_f32 v232, v232, v226\n"
"v_add_f32 v232, v232, v227\n"
"v_cvt_pk_bf16_f32 v195, v226, v227\n"
"v_mfma_f32_16x16x32_bf16 a[56:59], a[184:187], v[200:203], a[56:59]\n"
"s_cselect_b32 s20, s22, s20\n"
"v_exp_f32 v230, v142\n"
"v_exp_f32 v231, v143\n"
"v_mfma_f32_16x16x32_bf16 a[56:59], a[188:191], v[208:211], a[56:59]\n"
"s_cselect_b32 s21, s23, s21\n"
"v_add_f32 v233, v233, v228\n"
"v_add_f32 v233, v233, v229\n"
"v_cvt_pk_bf16_f32 v198, v228, v229\n"
"v_mfma_f32_16x16x32_bf16 a[60:63], a[184:187], v[204:207], a[60:63]\n"
"v_exp_f32 v224, v144\n"
"v_exp_f32 v225, v145\n"
"v_mfma_f32_16x16x32_bf16 a[60:63], a[188:191], v[212:215], a[60:63]\n"
"v_add_f32 v233, v233, v230\n"
"v_add_f32 v233, v233, v231\n"
"v_cvt_pk_bf16_f32 v199, v230, v231\n"
"s_waitcnt lgkmcnt(0)\n"
"v_mfma_f32_16x16x32_bf16 v[160:163], a[64:67], v[64:67], v[216:219]\n"
"ds_read_b128 a[128:131], v243 offset:16384\n"
"v_exp_f32 v226, v146\n"
"v_mfma_f32_16x16x32_bf16 v[160:163], a[68:71], v[68:71], v[160:163]\n"
"s_bitcmp1_b64 s[30:31], s26\n"
"v_exp_f32 v227, v147\n"
"v_mfma_f32_16x16x32_bf16 v[160:163], a[72:75], v[72:75], v[160:163]\n"
"ds_read_b128 a[132:135], v243 offset:17408\n"
"v_add_f32 v232, v232, v224\n"
"v_mfma_f32_16x16x32_bf16 v[160:163], a[76:79], v[76:79], v[160:163]\n"
"ds_read_b128 a[136:139], v243 offset:18432\n"
"v_add_f32 v232, v232, v225\n"
"v_mfma_f32_16x16x32_bf16 v[164:167], a[64:67], v[80:83], v[220:223]\n"
"s_cselect_b64 s[56:57], -1, 0\n"
"v_cvt_pk_bf16_f32 v208, v224, v225\n"
"v_exp_f32 v228, v148\n"
"v_mfma_f32_16x16x32_bf16 v[164:167], a[68:71], v[84:87], v[164:167]\n"
"ds_read_b128 a[140:143], v243 offset:19456\n"
"v_exp_f32 v229, v149\n"
"v_mfma_f32_16x16x32_bf16 v[164:167], a[72:75], v[88:91], v[164:167]\n"
"v_add_f32 v232, v232, v226\n"
"v_add_f32 v232, v232, v227\n"
"v_mfma_f32_16x16x32_bf16 v[164:167], a[76:79], v[92:95], v[164:167]\n"
"ds_read_b128 a[144:147], v243 offset:20480\n"
"v_cvt_pk_bf16_f32 v209, v226, v227\n"
"v_mfma_f32_16x16x32_bf16 v[168:171], a[80:83], v[64:67], v[216:219]\n"
"ds_read_b128 a[148:151], v243 offset:21504\n"
"v_exp_f32 v230, v150\n"
"v_mfma_f32_16x16x32_bf16 v[168:171], a[84:87], v[68:71], v[168:171]\n"
"v_exp_f32 v231, v151\n"
"v_mfma_f32_16x16x32_bf16 v[168:171], a[88:91], v[72:75], v[168:171]\n"
"ds_read_b128 a[152:155], v243 offset:22528\n"
"v_add_f32 v233, v233, v228\n"
"v_mfma_f32_16x16x32_bf16 v[168:171], a[92:95], v[76:79], v[168:171]\n"
"ds_read_b128 a[156:159], v243 offset:23552\n"
"v_add_f32 v233, v233, v229\n"
"v_mfma_f32_16x16x32_bf16 v[172:175], a[80:83], v[80:83], v[220:223]\n"
"v_cvt_pk_bf16_f32 v212, v228, v229\n"
"v_exp_f32 v224, v152\n"
"v_mfma_f32_16x16x32_bf16 v[172:175], a[84:87], v[84:87], v[172:175]\n"
"ds_read_b128 a[160:163], v243 offset:24576\n"
"v_exp_f32 v225, v153\n"
"v_mfma_f32_16x16x32_bf16 v[172:175], a[88:91], v[88:91], v[172:175]\n"
"ds_read_b128 a[164:167], v243 offset:25600\n"
"v_add_f32 v233, v233, v230\n"
"v_mfma_f32_16x16x32_bf16 v[172:175], a[92:95], v[92:95], v[172:175]\n"
"v_add_f32 v233, v233, v231\n"
"v_cvt_pk_bf16_f32 v213, v230, v231\n"
"v_mfma_f32_16x16x32_bf16 v[176:179], a[96:99], v[64:67], v[216:219]\n"
"ds_read_b128 a[168:171], v243 offset:26624\n"
"v_exp_f32 v226, v154\n"
"v_mfma_f32_16x16x32_bf16 v[176:179], a[100:103], v[68:71], v[176:179]\n"
"ds_read_b128 a[172:175], v243 offset:27648\n"
"v_exp_f32 v227, v155\n"
"v_mfma_f32_16x16x32_bf16 v[176:179], a[104:107], v[72:75], v[176:179]\n"
"v_add_f32 v232, v232, v224\n"
"v_add_f32 v232, v232, v225\n"
"v_mfma_f32_16x16x32_bf16 v[176:179], a[108:111], v[76:79], v[176:179]\n"
"ds_read_b128 a[176:179], v243 offset:28672\n"
"v_cvt_pk_bf16_f32 v210, v224, v225\n"
"v_mfma_f32_16x16x32_bf16 v[180:183], a[96:99], v[80:83], v[220:223]\n"
"v_exp_f32 v228, v156\n"
"v_mfma_f32_16x16x32_bf16 v[180:183], a[100:103], v[84:87], v[180:183]\n"
"ds_read_b128 a[180:183], v243 offset:29696\n"
"v_exp_f32 v229, v157\n"
"v_mfma_f32_16x16x32_bf16 v[180:183], a[104:107], v[88:91], v[180:183]\n"
"ds_read_b128 a[184:187], v243 offset:30720\n"
"v_add_f32 v232, v232, v226\n"
"v_mfma_f32_16x16x32_bf16 v[180:183], a[108:111], v[92:95], v[180:183]\n"
"v_add_f32 v232, v232, v227\n"
"v_cvt_pk_bf16_f32 v211, v226, v227\n"
"v_mfma_f32_16x16x32_bf16 v[184:187], a[112:115], v[64:67], v[216:219]\n"
"ds_read_b128 a[188:191], v243 offset:31744\n"
"v_exp_f32 v230, v158\n"
"v_mfma_f32_16x16x32_bf16 v[184:187], a[116:119], v[68:71], v[184:187]\n"
"v_exp_f32 v231, v159\n"
"v_mfma_f32_16x16x32_bf16 v[184:187], a[120:123], v[72:75], v[184:187]\n"
"v_add_f32 v233, v233, v228\n"
"v_add_f32 v233, v233, v229\n"
"v_cvt_pk_bf16_f32 v214, v228, v229\n"
"v_mfma_f32_16x16x32_bf16 v[184:187], a[124:127], v[76:79], v[184:187]\n"
"v_add_f32 v233, v233, v230\n"
"v_add_f32 v233, v233, v231\n"
"v_cvt_pk_bf16_f32 v215, v230, v231\n"
"v_mfma_f32_16x16x32_bf16 v[188:191], a[112:115], v[80:83], v[220:223]\n"
"v_max_f32 v246, v232, v233\n"
"v_mfma_f32_16x16x32_bf16 v[188:191], a[116:119], v[84:87], v[188:191]\n"
"v_cmp_nle_f32 vcc, v246, v247\n"
"v_xor_b32 v243, 0x10000, v243\n"
"v_mfma_f32_16x16x32_bf16 v[188:191], a[120:123], v[88:91], v[188:191]\n"
"s_or_b64 vcc, vcc, s[56:57]\n"
"v_mfma_f32_16x16x32_bf16 v[188:191], a[124:127], v[92:95], v[188:191]\n"
"s_cbranch_vccnz 21f\n"
"31:\n"
"s_waitcnt vmcnt(8) lgkmcnt(0)\n"
"s_barrier\n"
"v_mfma_f32_16x16x32_bf16 a[0:3], a[128:131], v[192:195], a[0:3]\n"
"s_mov_b32 m0, s24\n"
"v_exp_f32 v224, v160\n"
"v_mfma_f32_16x16x32_bf16 a[0:3], a[132:135], v[208:211], a[0:3]\n"
"ds_read_b128 a[64:67], v244 offset:0\n"
"global_load_lds_dwordx4 v245, s[20:21] offset:-4096 sc1\n"
"v_mfma_f32_16x16x32_bf16 a[4:7], a[128:131], v[196:199], a[4:7]\n"
"ds_read_b128 a[68:71], v244 offset:1024\n"
"v_exp_f32 v225, v161\n"
"v_mfma_f32_16x16x32_bf16 a[4:7], a[132:135], v[212:215], a[4:7]\n"
"s_add_u32 s26, s26, 1\n"
"v_exp_f32 v226, v162\n"
"v_mfma_f32_16x16x32_bf16 a[8:11], a[136:139], v[192:195], a[8:11]\n"
"ds_read_b128 a[72:75], v244 offset:2048\n"
"global_load_lds_dwordx4 v245, s[20:21] offset:-3072 sc1\n"
"v_mfma_f32_16x16x32_bf16 a[8:11], a[140:143], v[208:211], a[8:11]\n"
"ds_read_b128 a[76:79], v244 offset:3072\n"
"v_exp_f32 v227, v163\n"
"v_mfma_f32_16x16x32_bf16 a[12:15], a[136:139], v[196:199], a[12:15]\n"
"s_add_u32 s43, s43, 1\n"
"v_add_f32 v234, v232, v224\n"
"v_add_f32 v234, v234, v225\n"
"v_cvt_pk_bf16_f32 v200, v224, v225\n"
"v_mfma_f32_16x16x32_bf16 a[12:15], a[140:143], v[212:215], a[12:15]\n"
"ds_read_b128 a[80:83], v244 offset:4096\n"
"global_load_lds_dwordx4 v245, s[20:21] offset:-2048 sc1\n"
"v_mfma_f32_16x16x32_bf16 a[16:19], a[144:147], v[192:195], a[16:19]\n"
"ds_read_b128 a[84:87], v244 offset:5120\n"
"v_exp_f32 v228, v164\n"
"v_mfma_f32_16x16x32_bf16 a[16:19], a[148:151], v[208:211], a[16:19]\n"
"s_add_u32 s24, s24, 32768\n"
"v_exp_f32 v229, v165\n"
"v_mfma_f32_16x16x32_bf16 a[20:23], a[144:147], v[196:199], a[20:23]\n"
"ds_read_b128 a[88:91], v244 offset:6144\n"
"global_load_lds_dwordx4 v245, s[20:21] offset:-1024 sc1\n"
"v_mfma_f32_16x16x32_bf16 a[20:23], a[148:151], v[212:215], a[20:23]\n"
"s_and_b32 s24, s24, 0x1ffff\n"
"v_add_f32 v234, v234, v226\n"
"v_add_f32 v234, v234, v227\n"
"v_cvt_pk_bf16_f32 v201, v226, v227\n"
"v_mfma_f32_16x16x32_bf16 a[24:27], a[152:155], v[192:195], a[24:27]\n"
"ds_read_b128 a[92:95], v244 offset:7168\n"
"v_exp_f32 v230, v166\n"
"v_mfma_f32_16x16x32_bf16 a[24:27], a[156:159], v[208:211], a[24:27]\n"
"ds_read_b128 a[96:99], v244 offset:8192\n"
"global_load_lds_dwordx4 v245, s[20:21] offset:0 sc1\n"
"v_mfma_f32_16x16x32_bf16 a[28:31], a[152:155], v[196:199], a[28:31]\n"
"s_cmp_eq_u32 s43, s34\n"
"v_exp_f32 v231, v167\n"
"v_mfma_f32_16x16x32_bf16 a[28:31], a[156:159], v[212:215], a[28:31]\n"
"ds_read_b128 a[100:103], v244 offset:9216\n"
"v_add_f32 v235, v233, v228\n"
"v_add_f32 v235, v235, v229\n"
"v_mfma_f32_16x16x32_bf16 a[32:35], a[160:163], v[192:195], a[32:35]\n"
"s_cselect_b32 s33, 5, 1\n"
"v_cvt_pk_bf16_f32 v204, v228, v229\n"
"v_exp_f32 v224, v168\n"
"v_mfma_f32_16x16x32_bf16 a[32:35], a[164:167], v[208:211], a[32:35]\n"
"ds_read_b128 a[104:107], v244 offset:10240\n"
"global_load_lds_dwordx4 v245, s[20:21] offset:1024 sc1\n"
"v_mfma_f32_16x16x32_bf16 a[36:39], a[160:163], v[196:199], a[36:39]\n"
"ds_read_b128 a[108:111], v244 offset:11264\n"
"v_exp_f32 v225, v169\n"
"v_mfma_f32_16x16x32_bf16 a[36:39], a[164:167], v[212:215], a[36:39]\n"
"s_lshl_b32 s33, s33, 15\n"
"v_add_f32 v235, v235, v230\n"
"v_add_f32 v235, v235, v231\n"
"v_cvt_pk_bf16_f32 v205, v230, v231\n"
"v_mfma_f32_16x16x32_bf16 a[40:43], a[168:171], v[192:195], a[40:43]\n"
"ds_read_b128 a[112:115], v244 offset:12288\n"
"global_load_lds_dwordx4 v245, s[20:21] offset:2048 sc1\n"
"v_mfma_f32_16x16x32_bf16 a[40:43], a[172:175], v[208:211], a[40:43]\n"
"ds_read_b128 a[116:119], v244 offset:13312\n"
"v_exp_f32 v226, v170\n"
"v_mfma_f32_16x16x32_bf16 a[44:47], a[168:171], v[196:199], a[44:47]\n"
"v_exp_f32 v227, v171\n"
"v_mfma_f32_16x16x32_bf16 a[44:47], a[172:175], v[212:215], a[44:47]\n"
"ds_read_b128 a[120:123], v244 offset:14336\n"
"global_load_lds_dwordx4 v245, s[20:21] offset:3072 sc1\n"
"v_mfma_f32_16x16x32_bf16 a[48:51], a[176:179], v[192:195], a[48:51]\n"
"s_add_u32 s20, s20, s33\n"
"v_add_f32 v234, v234, v224\n"
"v_add_f32 v234, v234, v225\n"
"v_cvt_pk_bf16_f32 v202, v224, v225\n"
"v_mfma_f32_16x16x32_bf16 a[48:51], a[180:183], v[208:211], a[48:51]\n"
"ds_read_b128 a[124:127], v244 offset:15360\n"
"v_exp_f32 v228, v172\n"
"v_mfma_f32_16x16x32_bf16 a[52:55], a[176:179], v[196:199], a[52:55]\n"
"s_addc_u32 s21, s21, 0\n"
"v_exp_f32 v229, v173\n"
"v_add_f32 v234, v234, v226\n"
"v_mfma_f32_16x16x32_bf16 a[52:55], a[180:183], v[212:215], a[52:55]\n"
"s_cmp_eq_u32 s43, s32\n"
"v_add_f32 v234, v234, v227\n"
"v_cvt_pk_bf16_f32 v203, v226, v227\n"
"v_xor_b32 v244, 0x10000, v244\n"
"v_mfma_f32_16x16x32_bf16 a[56:59], a[184:187], v[192:195], a[56:59]\n"
"s_cselect_b32 s20, s22, s20\n"
"v_exp_f32 v230, v174\n"
"v_exp_f32 v231, v175\n"
"v_mfma_f32_16x16x32_bf16 a[56:59], a[188:191], v[208:211], a[56:59]\n"
"s_cselect_b32 s21, s23, s21\n"
"v_add_f32 v235, v235, v228\n"
"v_add_f32 v235, v235, v229\n"
"v_cvt_pk_bf16_f32 v206, v228, v229\n"
"v_mfma_f32_16x16x32_bf16 a[60:63], a[184:187], v[196:199], a[60:63]\n"
"s_cmp_lg_u32 s26, 34\n"
"v_exp_f32 v224, v176\n"
"v_exp_f32 v225, v177\n"
"v_mfma_f32_16x16x32_bf16 a[60:63], a[188:191], v[212:215], a[60:63]\n"
"v_add_f32 v235, v235, v230\n"
"v_add_f32 v235, v235, v231\n"
"v_cvt_pk_bf16_f32 v207, v230, v231\n"
"s_waitcnt lgkmcnt(0)\n"
"s_cbranch_scc1 1b\n"
"ds_read_b128 a[128:131], v244 offset:49152\n"
"ds_read_b128 a[132:135], v244 offset:50176\n"
"ds_read_b128 a[136:139], v244 offset:51200\n"
"ds_read_b128 a[140:143], v244 offset:52224\n"
"ds_read_b128 a[144:147], v244 offset:53248\n"
"ds_read_b128 a[148:151], v244 offset:54272\n"
"ds_read_b128 a[152:155], v244 offset:55296\n"
"ds_read_b128 a[156:159], v244 offset:56320\n"
"ds_read_b128 a[160:163], v244 offset:57344\n"
"ds_read_b128 a[164:167], v244 offset:58368\n"
"ds_read_b128 a[168:171], v244 offset:59392\n"
"ds_read_b128 a[172:175], v244 offset:60416\n"
"ds_read_b128 a[176:179], v244 offset:61440\n"
"ds_read_b128 a[180:183], v244 offset:62464\n"
"ds_read_b128 a[184:187], v244 offset:63488\n"
"ds_read_b128 a[188:191], v244 offset:64512\n"
"v_exp_f32 v226, v178\n"
"v_exp_f32 v227, v179\n"
"v_add_f32 v234, v234, v224\n"
"v_add_f32 v234, v234, v225\n"
"v_cvt_pk_bf16_f32 v208, v224, v225\n"
"v_exp_f32 v228, v180\n"
"v_exp_f32 v229, v181\n"
"v_add_f32 v234, v234, v226\n"
"v_add_f32 v234, v234, v227\n"
"v_cvt_pk_bf16_f32 v209, v226, v227\n"
"v_exp_f32 v230, v182\n"
"v_exp_f32 v231, v183\n"
"v_add_f32 v235, v235, v228\n"
"v_add_f32 v235, v235, v229\n"
"v_cvt_pk_bf16_f32 v212, v228, v229\n"
"v_exp_f32 v224, v184\n"
"v_exp_f32 v225, v185\n"
"v_add_f32 v235, v235, v230\n"
"v_add_f32 v235, v235, v231\n"
"v_cvt_pk_bf16_f32 v213, v230, v231\n"
"v_exp_f32 v226, v186\n"
"v_exp_f32 v227, v187\n"
"v_add_f32 v234, v234, v224\n"
"v_add_f32 v234, v234, v225\n"
"v_cvt_pk_bf16_f32 v210, v224, v225\n"
"v_exp_f32 v228, v188\n"
"v_exp_f32 v229, v189\n"
"v_add_f32 v234, v234, v226\n"
"v_add_f32 v234, v234, v227\n"
"v_cvt_pk_bf16_f32 v211, v226, v227\n"
"v_exp_f32 v230, v190\n"
"v_exp_f32 v231, v191\n"
"v_add_f32 v235, v235, v228\n"
"v_add_f32 v235, v235, v229\n"
"v_cvt_pk_bf16_f32 v214, v228, v229\n"
"v_add_f32 v235, v235, v230\n"
"v_add_f32 v235, v235, v231\n"
"v_cvt_pk_bf16_f32 v215, v230, v231\n"
"v_max_f32 v246, v234, v235\n"
"v_cmp_nle_f32 vcc, v246, v247\n"
"s_mov_b32 s35, 1\n"
"s_waitcnt lgkmcnt(0)\n"
"s_cbranch_vccz 8f\n"
"s_call_b64 s[36:37], 61f\n"
"8:\n"
"v_mfma_f32_16x16x32_bf16 a[0:3], a[128:131], v[200:203], a[0:3]\n"
"v_mfma_f32_16x16x32_bf16 a[0:3], a[132:135], v[208:211], a[0:3]\n"
"v_mfma_f32_16x16x32_bf16 a[4:7], a[128:131], v[204:207], a[4:7]\n"
"v_mfma_f32_16x16x32_bf16 a[4:7], a[132:135], v[212:215], a[4:7]\n"
"v_mfma_f32_16x16x32_bf16 a[8:11], a[136:139], v[200:203], a[8:11]\n"
"v_mfma_f32_16x16x32_bf16 a[8:11], a[140:143], v[208:211], a[8:11]\n"
"v_mfma_f32_16x16x32_bf16 a[12:15], a[136:139], v[204:207], a[12:15]\n"
"v_mfma_f32_16x16x32_bf16 a[12:15], a[140:143], v[212:215], a[12:15]\n"
"v_mfma_f32_16x16x32_bf16 a[16:19], a[144:147], v[200:203], a[16:19]\n"
"v_mfma_f32_16x16x32_bf16 a[16:19], a[148:151], v[208:211], a[16:19]\n"
"v_mfma_f32_16x16x32_bf16 a[20:23], a[144:147], v[204:207], a[20:23]\n"
"v_mfma_f32_16x16x32_bf16 a[20:23], a[148:151], v[212:215], a[20:23]\n"
"v_mfma_f32_16x16x32_bf16 a[24:27], a[152:155], v[200:203], a[24:27]\n"
"v_mfma_f32_16x16x32_bf16 a[24:27], a[156:159], v[208:211], a[24:27]\n"
"v_mfma_f32_16x16x32_bf16 a[28:31], a[152:155], v[204:207], a[28:31]\n"
"v_mfma_f32_16x16x32_bf16 a[28:31], a[156:159], v[212:215], a[28:31]\n"
"v_mfma_f32_16x16x32_bf16 a[32:35], a[160:163], v[200:203], a[32:35]\n"
"v_mfma_f32_16x16x32_bf16 a[32:35], a[164:167], v[208:211], a[32:35]\n"
"v_mfma_f32_16x16x32_bf16 a[36:39], a[160:163], v[204:207], a[36:39]\n"
"v_mfma_f32_16x16x32_bf16 a[36:39], a[164:167], v[212:215], a[36:39]\n"
"v_mfma_f32_16x16x32_bf16 a[40:43], a[168:171], v[200:203], a[40:43]\n"
"v_mfma_f32_16x16x32_bf16 a[40:43], a[172:175], v[208:211], a[40:43]\n"
"v_mfma_f32_16x16x32_bf16 a[44:47], a[168:171], v[204:207], a[44:47]\n"
"v_mfma_f32_16x16x32_bf16 a[44:47], a[172:175], v[212:215], a[44:47]\n"
"v_mfma_f32_16x16x32_bf16 a[48:51], a[176:179], v[200:203], a[48:51]\n"
"v_mfma_f32_16x16x32_bf16 a[48:51], a[180:183], v[208:211], a[48:51]\n"
"v_mfma_f32_16x16x32_bf16 a[52:55], a[176:179], v[204:207], a[52:55]\n"
"v_mfma_f32_16x16x32_bf16 a[52:55], a[180:183], v[212:215], a[52:55]\n"
"v_mfma_f32_16x16x32_bf16 a[56:59], a[184:187], v[200:203], a[56:59]\n"
"v_mfma_f32_16x16x32_bf16 a[56:59], a[188:191], v[208:211], a[56:59]\n"
"v_mfma_f32_16x16x32_bf16 a[60:63], a[184:187], v[204:207], a[60:63]\n"
"v_mfma_f32_16x16x32_bf16 a[60:63], a[188:191], v[212:215], a[60:63]\n"
"v_mov_b32 v236, v234\n"
"v_mov_b32 v237, v235\n"
"s_nop 7\n"
"s_call_b64 s[36:37], 7f\n"
"s_branch 99f\n"
"20:\n"
"v_cmp_nle_f32 vcc, v246, v247\n"
"s_cmp_eq_u32 s26, s32\n"
"s_cselect_b32 s35, 1, 0\n"
"s_nop 4\n"
"s_cbranch_vccz 40f\n"
"s_call_b64 s[36:37], 61f\n"
"40:\n"
"s_cmp_eq_u32 s26, 2\n"
"s_cbranch_scc0 47f\n"
"global_load_dword v5, v6, s[68:69] sc1\n"
"s_lshl_b32 s34, s32, 1\n"
"s_sub_u32 s34, s34, 32\n"
"s_cmp_eq_u32 s34, 4\n"
"s_cselect_b32 s28, 0x20000, 0\n"
"s_add_u32 s20, s22, s28\n"
"s_addc_u32 s21, s23, 0\n"
"s_mov_b32 s24, s25\n"
"s_mov_b32 m0, s25\n"
"s_mov_b32 s43, 4\n"
"81:\n"
"s_waitcnt vmcnt(0)\n"
"v_cmp_ne_u32 vcc, 1, v5\n"
"s_cbranch_vccnz 811f\n"
"812:\n"
"global_load_lds_dwordx4 v245, s[20:21] offset:-4096 sc1\n"
"global_load_lds_dwordx4 v245, s[20:21] offset:-3072 sc1\n"
"global_load_lds_dwordx4 v245, s[20:21] offset:-2048 sc1\n"
"global_load_lds_dwordx4 v245, s[20:21] offset:-1024 sc1\n"
"global_load_lds_dwordx4 v245, s[20:21] offset:0 sc1\n"
"global_load_lds_dwordx4 v245, s[20:21] offset:1024 sc1\n"
"global_load_lds_dwordx4 v245, s[20:21] offset:2048 sc1\n"
"global_load_lds_dwordx4 v245, s[20:21] offset:3072 sc1\n"
"s_add_u32 s43, s43, 1\n"
"s_cmp_eq_u32 s43, s34\n"
"s_cselect_b32 s33, 5, 1\n"
"s_lshl_b32 s33, s33, 15\n"
"s_add_u32 s20, s20, s33\n"
"s_addc_u32 s21, s21, 0\n"
"s_cmp_eq_u32 s43, s32\n"
"s_cselect_b32 s20, s22, s20\n"
"s_cselect_b32 s21, s23, s21\n"
"s_add_u32 s24, s24, 32768\n"
"s_and_b32 s24, s24, 0x1ffff\n"
"47:\n"
"s_cmp_eq_u32 s26, s42\n"
"s_cbranch_scc1 41f\n"
"s_cmp_eq_u32 s26, 32\n"
"s_cbranch_scc0 44f\n"
"41:\n"
"v_cmp_gt_i32 s[48:49], 0, v240\n"
"v_cmp_gt_i32 s[50:51], 1, v240\n"
"v_cmp_gt_i32 s[52:53], 2, v240\n"
"v_cmp_gt_i32 s[54:55], 3, v240\n"
"v_cndmask_b32 v128, v128, v242, s[48:49]\n"
"v_cndmask_b32 v129, v129, v242, s[50:51]\n"
"v_cndmask_b32 v130, v130, v242, s[52:53]\n"
"v_cndmask_b32 v131, v131, v242, s[54:55]\n"
"v_cmp_gt_i32 s[48:49], -16, v240\n"
"v_cmp_gt_i32 s[50:51], -15, v240\n"
"v_cmp_gt_i32 s[52:53], -14, v240\n"
"v_cmp_gt_i32 s[54:55], -13, v240\n"
"v_cndmask_b32 v132, v132, v242, s[48:49]\n"
"v_cndmask_b32 v133, v133, v242, s[50:51]\n"
"v_cndmask_b32 v134, v134, v242, s[52:53]\n"
"v_cndmask_b32 v135, v135, v242, s[54:55]\n"
"v_cmp_gt_i32 s[48:49], 16, v240\n"
"v_cmp_gt_i32 s[50:51], 17, v240\n"
"v_cmp_gt_i32 s[52:53], 18, v240\n"
"v_cmp_gt_i32 s[54:55], 19, v240\n"
"v_cndmask_b32 v136, v136, v242, s[48:49]\n"
"v_cndmask_b32 v137, v137, v242, s[50:51]\n"
"v_cndmask_b32 v138, v138, v242, s[52:53]\n"
"v_cndmask_b32 v139, v139, v242, s[54:55]\n"
"v_cmp_gt_i32 s[48:49], 0, v240\n"
"v_cmp_gt_i32 s[50:51], 1, v240\n"
"v_cmp_gt_i32 s[52:53], 2, v240\n"
"v_cmp_gt_i32 s[54:55], 3, v240\n"
"v_cndmask_b32 v140, v140, v242, s[48:49]\n"
"v_cndmask_b32 v141, v141, v242, s[50:51]\n"
"v_cndmask_b32 v142, v142, v242, s[52:53]\n"
"v_cndmask_b32 v143, v143, v242, s[54:55]\n"
"v_cmp_gt_i32 s[48:49], 32, v240\n"
"v_cmp_gt_i32 s[50:51], 33, v240\n"
"v_cmp_gt_i32 s[52:53], 34, v240\n"
"v_cmp_gt_i32 s[54:55], 35, v240\n"
"v_cndmask_b32 v144, v144, v242, s[48:49]\n"
"v_cndmask_b32 v145, v145, v242, s[50:51]\n"
"v_cndmask_b32 v146, v146, v242, s[52:53]\n"
"v_cndmask_b32 v147, v147, v242, s[54:55]\n"
"v_cmp_gt_i32 s[48:49], 16, v240\n"
"v_cmp_gt_i32 s[50:51], 17, v240\n"
"v_cmp_gt_i32 s[52:53], 18, v240\n"
"v_cmp_gt_i32 s[54:55], 19, v240\n"
"v_cndmask_b32 v148, v148, v242, s[48:49]\n"
"v_cndmask_b32 v149, v149, v242, s[50:51]\n"
"v_cndmask_b32 v150, v150, v242, s[52:53]\n"
"v_cndmask_b32 v151, v151, v242, s[54:55]\n"
"v_cmp_gt_i32 s[48:49], 48, v240\n"
"v_cmp_gt_i32 s[50:51], 49, v240\n"
"v_cmp_gt_i32 s[52:53], 50, v240\n"
"v_cmp_gt_i32 s[54:55], 51, v240\n"
"v_cndmask_b32 v152, v152, v242, s[48:49]\n"
"v_cndmask_b32 v153, v153, v242, s[50:51]\n"
"v_cndmask_b32 v154, v154, v242, s[52:53]\n"
"v_cndmask_b32 v155, v155, v242, s[54:55]\n"
"v_cmp_gt_i32 s[48:49], 32, v240\n"
"v_cmp_gt_i32 s[50:51], 33, v240\n"
"v_cmp_gt_i32 s[52:53], 34, v240\n"
"v_cmp_gt_i32 s[54:55], 35, v240\n"
"v_cndmask_b32 v156, v156, v242, s[48:49]\n"
"v_cndmask_b32 v157, v157, v242, s[50:51]\n"
"v_cndmask_b32 v158, v158, v242, s[52:53]\n"
"v_cndmask_b32 v159, v159, v242, s[54:55]\n"
"44:\n"
"s_cmp_eq_u32 s26, s32\n"
"s_cbranch_scc0 30b\n"
"v_mov_b32 v236, v234\n"
"v_mov_b32 v237, v235\n"
"v_mov_b32 v234, 0\n"
"v_mov_b32 v235, 0\n"
"43:\n"
"v_max3_f32 v238, v128, v129, v130\n"
"v_max3_f32 v238, v238, v131, v136\n"
"v_max3_f32 v238, v238, v137, v138\n"
"v_max3_f32 v238, v238, v139, v144\n"
"v_max3_f32 v238, v238, v145, v146\n"
"v_max3_f32 v238, v238, v147, v152\n"
"v_max3_f32 v238, v238, v153, v154\n"
"v_max_f32 v238, v238, v155\n"
"v_mov_b32 v255, v238\n"
"s_nop 1\n"
"v_permlane32_swap_b32 v238, v255\n"
"v_max_f32 v238, v238, v255\n"
"v_mov_b32 v255, v238\n"
"s_nop 1\n"
"v_permlane16_swap_b32 v238, v255\n"
"v_max_f32 v238, v238, v255\n"
"v_sub_f32 v128, v128, v238\n"
"v_sub_f32 v129, v129, v238\n"
"v_sub_f32 v130, v130, v238\n"
"v_sub_f32 v131, v131, v238\n"
"v_sub_f32 v136, v136, v238\n"
"v_sub_f32 v137, v137, v238\n"
"v_sub_f32 v138, v138, v238\n"
"v_sub_f32 v139, v139, v238\n"
"v_sub_f32 v144, v144, v238\n"
"v_sub_f32 v145, v145, v238\n"
"v_sub_f32 v146, v146, v238\n"
"v_sub_f32 v147, v147, v238\n"
"v_sub_f32 v152, v152, v238\n"
"v_sub_f32 v153, v153, v238\n"
"v_sub_f32 v154, v154, v238\n"
"v_sub_f32 v155, v155, v238\n"
"v_sub_f32 v216, 0, v238\n"
"v_sub_f32 v217, 0, v238\n"
"v_sub_f32 v218, 0, v238\n"
"v_sub_f32 v219, 0, v238\n"
"v_max3_f32 v239, v132, v133, v134\n"
"v_max3_f32 v239, v239, v135, v140\n"
"v_max3_f32 v239, v239, v141, v142\n"
"v_max3_f32 v239, v239, v143, v148\n"
"v_max3_f32 v239, v239, v149, v150\n"
"v_max3_f32 v239, v239, v151, v156\n"
"v_max3_f32 v239, v239, v157, v158\n"
"v_max_f32 v239, v239, v159\n"
"v_mov_b32 v255, v239\n"
"s_nop 1\n"
"v_permlane32_swap_b32 v239, v255\n"
"v_max_f32 v239, v239, v255\n"
"v_mov_b32 v255, v239\n"
"s_nop 1\n"
"v_permlane16_swap_b32 v239, v255\n"
"v_max_f32 v239, v239, v255\n"
"v_sub_f32 v132, v132, v239\n"
"v_sub_f32 v133, v133, v239\n"
"v_sub_f32 v134, v134, v239\n"
"v_sub_f32 v135, v135, v239\n"
"v_sub_f32 v140, v140, v239\n"
"v_sub_f32 v141, v141, v239\n"
"v_sub_f32 v142, v142, v239\n"
"v_sub_f32 v143, v143, v239\n"
"v_sub_f32 v148, v148, v239\n"
"v_sub_f32 v149, v149, v239\n"
"v_sub_f32 v150, v150, v239\n"
"v_sub_f32 v151, v151, v239\n"
"v_sub_f32 v156, v156, v239\n"
"v_sub_f32 v157, v157, v239\n"
"v_sub_f32 v158, v158, v239\n"
"v_sub_f32 v159, v159, v239\n"
"v_sub_f32 v220, 0, v239\n"
"v_sub_f32 v221, 0, v239\n"
"v_sub_f32 v222, 0, v239\n"
"v_sub_f32 v223, 0, v239\n"
"s_cmp_eq_u32 s26, 0\n"
"s_cbranch_scc1 33b\n"
"s_branch 30b\n"
"21:\n"
"v_cmp_nle_f32 vcc, v246, v247\n"
"s_cmp_eq_u32 s26, s32\n"
"s_cselect_b32 s35, 1, 0\n"
"s_nop 4\n"
"s_cbranch_vccz 42f\n"
"s_call_b64 s[36:37], 60f\n"
"42:\n"
"s_cmp_eq_u32 s26, 1\n"
"s_cbranch_scc0 48f\n"
"s_waitcnt vmcnt(0)\n"
"v_mov_b32 v3, 0\n"
"v_mov_b32 v4, 1\n"
"s_mov_b64 exec, 1\n"
"global_store_dword v3, v4, s[66:67] sc1\n"
"s_mov_b64 exec, -1\n"
"s_add_u32 s24, s25, 0x20000\n"
"v_and_b32 v6, 31, v2\n"
"v_lshlrev_b32 v6, 2, v6\n"
"s_branch 31b\n"
"48:\n"
"s_sub_u32 s28, s26, 1\n"
"s_cmp_eq_u32 s28, s32\n"
"s_cbranch_scc0 45f\n"
"s_call_b64 s[36:37], 7f\n"
"s_mov_b64 s[38:39], s[40:41]\n"
"45:\n"
"s_add_u32 s28, s26, 1\n"
"s_cmp_eq_u32 s28, s32\n"
"s_cbranch_scc0 49f\n"
"v_mov_b32 v64, v96\n"
"v_mov_b32 v65, v97\n"
"v_mov_b32 v66, v98\n"
"v_mov_b32 v67, v99\n"
"v_mov_b32 v68, v100\n"
"v_mov_b32 v69, v101\n"
"v_mov_b32 v70, v102\n"
"v_mov_b32 v71, v103\n"
"v_mov_b32 v72, v104\n"
"v_mov_b32 v73, v105\n"
"v_mov_b32 v74, v106\n"
"v_mov_b32 v75, v107\n"
"v_mov_b32 v76, v108\n"
"v_mov_b32 v77, v109\n"
"v_mov_b32 v78, v110\n"
"v_mov_b32 v79, v111\n"
"v_mov_b32 v80, v112\n"
"v_mov_b32 v81, v113\n"
"v_mov_b32 v82, v114\n"
"v_mov_b32 v83, v115\n"
"v_mov_b32 v84, v116\n"
"v_mov_b32 v85, v117\n"
"v_mov_b32 v86, v118\n"
"v_mov_b32 v87, v119\n"
"v_mov_b32 v88, v120\n"
"v_mov_b32 v89, v121\n"
"v_mov_b32 v90, v122\n"
"v_mov_b32 v91, v123\n"
"v_mov_b32 v92, v124\n"
"v_mov_b32 v93, v125\n"
"v_mov_b32 v94, v126\n"
"v_mov_b32 v95, v127\n"
"v_mov_b32 v216, 0\n"
"v_mov_b32 v217, 0\n"
"v_mov_b32 v218, 0\n"
"v_mov_b32 v219, 0\n"
"v_mov_b32 v220, 0\n"
"v_mov_b32 v221, 0\n"
"v_mov_b32 v222, 0\n"
"v_mov_b32 v223, 0\n"
"49:\n"
"s_cmp_eq_u32 s26, 33\n"
"s_cbranch_scc1 46f\n"
"s_sub_u32 s28, s26, 1\n"
"s_cmp_eq_u32 s28, s42\n"
"s_cbranch_scc0 31b\n"
"46:\n"
"v_cmp_gt_i32 s[48:49], 0, v241\n"
"v_cmp_gt_i32 s[50:51], 1, v241\n"
"v_cmp_gt_i32 s[52:53], 2, v241\n"
"v_cmp_gt_i32 s[54:55], 3, v241\n"
"v_cndmask_b32 v160, v160, v242, s[48:49]\n"
"v_cndmask_b32 v161, v161, v242, s[50:51]\n"
"v_cndmask_b32 v162, v162, v242, s[52:53]\n"
"v_cndmask_b32 v163, v163, v242, s[54:55]\n"
"v_cmp_gt_i32 s[48:49], -16, v241\n"
"v_cmp_gt_i32 s[50:51], -15, v241\n"
"v_cmp_gt_i32 s[52:53], -14, v241\n"
"v_cmp_gt_i32 s[54:55], -13, v241\n"
"v_cndmask_b32 v164, v164, v242, s[48:49]\n"
"v_cndmask_b32 v165, v165, v242, s[50:51]\n"
"v_cndmask_b32 v166, v166, v242, s[52:53]\n"
"v_cndmask_b32 v167, v167, v242, s[54:55]\n"
"v_cmp_gt_i32 s[48:49], 16, v241\n"
"v_cmp_gt_i32 s[50:51], 17, v241\n"
"v_cmp_gt_i32 s[52:53], 18, v241\n"
"v_cmp_gt_i32 s[54:55], 19, v241\n"
"v_cndmask_b32 v168, v168, v242, s[48:49]\n"
"v_cndmask_b32 v169, v169, v242, s[50:51]\n"
"v_cndmask_b32 v170, v170, v242, s[52:53]\n"
"v_cndmask_b32 v171, v171, v242, s[54:55]\n"
"v_cmp_gt_i32 s[48:49], 0, v241\n"
"v_cmp_gt_i32 s[50:51], 1, v241\n"
"v_cmp_gt_i32 s[52:53], 2, v241\n"
"v_cmp_gt_i32 s[54:55], 3, v241\n"
"v_cndmask_b32 v172, v172, v242, s[48:49]\n"
"v_cndmask_b32 v173, v173, v242, s[50:51]\n"
"v_cndmask_b32 v174, v174, v242, s[52:53]\n"
"v_cndmask_b32 v175, v175, v242, s[54:55]\n"
"v_cmp_gt_i32 s[48:49], 32, v241\n"
"v_cmp_gt_i32 s[50:51], 33, v241\n"
"v_cmp_gt_i32 s[52:53], 34, v241\n"
"v_cmp_gt_i32 s[54:55], 35, v241\n"
"v_cndmask_b32 v176, v176, v242, s[48:49]\n"
"v_cndmask_b32 v177, v177, v242, s[50:51]\n"
"v_cndmask_b32 v178, v178, v242, s[52:53]\n"
"v_cndmask_b32 v179, v179, v242, s[54:55]\n"
"v_cmp_gt_i32 s[48:49], 16, v241\n"
"v_cmp_gt_i32 s[50:51], 17, v241\n"
"v_cmp_gt_i32 s[52:53], 18, v241\n"
"v_cmp_gt_i32 s[54:55], 19, v241\n"
"v_cndmask_b32 v180, v180, v242, s[48:49]\n"
"v_cndmask_b32 v181, v181, v242, s[50:51]\n"
"v_cndmask_b32 v182, v182, v242, s[52:53]\n"
"v_cndmask_b32 v183, v183, v242, s[54:55]\n"
"v_cmp_gt_i32 s[48:49], 48, v241\n"
"v_cmp_gt_i32 s[50:51], 49, v241\n"
"v_cmp_gt_i32 s[52:53], 50, v241\n"
"v_cmp_gt_i32 s[54:55], 51, v241\n"
"v_cndmask_b32 v184, v184, v242, s[48:49]\n"
"v_cndmask_b32 v185, v185, v242, s[50:51]\n"
"v_cndmask_b32 v186, v186, v242, s[52:53]\n"
"v_cndmask_b32 v187, v187, v242, s[54:55]\n"
"v_cmp_gt_i32 s[48:49], 32, v241\n"
"v_cmp_gt_i32 s[50:51], 33, v241\n"
"v_cmp_gt_i32 s[52:53], 34, v241\n"
"v_cmp_gt_i32 s[54:55], 35, v241\n"
"v_cndmask_b32 v188, v188, v242, s[48:49]\n"
"v_cndmask_b32 v189, v189, v242, s[50:51]\n"
"v_cndmask_b32 v190, v190, v242, s[52:53]\n"
"v_cndmask_b32 v191, v191, v242, s[54:55]\n"
"s_branch 31b\n"
"60:\n"
"s_nop 7\n"
"v_max3_f32 v248, v128, v129, v130\n"
"v_max3_f32 v248, v248, v131, v136\n"
"v_max3_f32 v248, v248, v137, v138\n"
"v_max3_f32 v248, v248, v139, v144\n"
"v_max3_f32 v248, v248, v145, v146\n"
"v_max3_f32 v248, v248, v147, v152\n"
"v_max3_f32 v248, v248, v153, v154\n"
"v_max_f32 v248, v248, v155\n"
"v_mov_b32 v255, v248\n"
"s_nop 1\n"
"v_permlane32_swap_b32 v248, v255\n"
"v_max_f32 v248, v248, v255\n"
"v_mov_b32 v255, v248\n"
"s_nop 1\n"
"v_permlane16_swap_b32 v248, v255\n"
"v_max_f32 v248, v248, v255\n"
"v_max_f32 v248, 0, v248\n"
"v_add_f32 v238, v238, v248\n"
"v_sub_f32 v250, 0, v248\n"
"v_exp_f32 v250, v250\n"
"v_sub_f32 v128, v128, v248\n"
"v_sub_f32 v129, v129, v248\n"
"v_sub_f32 v130, v130, v248\n"
"v_sub_f32 v131, v131, v248\n"
"v_sub_f32 v136, v136, v248\n"
"v_sub_f32 v137, v137, v248\n"
"v_sub_f32 v138, v138, v248\n"
"v_sub_f32 v139, v139, v248\n"
"v_sub_f32 v144, v144, v248\n"
"v_sub_f32 v145, v145, v248\n"
"v_sub_f32 v146, v146, v248\n"
"v_sub_f32 v147, v147, v248\n"
"v_sub_f32 v152, v152, v248\n"
"v_sub_f32 v153, v153, v248\n"
"v_sub_f32 v154, v154, v248\n"
"v_sub_f32 v155, v155, v248\n"
"v_mul_f32 v234, v234, v250\n"
"v_accvgpr_read_b32 v224, a0\n"
"v_accvgpr_read_b32 v225, a1\n"
"v_accvgpr_read_b32 v226, a2\n"
"v_accvgpr_read_b32 v227, a3\n"
"v_mul_f32 v224, v224, v250\n"
"v_mul_f32 v225, v225, v250\n"
"v_mul_f32 v226, v226, v250\n"
"v_mul_f32 v227, v227, v250\n"
"v_accvgpr_write_b32 a0, v224\n"
"v_accvgpr_write_b32 a1, v225\n"
"v_accvgpr_write_b32 a2, v226\n"
"v_accvgpr_write_b32 a3, v227\n"
"v_accvgpr_read_b32 v224, a8\n"
"v_accvgpr_read_b32 v225, a9\n"
"v_accvgpr_read_b32 v226, a10\n"
"v_accvgpr_read_b32 v227, a11\n"
"v_mul_f32 v224, v224, v250\n"
"v_mul_f32 v225, v225, v250\n"
"v_mul_f32 v226, v226, v250\n"
"v_mul_f32 v227, v227, v250\n"
"v_accvgpr_write_b32 a8, v224\n"
"v_accvgpr_write_b32 a9, v225\n"
"v_accvgpr_write_b32 a10, v226\n"
"v_accvgpr_write_b32 a11, v227\n"
"v_accvgpr_read_b32 v224, a16\n"
"v_accvgpr_read_b32 v225, a17\n"
"v_accvgpr_read_b32 v226, a18\n"
"v_accvgpr_read_b32 v227, a19\n"
"v_mul_f32 v224, v224, v250\n"
"v_mul_f32 v225, v225, v250\n"
"v_mul_f32 v226, v226, v250\n"
"v_mul_f32 v227, v227, v250\n"
"v_accvgpr_write_b32 a16, v224\n"
"v_accvgpr_write_b32 a17, v225\n"
"v_accvgpr_write_b32 a18, v226\n"
"v_accvgpr_write_b32 a19, v227\n"
"v_accvgpr_read_b32 v224, a24\n"
"v_accvgpr_read_b32 v225, a25\n"
"v_accvgpr_read_b32 v226, a26\n"
"v_accvgpr_read_b32 v227, a27\n"
"v_mul_f32 v224, v224, v250\n"
"v_mul_f32 v225, v225, v250\n"
"v_mul_f32 v226, v226, v250\n"
"v_mul_f32 v227, v227, v250\n"
"v_accvgpr_write_b32 a24, v224\n"
"v_accvgpr_write_b32 a25, v225\n"
"v_accvgpr_write_b32 a26, v226\n"
"v_accvgpr_write_b32 a27, v227\n"
"v_accvgpr_read_b32 v224, a32\n"
"v_accvgpr_read_b32 v225, a33\n"
"v_accvgpr_read_b32 v226, a34\n"
"v_accvgpr_read_b32 v227, a35\n"
"v_mul_f32 v224, v224, v250\n"
"v_mul_f32 v225, v225, v250\n"
"v_mul_f32 v226, v226, v250\n"
"v_mul_f32 v227, v227, v250\n"
"v_accvgpr_write_b32 a32, v224\n"
"v_accvgpr_write_b32 a33, v225\n"
"v_accvgpr_write_b32 a34, v226\n"
"v_accvgpr_write_b32 a35, v227\n"
"v_accvgpr_read_b32 v224, a40\n"
"v_accvgpr_read_b32 v225, a41\n"
"v_accvgpr_read_b32 v226, a42\n"
"v_accvgpr_read_b32 v227, a43\n"
"v_mul_f32 v224, v224, v250\n"
"v_mul_f32 v225, v225, v250\n"
"v_mul_f32 v226, v226, v250\n"
"v_mul_f32 v227, v227, v250\n"
"v_accvgpr_write_b32 a40, v224\n"
"v_accvgpr_write_b32 a41, v225\n"
"v_accvgpr_write_b32 a42, v226\n"
"v_accvgpr_write_b32 a43, v227\n"
"v_accvgpr_read_b32 v224, a48\n"
"v_accvgpr_read_b32 v225, a49\n"
"v_accvgpr_read_b32 v226, a50\n"
"v_accvgpr_read_b32 v227, a51\n"
"v_mul_f32 v224, v224, v250\n"
"v_mul_f32 v225, v225, v250\n"
"v_mul_f32 v226, v226, v250\n"
"v_mul_f32 v227, v227, v250\n"
"v_accvgpr_write_b32 a48, v224\n"
"v_accvgpr_write_b32 a49, v225\n"
"v_accvgpr_write_b32 a50, v226\n"
"v_accvgpr_write_b32 a51, v227\n"
"v_accvgpr_read_b32 v224, a56\n"
"v_accvgpr_read_b32 v225, a57\n"
"v_accvgpr_read_b32 v226, a58\n"
"v_accvgpr_read_b32 v227, a59\n"
"v_mul_f32 v224, v224, v250\n"
"v_mul_f32 v225, v225, v250\n"
"v_mul_f32 v226, v226, v250\n"
"v_mul_f32 v227, v227, v250\n"
"v_accvgpr_write_b32 a56, v224\n"
"v_accvgpr_write_b32 a57, v225\n"
"v_accvgpr_write_b32 a58, v226\n"
"v_accvgpr_write_b32 a59, v227\n"
"v_max3_f32 v249, v132, v133, v134\n"
"v_max3_f32 v249, v249, v135, v140\n"
"v_max3_f32 v249, v249, v141, v142\n"
"v_max3_f32 v249, v249, v143, v148\n"
"v_max3_f32 v249, v249, v149, v150\n"
"v_max3_f32 v249, v249, v151, v156\n"
"v_max3_f32 v249, v249, v157, v158\n"
"v_max_f32 v249, v249, v159\n"
"v_mov_b32 v255, v249\n"
"s_nop 1\n"
"v_permlane32_swap_b32 v249, v255\n"
"v_max_f32 v249, v249, v255\n"
"v_mov_b32 v255, v249\n"
"s_nop 1\n"
"v_permlane16_swap_b32 v249, v255\n"
"v_max_f32 v249, v249, v255\n"
"v_max_f32 v249, 0, v249\n"
"v_add_f32 v239, v239, v249\n"
"v_sub_f32 v251, 0, v249\n"
"v_exp_f32 v251, v251\n"
"v_sub_f32 v132, v132, v249\n"
"v_sub_f32 v133, v133, v249\n"
"v_sub_f32 v134, v134, v249\n"
"v_sub_f32 v135, v135, v249\n"
"v_sub_f32 v140, v140, v249\n"
"v_sub_f32 v141, v141, v249\n"
"v_sub_f32 v142, v142, v249\n"
"v_sub_f32 v143, v143, v249\n"
"v_sub_f32 v148, v148, v249\n"
"v_sub_f32 v149, v149, v249\n"
"v_sub_f32 v150, v150, v249\n"
"v_sub_f32 v151, v151, v249\n"
"v_sub_f32 v156, v156, v249\n"
"v_sub_f32 v157, v157, v249\n"
"v_sub_f32 v158, v158, v249\n"
"v_sub_f32 v159, v159, v249\n"
"v_mul_f32 v235, v235, v251\n"
"v_accvgpr_read_b32 v224, a4\n"
"v_accvgpr_read_b32 v225, a5\n"
"v_accvgpr_read_b32 v226, a6\n"
"v_accvgpr_read_b32 v227, a7\n"
"v_mul_f32 v224, v224, v251\n"
"v_mul_f32 v225, v225, v251\n"
"v_mul_f32 v226, v226, v251\n"
"v_mul_f32 v227, v227, v251\n"
"v_accvgpr_write_b32 a4, v224\n"
"v_accvgpr_write_b32 a5, v225\n"
"v_accvgpr_write_b32 a6, v226\n"
"v_accvgpr_write_b32 a7, v227\n"
"v_accvgpr_read_b32 v224, a12\n"
"v_accvgpr_read_b32 v225, a13\n"
"v_accvgpr_read_b32 v226, a14\n"
"v_accvgpr_read_b32 v227, a15\n"
"v_mul_f32 v224, v224, v251\n"
"v_mul_f32 v225, v225, v251\n"
"v_mul_f32 v226, v226, v251\n"
"v_mul_f32 v227, v227, v251\n"
"v_accvgpr_write_b32 a12, v224\n"
"v_accvgpr_write_b32 a13, v225\n"
"v_accvgpr_write_b32 a14, v226\n"
"v_accvgpr_write_b32 a15, v227\n"
"v_accvgpr_read_b32 v224, a20\n"
"v_accvgpr_read_b32 v225, a21\n"
"v_accvgpr_read_b32 v226, a22\n"
"v_accvgpr_read_b32 v227, a23\n"
"v_mul_f32 v224, v224, v251\n"
"v_mul_f32 v225, v225, v251\n"
"v_mul_f32 v226, v226, v251\n"
"v_mul_f32 v227, v227, v251\n"
"v_accvgpr_write_b32 a20, v224\n"
"v_accvgpr_write_b32 a21, v225\n"
"v_accvgpr_write_b32 a22, v226\n"
"v_accvgpr_write_b32 a23, v227\n"
"v_accvgpr_read_b32 v224, a28\n"
"v_accvgpr_read_b32 v225, a29\n"
"v_accvgpr_read_b32 v226, a30\n"
"v_accvgpr_read_b32 v227, a31\n"
"v_mul_f32 v224, v224, v251\n"
"v_mul_f32 v225, v225, v251\n"
"v_mul_f32 v226, v226, v251\n"
"v_mul_f32 v227, v227, v251\n"
"v_accvgpr_write_b32 a28, v224\n"
"v_accvgpr_write_b32 a29, v225\n"
"v_accvgpr_write_b32 a30, v226\n"
"v_accvgpr_write_b32 a31, v227\n"
"v_accvgpr_read_b32 v224, a36\n"
"v_accvgpr_read_b32 v225, a37\n"
"v_accvgpr_read_b32 v226, a38\n"
"v_accvgpr_read_b32 v227, a39\n"
"v_mul_f32 v224, v224, v251\n"
"v_mul_f32 v225, v225, v251\n"
"v_mul_f32 v226, v226, v251\n"
"v_mul_f32 v227, v227, v251\n"
"v_accvgpr_write_b32 a36, v224\n"
"v_accvgpr_write_b32 a37, v225\n"
"v_accvgpr_write_b32 a38, v226\n"
"v_accvgpr_write_b32 a39, v227\n"
"v_accvgpr_read_b32 v224, a44\n"
"v_accvgpr_read_b32 v225, a45\n"
"v_accvgpr_read_b32 v226, a46\n"
"v_accvgpr_read_b32 v227, a47\n"
"v_mul_f32 v224, v224, v251\n"
"v_mul_f32 v225, v225, v251\n"
"v_mul_f32 v226, v226, v251\n"
"v_mul_f32 v227, v227, v251\n"
"v_accvgpr_write_b32 a44, v224\n"
"v_accvgpr_write_b32 a45, v225\n"
"v_accvgpr_write_b32 a46, v226\n"
"v_accvgpr_write_b32 a47, v227\n"
"v_accvgpr_read_b32 v224, a52\n"
"v_accvgpr_read_b32 v225, a53\n"
"v_accvgpr_read_b32 v226, a54\n"
"v_accvgpr_read_b32 v227, a55\n"
"v_mul_f32 v224, v224, v251\n"
"v_mul_f32 v225, v225, v251\n"
"v_mul_f32 v226, v226, v251\n"
"v_mul_f32 v227, v227, v251\n"
"v_accvgpr_write_b32 a52, v224\n"
"v_accvgpr_write_b32 a53, v225\n"
"v_accvgpr_write_b32 a54, v226\n"
"v_accvgpr_write_b32 a55, v227\n"
"v_accvgpr_read_b32 v224, a60\n"
"v_accvgpr_read_b32 v225, a61\n"
"v_accvgpr_read_b32 v226, a62\n"
"v_accvgpr_read_b32 v227, a63\n"
"v_mul_f32 v224, v224, v251\n"
"v_mul_f32 v225, v225, v251\n"
"v_mul_f32 v226, v226, v251\n"
"v_mul_f32 v227, v227, v251\n"
"v_accvgpr_write_b32 a60, v224\n"
"v_accvgpr_write_b32 a61, v225\n"
"v_accvgpr_write_b32 a62, v226\n"
"v_accvgpr_write_b32 a63, v227\n"
"s_cmp_lg_u32 s35, 0\n"
"s_cbranch_scc1 9f\n"
"v_sub_f32 v160, v160, v248\n"
"v_sub_f32 v161, v161, v248\n"
"v_sub_f32 v162, v162, v248\n"
"v_sub_f32 v163, v163, v248\n"
"v_sub_f32 v168, v168, v248\n"
"v_sub_f32 v169, v169, v248\n"
"v_sub_f32 v170, v170, v248\n"
"v_sub_f32 v171, v171, v248\n"
"v_sub_f32 v176, v176, v248\n"
"v_sub_f32 v177, v177, v248\n"
"v_sub_f32 v178, v178, v248\n"
"v_sub_f32 v179, v179, v248\n"
"v_sub_f32 v184, v184, v248\n"
"v_sub_f32 v185, v185, v248\n"
"v_sub_f32 v186, v186, v248\n"
"v_sub_f32 v187, v187, v248\n"
"v_sub_f32 v216, 0, v238\n"
"v_sub_f32 v217, 0, v238\n"
"v_sub_f32 v218, 0, v238\n"
"v_sub_f32 v219, 0, v238\n"
"v_sub_f32 v164, v164, v249\n"
"v_sub_f32 v165, v165, v249\n"
"v_sub_f32 v166, v166, v249\n"
"v_sub_f32 v167, v167, v249\n"
"v_sub_f32 v172, v172, v249\n"
"v_sub_f32 v173, v173, v249\n"
"v_sub_f32 v174, v174, v249\n"
"v_sub_f32 v175, v175, v249\n"
"v_sub_f32 v180, v180, v249\n"
"v_sub_f32 v181, v181, v249\n"
"v_sub_f32 v182, v182, v249\n"
"v_sub_f32 v183, v183, v249\n"
"v_sub_f32 v188, v188, v249\n"
"v_sub_f32 v189, v189, v249\n"
"v_sub_f32 v190, v190, v249\n"
"v_sub_f32 v191, v191, v249\n"
"v_sub_f32 v220, 0, v239\n"
"v_sub_f32 v221, 0, v239\n"
"v_sub_f32 v222, 0, v239\n"
"v_sub_f32 v223, 0, v239\n"
"9:\n"
"v_exp_f32 v224, v128\n"
"v_exp_f32 v225, v129\n"
"s_nop 0\n"
"v_add_f32 v232, v234, v224\n"
"v_add_f32 v232, v232, v225\n"
"v_cvt_pk_bf16_f32 v192, v224, v225\n"
"v_exp_f32 v226, v130\n"
"v_exp_f32 v227, v131\n"
"s_nop 0\n"
"v_add_f32 v232, v232, v226\n"
"v_add_f32 v232, v232, v227\n"
"v_cvt_pk_bf16_f32 v193, v226, v227\n"
"v_exp_f32 v228, v132\n"
"v_exp_f32 v229, v133\n"
"s_nop 0\n"
"v_add_f32 v233, v235, v228\n"
"v_add_f32 v233, v233, v229\n"
"v_cvt_pk_bf16_f32 v196, v228, v229\n"
"v_exp_f32 v230, v134\n"
"v_exp_f32 v231, v135\n"
"s_nop 0\n"
"v_add_f32 v233, v233, v230\n"
"v_add_f32 v233, v233, v231\n"
"v_cvt_pk_bf16_f32 v197, v230, v231\n"
"v_exp_f32 v224, v136\n"
"v_exp_f32 v225, v137\n"
"s_nop 0\n"
"v_add_f32 v232, v232, v224\n"
"v_add_f32 v232, v232, v225\n"
"v_cvt_pk_bf16_f32 v194, v224, v225\n"
"v_exp_f32 v226, v138\n"
"v_exp_f32 v227, v139\n"
"s_nop 0\n"
"v_add_f32 v232, v232, v226\n"
"v_add_f32 v232, v232, v227\n"
"v_cvt_pk_bf16_f32 v195, v226, v227\n"
"v_exp_f32 v228, v140\n"
"v_exp_f32 v229, v141\n"
"s_nop 0\n"
"v_add_f32 v233, v233, v228\n"
"v_add_f32 v233, v233, v229\n"
"v_cvt_pk_bf16_f32 v198, v228, v229\n"
"v_exp_f32 v230, v142\n"
"v_exp_f32 v231, v143\n"
"s_nop 0\n"
"v_add_f32 v233, v233, v230\n"
"v_add_f32 v233, v233, v231\n"
"v_cvt_pk_bf16_f32 v199, v230, v231\n"
"v_exp_f32 v224, v144\n"
"v_exp_f32 v225, v145\n"
"s_nop 0\n"
"v_add_f32 v232, v232, v224\n"
"v_add_f32 v232, v232, v225\n"
"v_cvt_pk_bf16_f32 v208, v224, v225\n"
"v_exp_f32 v226, v146\n"
"v_exp_f32 v227, v147\n"
"s_nop 0\n"
"v_add_f32 v232, v232, v226\n"
"v_add_f32 v232, v232, v227\n"
"v_cvt_pk_bf16_f32 v209, v226, v227\n"
"v_exp_f32 v228, v148\n"
"v_exp_f32 v229, v149\n"
"s_nop 0\n"
"v_add_f32 v233, v233, v228\n"
"v_add_f32 v233, v233, v229\n"
"v_cvt_pk_bf16_f32 v212, v228, v229\n"
"v_exp_f32 v230, v150\n"
"v_exp_f32 v231, v151\n"
"s_nop 0\n"
"v_add_f32 v233, v233, v230\n"
"v_add_f32 v233, v233, v231\n"
"v_cvt_pk_bf16_f32 v213, v230, v231\n"
"v_exp_f32 v224, v152\n"
"v_exp_f32 v225, v153\n"
"s_nop 0\n"
"v_add_f32 v232, v232, v224\n"
"v_add_f32 v232, v232, v225\n"
"v_cvt_pk_bf16_f32 v210, v224, v225\n"
"v_exp_f32 v226, v154\n"
"v_exp_f32 v227, v155\n"
"s_nop 0\n"
"v_add_f32 v232, v232, v226\n"
"v_add_f32 v232, v232, v227\n"
"v_cvt_pk_bf16_f32 v211, v226, v227\n"
"v_exp_f32 v228, v156\n"
"v_exp_f32 v229, v157\n"
"s_nop 0\n"
"v_add_f32 v233, v233, v228\n"
"v_add_f32 v233, v233, v229\n"
"v_cvt_pk_bf16_f32 v214, v228, v229\n"
"v_exp_f32 v230, v158\n"
"v_exp_f32 v231, v159\n"
"s_nop 0\n"
"v_add_f32 v233, v233, v230\n"
"v_add_f32 v233, v233, v231\n"
"v_cvt_pk_bf16_f32 v215, v230, v231\n"
"s_nop 1\n"
"s_setpc_b64 s[36:37]\n"
"61:\n"
"s_nop 7\n"
"v_max3_f32 v248, v160, v161, v162\n"
"v_max3_f32 v248, v248, v163, v168\n"
"v_max3_f32 v248, v248, v169, v170\n"
"v_max3_f32 v248, v248, v171, v176\n"
"v_max3_f32 v248, v248, v177, v178\n"
"v_max3_f32 v248, v248, v179, v184\n"
"v_max3_f32 v248, v248, v185, v186\n"
"v_max_f32 v248, v248, v187\n"
"v_mov_b32 v255, v248\n"
"s_nop 1\n"
"v_permlane32_swap_b32 v248, v255\n"
"v_max_f32 v248, v248, v255\n"
"v_mov_b32 v255, v248\n"
"s_nop 1\n"
"v_permlane16_swap_b32 v248, v255\n"
"v_max_f32 v248, v248, v255\n"
"v_max_f32 v248, 0, v248\n"
"v_add_f32 v238, v238, v248\n"
"v_sub_f32 v250, 0, v248\n"
"v_exp_f32 v250, v250\n"
"v_sub_f32 v160, v160, v248\n"
"v_sub_f32 v161, v161, v248\n"
"v_sub_f32 v162, v162, v248\n"
"v_sub_f32 v163, v163, v248\n"
"v_sub_f32 v168, v168, v248\n"
"v_sub_f32 v169, v169, v248\n"
"v_sub_f32 v170, v170, v248\n"
"v_sub_f32 v171, v171, v248\n"
"v_sub_f32 v176, v176, v248\n"
"v_sub_f32 v177, v177, v248\n"
"v_sub_f32 v178, v178, v248\n"
"v_sub_f32 v179, v179, v248\n"
"v_sub_f32 v184, v184, v248\n"
"v_sub_f32 v185, v185, v248\n"
"v_sub_f32 v186, v186, v248\n"
"v_sub_f32 v187, v187, v248\n"
"v_mul_f32 v232, v232, v250\n"
"v_accvgpr_read_b32 v224, a0\n"
"v_accvgpr_read_b32 v225, a1\n"
"v_accvgpr_read_b32 v226, a2\n"
"v_accvgpr_read_b32 v227, a3\n"
"v_mul_f32 v224, v224, v250\n"
"v_mul_f32 v225, v225, v250\n"
"v_mul_f32 v226, v226, v250\n"
"v_mul_f32 v227, v227, v250\n"
"v_accvgpr_write_b32 a0, v224\n"
"v_accvgpr_write_b32 a1, v225\n"
"v_accvgpr_write_b32 a2, v226\n"
"v_accvgpr_write_b32 a3, v227\n"
"v_accvgpr_read_b32 v224, a8\n"
"v_accvgpr_read_b32 v225, a9\n"
"v_accvgpr_read_b32 v226, a10\n"
"v_accvgpr_read_b32 v227, a11\n"
"v_mul_f32 v224, v224, v250\n"
"v_mul_f32 v225, v225, v250\n"
"v_mul_f32 v226, v226, v250\n"
"v_mul_f32 v227, v227, v250\n"
"v_accvgpr_write_b32 a8, v224\n"
"v_accvgpr_write_b32 a9, v225\n"
"v_accvgpr_write_b32 a10, v226\n"
"v_accvgpr_write_b32 a11, v227\n"
"v_accvgpr_read_b32 v224, a16\n"
"v_accvgpr_read_b32 v225, a17\n"
"v_accvgpr_read_b32 v226, a18\n"
"v_accvgpr_read_b32 v227, a19\n"
"v_mul_f32 v224, v224, v250\n"
"v_mul_f32 v225, v225, v250\n"
"v_mul_f32 v226, v226, v250\n"
"v_mul_f32 v227, v227, v250\n"
"v_accvgpr_write_b32 a16, v224\n"
"v_accvgpr_write_b32 a17, v225\n"
"v_accvgpr_write_b32 a18, v226\n"
"v_accvgpr_write_b32 a19, v227\n"
"v_accvgpr_read_b32 v224, a24\n"
"v_accvgpr_read_b32 v225, a25\n"
"v_accvgpr_read_b32 v226, a26\n"
"v_accvgpr_read_b32 v227, a27\n"
"v_mul_f32 v224, v224, v250\n"
"v_mul_f32 v225, v225, v250\n"
"v_mul_f32 v226, v226, v250\n"
"v_mul_f32 v227, v227, v250\n"
"v_accvgpr_write_b32 a24, v224\n"
"v_accvgpr_write_b32 a25, v225\n"
"v_accvgpr_write_b32 a26, v226\n"
"v_accvgpr_write_b32 a27, v227\n"
"v_accvgpr_read_b32 v224, a32\n"
"v_accvgpr_read_b32 v225, a33\n"
"v_accvgpr_read_b32 v226, a34\n"
"v_accvgpr_read_b32 v227, a35\n"
"v_mul_f32 v224, v224, v250\n"
"v_mul_f32 v225, v225, v250\n"
"v_mul_f32 v226, v226, v250\n"
"v_mul_f32 v227, v227, v250\n"
"v_accvgpr_write_b32 a32, v224\n"
"v_accvgpr_write_b32 a33, v225\n"
"v_accvgpr_write_b32 a34, v226\n"
"v_accvgpr_write_b32 a35, v227\n"
"v_accvgpr_read_b32 v224, a40\n"
"v_accvgpr_read_b32 v225, a41\n"
"v_accvgpr_read_b32 v226, a42\n"
"v_accvgpr_read_b32 v227, a43\n"
"v_mul_f32 v224, v224, v250\n"
"v_mul_f32 v225, v225, v250\n"
"v_mul_f32 v226, v226, v250\n"
"v_mul_f32 v227, v227, v250\n"
"v_accvgpr_write_b32 a40, v224\n"
"v_accvgpr_write_b32 a41, v225\n"
"v_accvgpr_write_b32 a42, v226\n"
"v_accvgpr_write_b32 a43, v227\n"
"v_accvgpr_read_b32 v224, a48\n"
"v_accvgpr_read_b32 v225, a49\n"
"v_accvgpr_read_b32 v226, a50\n"
"v_accvgpr_read_b32 v227, a51\n"
"v_mul_f32 v224, v224, v250\n"
"v_mul_f32 v225, v225, v250\n"
"v_mul_f32 v226, v226, v250\n"
"v_mul_f32 v227, v227, v250\n"
"v_accvgpr_write_b32 a48, v224\n"
"v_accvgpr_write_b32 a49, v225\n"
"v_accvgpr_write_b32 a50, v226\n"
"v_accvgpr_write_b32 a51, v227\n"
"v_accvgpr_read_b32 v224, a56\n"
"v_accvgpr_read_b32 v225, a57\n"
"v_accvgpr_read_b32 v226, a58\n"
"v_accvgpr_read_b32 v227, a59\n"
"v_mul_f32 v224, v224, v250\n"
"v_mul_f32 v225, v225, v250\n"
"v_mul_f32 v226, v226, v250\n"
"v_mul_f32 v227, v227, v250\n"
"v_accvgpr_write_b32 a56, v224\n"
"v_accvgpr_write_b32 a57, v225\n"
"v_accvgpr_write_b32 a58, v226\n"
"v_accvgpr_write_b32 a59, v227\n"
"v_max3_f32 v249, v164, v165, v166\n"
"v_max3_f32 v249, v249, v167, v172\n"
"v_max3_f32 v249, v249, v173, v174\n"
"v_max3_f32 v249, v249, v175, v180\n"
"v_max3_f32 v249, v249, v181, v182\n"
"v_max3_f32 v249, v249, v183, v188\n"
"v_max3_f32 v249, v249, v189, v190\n"
"v_max_f32 v249, v249, v191\n"
"v_mov_b32 v255, v249\n"
"s_nop 1\n"
"v_permlane32_swap_b32 v249, v255\n"
"v_max_f32 v249, v249, v255\n"
"v_mov_b32 v255, v249\n"
"s_nop 1\n"
"v_permlane16_swap_b32 v249, v255\n"
"v_max_f32 v249, v249, v255\n"
"v_max_f32 v249, 0, v249\n"
"v_add_f32 v239, v239, v249\n"
"v_sub_f32 v251, 0, v249\n"
"v_exp_f32 v251, v251\n"
"v_sub_f32 v164, v164, v249\n"
"v_sub_f32 v165, v165, v249\n"
"v_sub_f32 v166, v166, v249\n"
"v_sub_f32 v167, v167, v249\n"
"v_sub_f32 v172, v172, v249\n"
"v_sub_f32 v173, v173, v249\n"
"v_sub_f32 v174, v174, v249\n"
"v_sub_f32 v175, v175, v249\n"
"v_sub_f32 v180, v180, v249\n"
"v_sub_f32 v181, v181, v249\n"
"v_sub_f32 v182, v182, v249\n"
"v_sub_f32 v183, v183, v249\n"
"v_sub_f32 v188, v188, v249\n"
"v_sub_f32 v189, v189, v249\n"
"v_sub_f32 v190, v190, v249\n"
"v_sub_f32 v191, v191, v249\n"
"v_mul_f32 v233, v233, v251\n"
"v_accvgpr_read_b32 v224, a4\n"
"v_accvgpr_read_b32 v225, a5\n"
"v_accvgpr_read_b32 v226, a6\n"
"v_accvgpr_read_b32 v227, a7\n"
"v_mul_f32 v224, v224, v251\n"
"v_mul_f32 v225, v225, v251\n"
"v_mul_f32 v226, v226, v251\n"
"v_mul_f32 v227, v227, v251\n"
"v_accvgpr_write_b32 a4, v224\n"
"v_accvgpr_write_b32 a5, v225\n"
"v_accvgpr_write_b32 a6, v226\n"
"v_accvgpr_write_b32 a7, v227\n"
"v_accvgpr_read_b32 v224, a12\n"
"v_accvgpr_read_b32 v225, a13\n"
"v_accvgpr_read_b32 v226, a14\n"
"v_accvgpr_read_b32 v227, a15\n"
"v_mul_f32 v224, v224, v251\n"
"v_mul_f32 v225, v225, v251\n"
"v_mul_f32 v226, v226, v251\n"
"v_mul_f32 v227, v227, v251\n"
"v_accvgpr_write_b32 a12, v224\n"
"v_accvgpr_write_b32 a13, v225\n"
"v_accvgpr_write_b32 a14, v226\n"
"v_accvgpr_write_b32 a15, v227\n"
"v_accvgpr_read_b32 v224, a20\n"
"v_accvgpr_read_b32 v225, a21\n"
"v_accvgpr_read_b32 v226, a22\n"
"v_accvgpr_read_b32 v227, a23\n"
"v_mul_f32 v224, v224, v251\n"
"v_mul_f32 v225, v225, v251\n"
"v_mul_f32 v226, v226, v251\n"
"v_mul_f32 v227, v227, v251\n"
"v_accvgpr_write_b32 a20, v224\n"
"v_accvgpr_write_b32 a21, v225\n"
"v_accvgpr_write_b32 a22, v226\n"
"v_accvgpr_write_b32 a23, v227\n"
"v_accvgpr_read_b32 v224, a28\n"
"v_accvgpr_read_b32 v225, a29\n"
"v_accvgpr_read_b32 v226, a30\n"
"v_accvgpr_read_b32 v227, a31\n"
"v_mul_f32 v224, v224, v251\n"
"v_mul_f32 v225, v225, v251\n"
"v_mul_f32 v226, v226, v251\n"
"v_mul_f32 v227, v227, v251\n"
"v_accvgpr_write_b32 a28, v224\n"
"v_accvgpr_write_b32 a29, v225\n"
"v_accvgpr_write_b32 a30, v226\n"
"v_accvgpr_write_b32 a31, v227\n"
"v_accvgpr_read_b32 v224, a36\n"
"v_accvgpr_read_b32 v225, a37\n"
"v_accvgpr_read_b32 v226, a38\n"
"v_accvgpr_read_b32 v227, a39\n"
"v_mul_f32 v224, v224, v251\n"
"v_mul_f32 v225, v225, v251\n"
"v_mul_f32 v226, v226, v251\n"
"v_mul_f32 v227, v227, v251\n"
"v_accvgpr_write_b32 a36, v224\n"
"v_accvgpr_write_b32 a37, v225\n"
"v_accvgpr_write_b32 a38, v226\n"
"v_accvgpr_write_b32 a39, v227\n"
"v_accvgpr_read_b32 v224, a44\n"
"v_accvgpr_read_b32 v225, a45\n"
"v_accvgpr_read_b32 v226, a46\n"
"v_accvgpr_read_b32 v227, a47\n"
"v_mul_f32 v224, v224, v251\n"
"v_mul_f32 v225, v225, v251\n"
"v_mul_f32 v226, v226, v251\n"
"v_mul_f32 v227, v227, v251\n"
"v_accvgpr_write_b32 a44, v224\n"
"v_accvgpr_write_b32 a45, v225\n"
"v_accvgpr_write_b32 a46, v226\n"
"v_accvgpr_write_b32 a47, v227\n"
"v_accvgpr_read_b32 v224, a52\n"
"v_accvgpr_read_b32 v225, a53\n"
"v_accvgpr_read_b32 v226, a54\n"
"v_accvgpr_read_b32 v227, a55\n"
"v_mul_f32 v224, v224, v251\n"
"v_mul_f32 v225, v225, v251\n"
"v_mul_f32 v226, v226, v251\n"
"v_mul_f32 v227, v227, v251\n"
"v_accvgpr_write_b32 a52, v224\n"
"v_accvgpr_write_b32 a53, v225\n"
"v_accvgpr_write_b32 a54, v226\n"
"v_accvgpr_write_b32 a55, v227\n"
"v_accvgpr_read_b32 v224, a60\n"
"v_accvgpr_read_b32 v225, a61\n"
"v_accvgpr_read_b32 v226, a62\n"
"v_accvgpr_read_b32 v227, a63\n"
"v_mul_f32 v224, v224, v251\n"
"v_mul_f32 v225, v225, v251\n"
"v_mul_f32 v226, v226, v251\n"
"v_mul_f32 v227, v227, v251\n"
"v_accvgpr_write_b32 a60, v224\n"
"v_accvgpr_write_b32 a61, v225\n"
"v_accvgpr_write_b32 a62, v226\n"
"v_accvgpr_write_b32 a63, v227\n"
"s_cmp_lg_u32 s35, 0\n"
"s_cbranch_scc1 9f\n"
"v_sub_f32 v128, v128, v248\n"
"v_sub_f32 v129, v129, v248\n"
"v_sub_f32 v130, v130, v248\n"
"v_sub_f32 v131, v131, v248\n"
"v_sub_f32 v136, v136, v248\n"
"v_sub_f32 v137, v137, v248\n"
"v_sub_f32 v138, v138, v248\n"
"v_sub_f32 v139, v139, v248\n"
"v_sub_f32 v144, v144, v248\n"
"v_sub_f32 v145, v145, v248\n"
"v_sub_f32 v146, v146, v248\n"
"v_sub_f32 v147, v147, v248\n"
"v_sub_f32 v152, v152, v248\n"
"v_sub_f32 v153, v153, v248\n"
"v_sub_f32 v154, v154, v248\n"
"v_sub_f32 v155, v155, v248\n"
"v_sub_f32 v216, 0, v238\n"
"v_sub_f32 v217, 0, v238\n"
"v_sub_f32 v218, 0, v238\n"
"v_sub_f32 v219, 0, v238\n"
"v_sub_f32 v132, v132, v249\n"
"v_sub_f32 v133, v133, v249\n"
"v_sub_f32 v134, v134, v249\n"
"v_sub_f32 v135, v135, v249\n"
"v_sub_f32 v140, v140, v249\n"
"v_sub_f32 v141, v141, v249\n"
"v_sub_f32 v142, v142, v249\n"
"v_sub_f32 v143, v143, v249\n"
"v_sub_f32 v148, v148, v249\n"
"v_sub_f32 v149, v149, v249\n"
"v_sub_f32 v150, v150, v249\n"
"v_sub_f32 v151, v151, v249\n"
"v_sub_f32 v156, v156, v249\n"
"v_sub_f32 v157, v157, v249\n"
"v_sub_f32 v158, v158, v249\n"
"v_sub_f32 v159, v159, v249\n"
"v_sub_f32 v220, 0, v239\n"
"v_sub_f32 v221, 0, v239\n"
"v_sub_f32 v222, 0, v239\n"
"v_sub_f32 v223, 0, v239\n"
"9:\n"
"v_exp_f32 v224, v160\n"
"v_exp_f32 v225, v161\n"
"s_nop 0\n"
"v_add_f32 v234, v232, v224\n"
"v_add_f32 v234, v234, v225\n"
"v_cvt_pk_bf16_f32 v200, v224, v225\n"
"v_exp_f32 v226, v162\n"
"v_exp_f32 v227, v163\n"
"s_nop 0\n"
"v_add_f32 v234, v234, v226\n"
"v_add_f32 v234, v234, v227\n"
"v_cvt_pk_bf16_f32 v201, v226, v227\n"
"v_exp_f32 v228, v164\n"
"v_exp_f32 v229, v165\n"
"s_nop 0\n"
"v_add_f32 v235, v233, v228\n"
"v_add_f32 v235, v235, v229\n"
"v_cvt_pk_bf16_f32 v204, v228, v229\n"
"v_exp_f32 v230, v166\n"
"v_exp_f32 v231, v167\n"
"s_nop 0\n"
"v_add_f32 v235, v235, v230\n"
"v_add_f32 v235, v235, v231\n"
"v_cvt_pk_bf16_f32 v205, v230, v231\n"
"v_exp_f32 v224, v168\n"
"v_exp_f32 v225, v169\n"
"s_nop 0\n"
"v_add_f32 v234, v234, v224\n"
"v_add_f32 v234, v234, v225\n"
"v_cvt_pk_bf16_f32 v202, v224, v225\n"
"v_exp_f32 v226, v170\n"
"v_exp_f32 v227, v171\n"
"s_nop 0\n"
"v_add_f32 v234, v234, v226\n"
"v_add_f32 v234, v234, v227\n"
"v_cvt_pk_bf16_f32 v203, v226, v227\n"
"v_exp_f32 v228, v172\n"
"v_exp_f32 v229, v173\n"
"s_nop 0\n"
"v_add_f32 v235, v235, v228\n"
"v_add_f32 v235, v235, v229\n"
"v_cvt_pk_bf16_f32 v206, v228, v229\n"
"v_exp_f32 v230, v174\n"
"v_exp_f32 v231, v175\n"
"s_nop 0\n"
"v_add_f32 v235, v235, v230\n"
"v_add_f32 v235, v235, v231\n"
"v_cvt_pk_bf16_f32 v207, v230, v231\n"
"v_exp_f32 v224, v176\n"
"v_exp_f32 v225, v177\n"
"s_nop 0\n"
"v_add_f32 v234, v234, v224\n"
"v_add_f32 v234, v234, v225\n"
"v_cvt_pk_bf16_f32 v208, v224, v225\n"
"v_exp_f32 v226, v178\n"
"v_exp_f32 v227, v179\n"
"s_nop 0\n"
"v_add_f32 v234, v234, v226\n"
"v_add_f32 v234, v234, v227\n"
"v_cvt_pk_bf16_f32 v209, v226, v227\n"
"v_exp_f32 v228, v180\n"
"v_exp_f32 v229, v181\n"
"s_nop 0\n"
"v_add_f32 v235, v235, v228\n"
"v_add_f32 v235, v235, v229\n"
"v_cvt_pk_bf16_f32 v212, v228, v229\n"
"v_exp_f32 v230, v182\n"
"v_exp_f32 v231, v183\n"
"s_nop 0\n"
"v_add_f32 v235, v235, v230\n"
"v_add_f32 v235, v235, v231\n"
"v_cvt_pk_bf16_f32 v213, v230, v231\n"
"v_exp_f32 v224, v184\n"
"v_exp_f32 v225, v185\n"
"s_nop 0\n"
"v_add_f32 v234, v234, v224\n"
"v_add_f32 v234, v234, v225\n"
"v_cvt_pk_bf16_f32 v210, v224, v225\n"
"v_exp_f32 v226, v186\n"
"v_exp_f32 v227, v187\n"
"s_nop 0\n"
"v_add_f32 v234, v234, v226\n"
"v_add_f32 v234, v234, v227\n"
"v_cvt_pk_bf16_f32 v211, v226, v227\n"
"v_exp_f32 v228, v188\n"
"v_exp_f32 v229, v189\n"
"s_nop 0\n"
"v_add_f32 v235, v235, v228\n"
"v_add_f32 v235, v235, v229\n"
"v_cvt_pk_bf16_f32 v214, v228, v229\n"
"v_exp_f32 v230, v190\n"
"v_exp_f32 v231, v191\n"
"s_nop 0\n"
"v_add_f32 v235, v235, v230\n"
"v_add_f32 v235, v235, v231\n"
"v_cvt_pk_bf16_f32 v215, v230, v231\n"
"s_nop 1\n"
"s_setpc_b64 s[36:37]\n"
"7:\n"
"v_mov_b32 v255, v236\n"
"s_nop 1\n"
"v_permlane32_swap_b32 v236, v255\n"
"v_add_f32 v236, v236, v255\n"
"v_mov_b32 v255, v236\n"
"s_nop 1\n"
"v_permlane16_swap_b32 v236, v255\n"
"v_add_f32 v236, v236, v255\n"
"v_rcp_f32 v236, v236\n"
"v_mov_b32 v255, v237\n"
"s_nop 1\n"
"v_permlane32_swap_b32 v237, v255\n"
"v_add_f32 v237, v237, v255\n"
"v_mov_b32 v255, v237\n"
"s_nop 1\n"
"v_permlane16_swap_b32 v237, v255\n"
"v_add_f32 v237, v237, v255\n"
"v_rcp_f32 v237, v237\n"
"v_add_u32 v254, 131072, v245\n"
"v_accvgpr_read_b32 v248, a0\n"
"v_accvgpr_read_b32 v249, a1\n"
"v_accvgpr_read_b32 v250, a2\n"
"v_accvgpr_read_b32 v251, a3\n"
"v_accvgpr_write_b32 a0, 0\n"
"v_accvgpr_write_b32 a1, 0\n"
"v_accvgpr_write_b32 a2, 0\n"
"v_accvgpr_write_b32 a3, 0\n"
"v_mul_f32 v248, v248, v236\n"
"v_mul_f32 v249, v249, v236\n"
"v_mul_f32 v250, v250, v236\n"
"v_mul_f32 v251, v251, v236\n"
"v_cvt_pk_bf16_f32 v224, v248, v249\n"
"v_cvt_pk_bf16_f32 v225, v250, v251\n"
"v_accvgpr_read_b32 v248, a8\n"
"v_accvgpr_read_b32 v249, a9\n"
"v_accvgpr_read_b32 v250, a10\n"
"v_accvgpr_read_b32 v251, a11\n"
"v_accvgpr_write_b32 a8, 0\n"
"v_accvgpr_write_b32 a9, 0\n"
"v_accvgpr_write_b32 a10, 0\n"
"v_accvgpr_write_b32 a11, 0\n"
"v_mul_f32 v248, v248, v236\n"
"v_mul_f32 v249, v249, v236\n"
"v_mul_f32 v250, v250, v236\n"
"v_mul_f32 v251, v251, v236\n"
"v_cvt_pk_bf16_f32 v226, v248, v249\n"
"v_cvt_pk_bf16_f32 v227, v250, v251\n"
"global_store_dwordx4 v245, v[224:227], s[38:39] offset:0 sc1\n"
"v_accvgpr_read_b32 v248, a16\n"
"v_accvgpr_read_b32 v249, a17\n"
"v_accvgpr_read_b32 v250, a18\n"
"v_accvgpr_read_b32 v251, a19\n"
"v_accvgpr_write_b32 a16, 0\n"
"v_accvgpr_write_b32 a17, 0\n"
"v_accvgpr_write_b32 a18, 0\n"
"v_accvgpr_write_b32 a19, 0\n"
"v_mul_f32 v248, v248, v236\n"
"v_mul_f32 v249, v249, v236\n"
"v_mul_f32 v250, v250, v236\n"
"v_mul_f32 v251, v251, v236\n"
"v_cvt_pk_bf16_f32 v228, v248, v249\n"
"v_cvt_pk_bf16_f32 v229, v250, v251\n"
"v_accvgpr_read_b32 v248, a24\n"
"v_accvgpr_read_b32 v249, a25\n"
"v_accvgpr_read_b32 v250, a26\n"
"v_accvgpr_read_b32 v251, a27\n"
"v_accvgpr_write_b32 a24, 0\n"
"v_accvgpr_write_b32 a25, 0\n"
"v_accvgpr_write_b32 a26, 0\n"
"v_accvgpr_write_b32 a27, 0\n"
"v_mul_f32 v248, v248, v236\n"
"v_mul_f32 v249, v249, v236\n"
"v_mul_f32 v250, v250, v236\n"
"v_mul_f32 v251, v251, v236\n"
"v_cvt_pk_bf16_f32 v230, v248, v249\n"
"v_cvt_pk_bf16_f32 v231, v250, v251\n"
"global_store_dwordx4 v245, v[228:231], s[38:39] offset:1024 sc1\n"
"v_accvgpr_read_b32 v248, a32\n"
"v_accvgpr_read_b32 v249, a33\n"
"v_accvgpr_read_b32 v250, a34\n"
"v_accvgpr_read_b32 v251, a35\n"
"v_accvgpr_write_b32 a32, 0\n"
"v_accvgpr_write_b32 a33, 0\n"
"v_accvgpr_write_b32 a34, 0\n"
"v_accvgpr_write_b32 a35, 0\n"
"v_mul_f32 v248, v248, v236\n"
"v_mul_f32 v249, v249, v236\n"
"v_mul_f32 v250, v250, v236\n"
"v_mul_f32 v251, v251, v236\n"
"v_cvt_pk_bf16_f32 v224, v248, v249\n"
"v_cvt_pk_bf16_f32 v225, v250, v251\n"
"v_accvgpr_read_b32 v248, a40\n"
"v_accvgpr_read_b32 v249, a41\n"
"v_accvgpr_read_b32 v250, a42\n"
"v_accvgpr_read_b32 v251, a43\n"
"v_accvgpr_write_b32 a40, 0\n"
"v_accvgpr_write_b32 a41, 0\n"
"v_accvgpr_write_b32 a42, 0\n"
"v_accvgpr_write_b32 a43, 0\n"
"v_mul_f32 v248, v248, v236\n"
"v_mul_f32 v249, v249, v236\n"
"v_mul_f32 v250, v250, v236\n"
"v_mul_f32 v251, v251, v236\n"
"v_cvt_pk_bf16_f32 v226, v248, v249\n"
"v_cvt_pk_bf16_f32 v227, v250, v251\n"
"global_store_dwordx4 v245, v[224:227], s[38:39] offset:2048 sc1\n"
"v_accvgpr_read_b32 v248, a48\n"
"v_accvgpr_read_b32 v249, a49\n"
"v_accvgpr_read_b32 v250, a50\n"
"v_accvgpr_read_b32 v251, a51\n"
"v_accvgpr_write_b32 a48, 0\n"
"v_accvgpr_write_b32 a49, 0\n"
"v_accvgpr_write_b32 a50, 0\n"
"v_accvgpr_write_b32 a51, 0\n"
"v_mul_f32 v248, v248, v236\n"
"v_mul_f32 v249, v249, v236\n"
"v_mul_f32 v250, v250, v236\n"
"v_mul_f32 v251, v251, v236\n"
"v_cvt_pk_bf16_f32 v228, v248, v249\n"
"v_cvt_pk_bf16_f32 v229, v250, v251\n"
"v_accvgpr_read_b32 v248, a56\n"
"v_accvgpr_read_b32 v249, a57\n"
"v_accvgpr_read_b32 v250, a58\n"
"v_accvgpr_read_b32 v251, a59\n"
"v_accvgpr_write_b32 a56, 0\n"
"v_accvgpr_write_b32 a57, 0\n"
"v_accvgpr_write_b32 a58, 0\n"
"v_accvgpr_write_b32 a59, 0\n"
"v_mul_f32 v248, v248, v236\n"
"v_mul_f32 v249, v249, v236\n"
"v_mul_f32 v250, v250, v236\n"
"v_mul_f32 v251, v251, v236\n"
"v_cvt_pk_bf16_f32 v230, v248, v249\n"
"v_cvt_pk_bf16_f32 v231, v250, v251\n"
"global_store_dwordx4 v245, v[228:231], s[38:39] offset:3072 sc1\n"
"v_accvgpr_read_b32 v248, a4\n"
"v_accvgpr_read_b32 v249, a5\n"
"v_accvgpr_read_b32 v250, a6\n"
"v_accvgpr_read_b32 v251, a7\n"
"v_accvgpr_write_b32 a4, 0\n"
"v_accvgpr_write_b32 a5, 0\n"
"v_accvgpr_write_b32 a6, 0\n"
"v_accvgpr_write_b32 a7, 0\n"
"v_mul_f32 v248, v248, v237\n"
"v_mul_f32 v249, v249, v237\n"
"v_mul_f32 v250, v250, v237\n"
"v_mul_f32 v251, v251, v237\n"
"v_cvt_pk_bf16_f32 v224, v248, v249\n"
"v_cvt_pk_bf16_f32 v225, v250, v251\n"
"v_accvgpr_read_b32 v248, a12\n"
"v_accvgpr_read_b32 v249, a13\n"
"v_accvgpr_read_b32 v250, a14\n"
"v_accvgpr_read_b32 v251, a15\n"
"v_accvgpr_write_b32 a12, 0\n"
"v_accvgpr_write_b32 a13, 0\n"
"v_accvgpr_write_b32 a14, 0\n"
"v_accvgpr_write_b32 a15, 0\n"
"v_mul_f32 v248, v248, v237\n"
"v_mul_f32 v249, v249, v237\n"
"v_mul_f32 v250, v250, v237\n"
"v_mul_f32 v251, v251, v237\n"
"v_cvt_pk_bf16_f32 v226, v248, v249\n"
"v_cvt_pk_bf16_f32 v227, v250, v251\n"
"global_store_dwordx4 v254, v[224:227], s[38:39] offset:0 sc1\n"
"v_accvgpr_read_b32 v248, a20\n"
"v_accvgpr_read_b32 v249, a21\n"
"v_accvgpr_read_b32 v250, a22\n"
"v_accvgpr_read_b32 v251, a23\n"
"v_accvgpr_write_b32 a20, 0\n"
"v_accvgpr_write_b32 a21, 0\n"
"v_accvgpr_write_b32 a22, 0\n"
"v_accvgpr_write_b32 a23, 0\n"
"v_mul_f32 v248, v248, v237\n"
"v_mul_f32 v249, v249, v237\n"
"v_mul_f32 v250, v250, v237\n"
"v_mul_f32 v251, v251, v237\n"
"v_cvt_pk_bf16_f32 v228, v248, v249\n"
"v_cvt_pk_bf16_f32 v229, v250, v251\n"
"v_accvgpr_read_b32 v248, a28\n"
"v_accvgpr_read_b32 v249, a29\n"
"v_accvgpr_read_b32 v250, a30\n"
"v_accvgpr_read_b32 v251, a31\n"
"v_accvgpr_write_b32 a28, 0\n"
"v_accvgpr_write_b32 a29, 0\n"
"v_accvgpr_write_b32 a30, 0\n"
"v_accvgpr_write_b32 a31, 0\n"
"v_mul_f32 v248, v248, v237\n"
"v_mul_f32 v249, v249, v237\n"
"v_mul_f32 v250, v250, v237\n"
"v_mul_f32 v251, v251, v237\n"
"v_cvt_pk_bf16_f32 v230, v248, v249\n"
"v_cvt_pk_bf16_f32 v231, v250, v251\n"
"global_store_dwordx4 v254, v[228:231], s[38:39] offset:1024 sc1\n"
"v_accvgpr_read_b32 v248, a36\n"
"v_accvgpr_read_b32 v249, a37\n"
"v_accvgpr_read_b32 v250, a38\n"
"v_accvgpr_read_b32 v251, a39\n"
"v_accvgpr_write_b32 a36, 0\n"
"v_accvgpr_write_b32 a37, 0\n"
"v_accvgpr_write_b32 a38, 0\n"
"v_accvgpr_write_b32 a39, 0\n"
"v_mul_f32 v248, v248, v237\n"
"v_mul_f32 v249, v249, v237\n"
"v_mul_f32 v250, v250, v237\n"
"v_mul_f32 v251, v251, v237\n"
"v_cvt_pk_bf16_f32 v224, v248, v249\n"
"v_cvt_pk_bf16_f32 v225, v250, v251\n"
"v_accvgpr_read_b32 v248, a44\n"
"v_accvgpr_read_b32 v249, a45\n"
"v_accvgpr_read_b32 v250, a46\n"
"v_accvgpr_read_b32 v251, a47\n"
"v_accvgpr_write_b32 a44, 0\n"
"v_accvgpr_write_b32 a45, 0\n"
"v_accvgpr_write_b32 a46, 0\n"
"v_accvgpr_write_b32 a47, 0\n"
"v_mul_f32 v248, v248, v237\n"
"v_mul_f32 v249, v249, v237\n"
"v_mul_f32 v250, v250, v237\n"
"v_mul_f32 v251, v251, v237\n"
"v_cvt_pk_bf16_f32 v226, v248, v249\n"
"v_cvt_pk_bf16_f32 v227, v250, v251\n"
"global_store_dwordx4 v254, v[224:227], s[38:39] offset:2048 sc1\n"
"v_accvgpr_read_b32 v248, a52\n"
"v_accvgpr_read_b32 v249, a53\n"
"v_accvgpr_read_b32 v250, a54\n"
"v_accvgpr_read_b32 v251, a55\n"
"v_accvgpr_write_b32 a52, 0\n"
"v_accvgpr_write_b32 a53, 0\n"
"v_accvgpr_write_b32 a54, 0\n"
"v_accvgpr_write_b32 a55, 0\n"
"v_mul_f32 v248, v248, v237\n"
"v_mul_f32 v249, v249, v237\n"
"v_mul_f32 v250, v250, v237\n"
"v_mul_f32 v251, v251, v237\n"
"v_cvt_pk_bf16_f32 v228, v248, v249\n"
"v_cvt_pk_bf16_f32 v229, v250, v251\n"
"v_accvgpr_read_b32 v248, a60\n"
"v_accvgpr_read_b32 v249, a61\n"
"v_accvgpr_read_b32 v250, a62\n"
"v_accvgpr_read_b32 v251, a63\n"
"v_accvgpr_write_b32 a60, 0\n"
"v_accvgpr_write_b32 a61, 0\n"
"v_accvgpr_write_b32 a62, 0\n"
"v_accvgpr_write_b32 a63, 0\n"
"v_mul_f32 v248, v248, v237\n"
"v_mul_f32 v249, v249, v237\n"
"v_mul_f32 v250, v250, v237\n"
"v_mul_f32 v251, v251, v237\n"
"v_cvt_pk_bf16_f32 v230, v248, v249\n"
"v_cvt_pk_bf16_f32 v231, v250, v251\n"
"global_store_dwordx4 v254, v[228:231], s[38:39] offset:3072 sc1\n"
"s_nop 1\n"
"s_setpc_b64 s[36:37]\n"
"70:\n"
"s_cmp_eq_u32 s26, 2\n"
"s_cbranch_scc0 71f\n"
"s_waitcnt vmcnt(0)\n"
"v_mov_b32 v3, 0\n"
"v_mov_b32 v4, 1\n"
"s_mov_b64 exec, 1\n"
"global_store_dword v3, v4, s[60:61] sc1\n"
"s_mov_b64 exec, -1\n"
"s_branch 72b\n"
"71:\n"
"s_cmp_eq_u32 s26, 5\n"
"s_cbranch_scc0 73f\n"
"s_add_u32 s22, s58, 0x380000\n"
"s_addc_u32 s23, s59, 0\n"
"s_branch 72b\n"
"73:\n"
"s_cmp_eq_u32 s26, 6\n"
"s_cbranch_scc0 74f\n"
"v_mov_b32 v8, v16\n"
"v_mov_b32 v9, v17\n"
"v_mov_b32 v10, v18\n"
"v_mov_b32 v11, v19\n"
"v_mov_b32 v12, v20\n"
"v_mov_b32 v13, v21\n"
"v_mov_b32 v14, v22\n"
"v_mov_b32 v15, v23\n"
"global_load_dword v31, v30, s[56:57] sc1\n"
"s_mov_b64 s[20:21], s[58:59]\n"
"s_add_u32 s22, s58, 0x100000\n"
"s_addc_u32 s23, s59, 0\n"
"s_branch 72b\n"
"74:\n"
"s_add_u32 s20, s58, 0x200000\n"
"s_addc_u32 s21, s59, 0\n"
"s_branch 72b\n"
"801:\n"
"s_sub_u32 s70, s70, 1\n"
"s_cmp_eq_u32 s70, 0\n"
"s_cbranch_scc1 802b\n"
"s_sleep 1\n"
"global_load_dword v31, v30, s[56:57] sc1\n"
"s_waitcnt vmcnt(0)\n"
"s_branch 80b\n"
"811:\n"
"s_sub_u32 s70, s70, 1\n"
"s_cmp_eq_u32 s70, 0\n"
"s_cbranch_scc1 812b\n"
"s_sleep 1\n"
"global_load_dword v5, v6, s[68:69] sc1\n"
"s_waitcnt vmcnt(0)\n"
"s_branch 81b\n"
"99:\n"
  :: [ab] "s"(ab), [bb] "s"(bb), [wq] "s"(wq), [fh] "s"(fh), [hl] "s"(hl), [hs] "s"(hs), [fp] "s"(fp), [kvb] "s"(kvb), [fk] "s"(fk), [fq] "s"(fq), [kv] "s"(kv), [cl] "s"(cl), [cs] "s"(cs), [pf] "s"(pf), [fd] "s"(fd), [ntl] "s"(ntl), [rot] "s"(rot), [wid] "s"(wid), [tid] "v"(tid) : "v2", "v3", "v4", "v5", "v6", "v7", "v8", "v9", "v10", "v11", "v12", "v13", "v14", "v15", "v16", "v17", "v18", "v19", "v20", "v21", "v22", "v23", "v24", "v25", "v26", "v27", "v28", "v29", "v30", "v31", "v32", "v33", "v34", "v35", "v36", "v37", "v38", "v39", "v40", "v41", "v42", "v43", "v44", "v45", "v46", "v47", "v48", "v49", "v50", "v51", "v52", "v53", "v54", "v55", "v56", "v57", "v58", "v59", "v60", "v61", "v62", "v63", "v64", "v65", "v66", "v67", "v68", "v69", "v70", "v71", "v72", "v73", "v74", "v75", "v76", "v77", "v78", "v79", "v80", "v81", "v82", "v83", "v84", "v85", "v86", "v87", "v88", "v89", "v90", "v91", "v92", "v93", "v94", "v95", "v96", "v97", "v98", "v99", "v100", "v101", "v102", "v103", "v104", "v105", "v106", "v107", "v108", "v109", "v110", "v111", "v112", "v113", "v114", "v115", "v116", "v117", "v118", "v119", "v120", "v121", "v122", "v123", "v124", "v125", "v126", "v127", "v128", "v129", "v130", "v131", "v132", "v133", "v134", "v135", "v136", "v137", "v138", "v139", "v140", "v141", "v142", "v143", "v144", "v145", "v146", "v147", "v148", "v149", "v150", "v151", "v152", "v153", "v154", "v155", "v156", "v157", "v158", "v159", "v160", "v161", "v162", "v163", "v164", "v165", "v166", "v167", "v168", "v169", "v170", "v171", "v172", "v173", "v174", "v175", "v176", "v177", "v178", "v179", "v180", "v181", "v182", "v183", "v184", "v185", "v186", "v187", "v188", "v189", "v190", "v191", "v192", "v193", "v194", "v195", "v196", "v197", "v198", "v199", "v200", "v201", "v202", "v203", "v204", "v205", "v206", "v207", "v208", "v209", "v210", "v211", "v212", "v213", "v214", "v215", "v216", "v217", "v218", "v219", "v220", "v221", "v222", "v223", "v224", "v225", "v226", "v227", "v228", "v229", "v230", "v231", "v232", "v233", "v234", "v235", "v236", "v237", "v238", "v239", "v240", "v241", "v242", "v243", "v244", "v245", "v246", "v247", "v248", "v249", "v250", "v251", "v252", "v253", "v254", "v255", "a0", "a1", "a2", "a3", "a4", "a5", "a6", "a7", "a8", "a9", "a10", "a11", "a12", "a13", "a14", "a15", "a16", "a17", "a18", "a19", "a20", "a21", "a22", "a23", "a24", "a25", "a26", "a27", "a28", "a29", "a30", "a31", "a32", "a33", "a34", "a35", "a36", "a37", "a38", "a39", "a40", "a41", "a42", "a43", "a44", "a45", "a46", "a47", "a48", "a49", "a50", "a51", "a52", "a53", "a54", "a55", "a56", "a57", "a58", "a59", "a60", "a61", "a62", "a63", "a64", "a65", "a66", "a67", "a68", "a69", "a70", "a71", "a72", "a73", "a74", "a75", "a76", "a77", "a78", "a79", "a80", "a81", "a82", "a83", "a84", "a85", "a86", "a87", "a88", "a89", "a90", "a91", "a92", "a93", "a94", "a95", "a96", "a97", "a98", "a99", "a100", "a101", "a102", "a103", "a104", "a105", "a106", "a107", "a108", "a109", "a110", "a111", "a112", "a113", "a114", "a115", "a116", "a117", "a118", "a119", "a120", "a121", "a122", "a123", "a124", "a125", "a126", "a127", "a128", "a129", "a130", "a131", "a132", "a133", "a134", "a135", "a136", "a137", "a138", "a139", "a140", "a141", "a142", "a143", "a144", "a145", "a146", "a147", "a148", "a149", "a150", "a151", "a152", "a153", "a154", "a155", "a156", "a157", "a158", "a159", "a160", "a161", "a162", "a163", "a164", "a165", "a166", "a167", "a168", "a169", "a170", "a171", "a172", "a173", "a174", "a175", "a176", "a177", "a178", "a179", "a180", "a181", "a182", "a183", "a184", "a185", "a186", "a187", "a188", "a189", "a190", "a191", "a192", "a193", "a194", "a195", "a196", "a197", "a198", "a199", "a200", "a201", "a202", "a203", "a204", "a205", "a206", "a207", "a208", "a209", "a210", "a211", "a212", "a213", "a214", "a215", "a216", "a217", "a218", "a219", "a220", "a221", "a222", "a223", "a224", "a225", "a226", "a227", "a228", "a229", "a230", "a231", "a232", "a233", "a234", "a235", "a236", "a237", "a238", "a239", "a240", "a241", "a242", "a243", "a244", "a245", "a246", "a247", "a248", "a249", "a250", "a251", "a252", "a253", "a254", "a255", "s20", "s21", "s22", "s23", "s24", "s25", "s26", "s27", "s28", "s29", "s30", "s31", "s32", "s33", "s34", "s35", "s36", "s37", "s38", "s39", "s40", "s41", "s42", "s43", "s44", "s45", "s46", "s47", "s48", "s49", "s50", "s51", "s52", "s53", "s54", "s55", "s56", "s57", "s58", "s59", "s60", "s61", "s62", "s63", "s64", "s65", "s66", "s67", "s68", "s69", "s70", "vcc", "scc", "memory");
}
struct Args { const u16 *x, *ckv, *wq, *wuk, *wuv, *wo, *w1, *b1, *w2, *b2; const float* mod; u16 *hf, *kv, *ctx, *x1, *h3T, *u, *out; unsigned* flags; };
typedef __attribute__((address_space(4))) const Args* KArgs;
__global__ __launch_bounds__(256, 1) void k_f(Args A) {
  extern __shared__ __attribute__((aligned(16))) char lds[];
  const u16 *x = A.x, *ckv = A.ckv, *wq = A.wq, *wuk = A.wuk, *wuv = A.wuv, *wo = A.wo, *w1 = A.w1, *w2 = A.w2; const float* mod = A.mod; u16 *hf = A.hf, *kv = A.kv, *ctx = A.ctx; unsigned* flags = A.flags;
  const int b = blockIdx.x, h = (b & 7) + 8 * (b >> 6), j = (b >> 3) & 7, rot = (j + b) & 7;
  const int tid = threadIdx.x, lane = tid & 63, w = __builtin_amdgcn_readfirstlane(tid >> 6);
  const int c = lane & 31, cc = c < 24 ? c : 23, row = 8 * b + 2 * w + (lane >> 5);
  u32x4 xv[3]; f32x4 ma[6], mb[6];
  { const unsigned xo = row * 1152 + cc * 16, mo = cc * 32;
    asm volatile("global_load_dwordx4 %0, %3, %4\n global_load_dwordx4 %1, %3, %4 offset:384\n global_load_dwordx4 %2, %3, %4 offset:768"
                 : "=&v"(xv[0]), "=&v"(xv[1]), "=&v"(xv[2]) : "v"(xo), "s"(x) : "memory");
    asm volatile("global_load_dwordx4 %0, %12, %13\n global_load_dwordx4 %1, %12, %13 offset:16\n global_load_dwordx4 %2, %12, %13 offset:768\n global_load_dwordx4 %3, %12, %13 offset:784\n"
                 "global_load_dwordx4 %4, %12, %13 offset:1536\n global_load_dwordx4 %5, %12, %13 offset:1552\n"
                 "global_load_dwordx4 %6, %12, %13 offset:2304\n global_load_dwordx4 %7, %12, %13 offset:2320\n global_load_dwordx4 %8, %12, %13 offset:3072\n global_load_dwordx4 %9, %12, %13 offset:3088\n"
                 "global_load_dwordx4 %10, %12, %13 offset:3840\n global_load_dwordx4 %11, %12, %13 offset:3856"
                 : "=&v"(ma[0]), "=&v"(ma[1]), "=&v"(ma[2]), "=&v"(ma[3]), "=&v"(ma[4]), "=&v"(ma[5]), "=&v"(mb[0]), "=&v"(mb[1]), "=&v"(mb[2]), "=&v"(mb[3]), "=&v"(mb[4]), "=&v"(mb[5])
                 : "v"(mo), "s"(mod) : "memory"); }
  const char* ab = (const char*)ckv + (size_t)(256 * (7 - j) + 64 * w) * 1024 + rot * 128;
  const char* bb = (const char*)(w < 2 ? wuk : wuv) + h * 256 + (w & 1) * 128 + (size_t)rot * 524288;
  pre_asm(ab, bb, w, tid);
  asm volatile("s_waitcnt vmcnt(28)" : "+v"(xv[0]), "+v"(xv[1]), "+v"(xv[2]) :: "memory");
  f32x4 v[6];
#pragma unroll
  for (int i = 0; i < 6; ++i) v[i] = unpk(u32x2{xv[i >> 1][(i & 1) * 2], xv[i >> 1][(i & 1) * 2 + 1]});
  const float rs = ln_center(v, c < 24);
  asm volatile("s_waitcnt vmcnt(16)" : "+v"(ma[0]), "+v"(ma[1]), "+v"(ma[2]), "+v"(ma[3]), "+v"(ma[4]), "+v"(ma[5]), "+v"(mb[0]), "+v"(mb[1]), "+v"(mb[2]), "+v"(mb[3]), "+v"(mb[4]), "+v"(mb[5]) :: "memory");
  u32x4 o[3];
#pragma unroll
  for (int i = 0; i < 3; ++i) { const u32x2 lo = pk4((v[2 * i] * rs) * ma[2 * i] + mb[2 * i]), hi = pk4((v[2 * i + 1] * rs) * ma[2 * i + 1] + mb[2 * i + 1]); o[i] = u32x4{lo[0], lo[1], hi[0], hi[1]}; }
  { const unsigned ho = (row >> 4) * 18432 + (cc >> 2) * 1024 + (cc & 3) * 256 + (row & 15) * 16;
    asm volatile("global_store_dwordx4 %0, %3, %6 sc1\n global_store_dwordx4 %1, %4, %6 sc1\n global_store_dwordx4 %2, %5, %6 sc1\n s_nop 1"
                 :: "v"(ho), "v"(ho + 6144), "v"(ho + 12288), "v"(o[0]), "v"(o[1]), "v"(o[2]), "s"(hf) : "memory"); }
  const int rl = 8 * (15 - j) + 2 * w, rsb = 8 * j + 2 * w;
  const int g = 4 * b + w;
  const char* pf = g < 83 ? (const char*)wo + min(g * 57344, 4718592 - 57344) : g < 413 ? (const char*)w1 + min((g - 83) * 57344, 18874368 - 57344) : g < 743 ? (const char*)w2 + min((g - 413) * 57344, 18874368 - 57344) : (const char*)wq;
  unsigned ts;
  f_asm(ab, bb, (const char*)wq + h * 256 + (w & 1) * 128 + (w >> 1) * 524288, flags + 4 * b + w, (const char*)hf + rl * 18432, (const char*)hf + rsb * 18432, flags + rsb * 8,
        (char*)kv + ((size_t)h * 32 + 4 * (7 - j) + 2 * (w >> 1)) * 32768 + (w & 1) * 2048, flags + 1024 + h * 32 + j * 4 + w, flags + 1024 + h * 32,
        (const char*)kv + (size_t)h * (32 * 32768) + w * 8192 + 4096, (char*)ctx + ((size_t)rl * 128 + 4 * h) * 1024, (char*)ctx + ((size_t)rsb * 128 + 4 * h) * 1024, pf,
        (rl - rsb) * 32, 32 - 2 * j, rot, w, tid, ts);
  { KArgs a = (KArgs)__builtin_amdgcn_kernarg_segment_ptr(); asm volatile("" : "+s"(a));
    unsigned bb = blockIdx.x; asm volatile("" : "+s"(bb));
    publish(a->flags + FCT + (((bb & 7) + 8 * (bb >> 6)) * 8 + ((bb >> 3) & 7)) * FS, 1u);
    back_half(lds, a->x, a->ctx, a->wo, a->w1, a->b1, a->w2, a->b2, a->mod, a->x1, a->h3T, a->u, a->out, a->flags, 1u); }
}
extern "C" void kernel_launch(void* const* d_in, const int* in_sizes, int n_in, void* d_out, int out_size, void* d_ws, size_t ws_size, hipStream_t stream) {
  const u16* x = (const u16*)d_in[0]; const u16* ckv = (const u16*)d_in[1]; const u16* wq = (const u16*)d_in[2]; const u16* wuk = (const u16*)d_in[3];
  const u16* wuv = (const u16*)d_in[4]; const u16* wo = (const u16*)d_in[5]; const u16* w1 = (const u16*)d_in[7]; const u16* b1 = (const u16*)d_in[8];
  const u16* w2 = (const u16*)d_in[9]; const u16* b2 = (const u16*)d_in[10]; const u16* c = (const u16*)d_in[11]; const u16* wmod = (const u16*)d_in[12];
  char* ws = (char*)d_ws;
  float* mod = (float*)ws; unsigned* flags = (unsigned*)(ws + 65536); u16* h = (u16*)(ws + (1 << 20)); u16* x1 = (u16*)(ws + (4 << 20)); u16* h3T = (u16*)(ws + (7 << 20));
  u16* u = (u16*)(ws + (10 << 20)); u16* ctx = (u16*)(ws + (16 << 20)); u16* kv = (u16*)(ws + (32 << 20));
  (void)hipFuncSetAttribute((const void*)k_f, hipFuncAttributeMaxDynamicSharedMemorySize, 163840);
  k_mod<<<54, 512, 0, stream>>>(c, wmod, mod, flags);
  const Args A{x, ckv, wq, wuk, wuv, wo, w1, b1, w2, b2, mod, h, kv, ctx, x1, h3T, u, (u16*)d_out, flags};
  k_f<<<256, 256, 163840, stream>>>(A);
}
```
